```python
import torch

def _softmax(x, axis=-1):
    return torch.softmax(x, dim=axis)
def _gelu(x):
    return torch.nn.functional.gelu(x, approximate="tanh")
def _silu(x):
    return torch.nn.functional.silu(x)
def _sigmoid(x):
    return torch.sigmoid(x)
def _layernorm(x, axis=-1, eps=1e-5):
    m = x.mean(dim=axis, keepdim=True)
    v = x.var(dim=axis, keepdim=True, unbiased=False)
    return (x - m) / torch.sqrt(v + eps)
def _scatter_add(tgt, idx, src, axis=0):
    return tgt.clone().index_add_(axis, idx, src)
def _segment_sum(src, seg, n):
    out = torch.zeros((n,) + src.shape[1:], dtype=src.dtype)
    return out.index_add_(0, seg, src)
def _pad(x, pw):
    flat = tuple(v for pair in reversed(pw) for v in pair)
    return torch.nn.functional.pad(x, flat)

kernel_name = 'gated_mix__grouped_kv_mqa__glu_gated_swiglu__prompt'

def setup_inputs():

    _w = (lambda a: (lambda t: t.to(torch.bfloat16) if t.is_floating_point() else t)(torch.as_tensor(a)))
    def _A_setup():
        B, S, D, H = 1, 2048, 640, 32
        Dh = D // H
        return {
            "x": _w((lambda *s: torch.randn(*s, dtype=torch.bfloat16) * 0.05)(B, S, D)),
            "wq": _w((lambda *s: torch.randn(*s, dtype=torch.bfloat16) * 0.8)(D, H * Dh)),
            "wk": _w((lambda *s: torch.randn(*s, dtype=torch.bfloat16) * 0.8)(D, Dh)),
            "wv": _w((lambda *s: torch.randn(*s, dtype=torch.bfloat16) * 0.05)(D, Dh)),
            "wo": _w((lambda *s: torch.randn(*s, dtype=torch.bfloat16) * 0.05)(H * Dh, D)),
        }
    _A_in = _A_setup()

    def _B_setup():
        B, S, D, F = 1, 2048, 640, 1536
        return {
            "x": _w((lambda *s: torch.randn(*s, dtype=torch.bfloat16) * 0.05)(B, S, D)),
            "w1": _w((lambda *s: torch.randn(*s, dtype=torch.bfloat16) * 0.8)(D, F) * (2.0 / D) ** 0.5),
            "w3": _w((lambda *s: torch.randn(*s, dtype=torch.bfloat16) * 0.8)(D, F) * (2.0 / D) ** 0.5),
            "w2": _w((lambda *s: torch.randn(*s, dtype=torch.bfloat16) * 0.8)(F, D) * (2.0 / F) ** 0.5),
        }
    _B_in = _B_setup()

    return {
        "A_x": _A_in["x"],
        "A_wq": _A_in["wq"],
        "A_wk": _A_in["wk"],
        "A_wv": _A_in["wv"],
        "A_wo": _A_in["wo"],
        "B_x": _B_in["x"],
        "B_w1": _B_in["w1"],
        "B_w3": _B_in["w3"],
        "B_w2": _B_in["w2"],
        "gate_w": _w((lambda *s: torch.randn(*s, dtype=torch.bfloat16) * 0.05)(640) * 16.0),
    }

def reference(A_x, A_wq, A_wk, A_wv, A_wo, B_x, B_w1, B_w3, B_w2, gate_w):
    def _A_ref(x, wq, wk, wv, wo):
        B, S, D = x.shape
        H = 32
        Dh = D // H
        q = (lambda x, *d: x.permute(*d))((x @ wq).reshape(B, S, H, Dh), 0, 2, 1, 3)
        k = x @ wk
        v = x @ wv
        scores = torch.einsum("bhqd,bkd->bhqk", q, k) * (Dh ** -0.5)
        p = _softmax(scores, axis=-1)
        o = torch.einsum("bhqk,bkd->bhqd", p, v)
        return (lambda x, *d: x.permute(*d))(o, 0, 2, 1, 3).reshape(B, S, H * Dh) @ wo

    def _B_ref(x, w1, w3, w2):
        gate = torch.einsum("bsd,df->bsf", x, w1)
        up = torch.einsum("bsd,df->bsf", x, w3)
        h = _silu(gate) * up
        out = torch.einsum("bsf,fd->bsd", h, w2)
        return out

    x = A_x
    g = _sigmoid((x * gate_w).sum(-1, keepdims=True))
    a = _A_ref(x, A_wq, A_wk, A_wv, A_wo)
    b = _B_ref(x, B_w1, B_w3, B_w2)
    return g * a + (1 - g) * b
```

```cpp
#include <hip/hip_runtime.h>
#include <hip/hip_bf16.h>
#define ZERO96 \
  "v_mov_b64 v[0:1], 0\n" \
  "v_mov_b64 v[2:3], 0\n" \
  "v_mov_b64 v[4:5], 0\n" \
  "v_mov_b64 v[6:7], 0\n" \
  "v_mov_b64 v[8:9], 0\n" \
  "v_mov_b64 v[10:11], 0\n" \
  "v_mov_b64 v[12:13], 0\n" \
  "v_mov_b64 v[14:15], 0\n" \
  "v_mov_b64 v[16:17], 0\n" \
  "v_mov_b64 v[18:19], 0\n" \
  "v_mov_b64 v[20:21], 0\n" \
  "v_mov_b64 v[22:23], 0\n" \
  "v_mov_b64 v[24:25], 0\n" \
  "v_mov_b64 v[26:27], 0\n" \
  "v_mov_b64 v[28:29], 0\n" \
  "v_mov_b64 v[30:31], 0\n" \
  "v_mov_b64 v[32:33], 0\n" \
  "v_mov_b64 v[34:35], 0\n" \
  "v_mov_b64 v[36:37], 0\n" \
  "v_mov_b64 v[38:39], 0\n" \
  "v_mov_b64 v[40:41], 0\n" \
  "v_mov_b64 v[42:43], 0\n" \
  "v_mov_b64 v[44:45], 0\n" \
  "v_mov_b64 v[46:47], 0\n" \
  "v_mov_b64 v[48:49], 0\n" \
  "v_mov_b64 v[50:51], 0\n" \
  "v_mov_b64 v[52:53], 0\n" \
  "v_mov_b64 v[54:55], 0\n" \
  "v_mov_b64 v[56:57], 0\n" \
  "v_mov_b64 v[58:59], 0\n" \
  "v_mov_b64 v[60:61], 0\n" \
  "v_mov_b64 v[62:63], 0\n" \
  "v_mov_b64 v[64:65], 0\n" \
  "v_mov_b64 v[66:67], 0\n" \
  "v_mov_b64 v[68:69], 0\n" \
  "v_mov_b64 v[70:71], 0\n" \
  "v_mov_b64 v[72:73], 0\n" \
  "v_mov_b64 v[74:75], 0\n" \
  "v_mov_b64 v[76:77], 0\n" \
  "v_mov_b64 v[78:79], 0\n" \
  "v_mov_b64 v[80:81], 0\n" \
  "v_mov_b64 v[82:83], 0\n" \
  "v_mov_b64 v[84:85], 0\n" \
  "v_mov_b64 v[86:87], 0\n" \
  "v_mov_b64 v[88:89], 0\n" \
  "v_mov_b64 v[90:91], 0\n" \
  "v_mov_b64 v[92:93], 0\n" \
  "v_mov_b64 v[94:95], 0\n"
#define GEMM1_ASM \
  "global_load_dword v235, v[238:239], off\n" \
  "s_mov_b32 %[sl], 0\n" \
  "s_waitcnt lgkmcnt(0)\n" \
  "s_barrier\n" \
  "ds_read_b64_tr_b16 v[128:129], v224 offset:16384\n" \
  "ds_read_b64_tr_b16 v[130:131], v224 offset:17408\n" \
  "ds_read_b128 v[160:163], v228 offset:0\n" \
  "ds_read_b128 v[164:167], v228 offset:2048\n" \
  "ds_read_b128 v[168:171], v228 offset:4096\n" \
  "ds_read_b128 v[172:175], v228 offset:6144\n" \
  "ds_read_b64_tr_b16 v[132:133], v224 offset:32768\n" \
  "ds_read_b64_tr_b16 v[134:135], v224 offset:33792\n" \
  "ds_read_b64_tr_b16 v[136:137], v225 offset:16384\n" \
  "ds_read_b64_tr_b16 v[138:139], v225 offset:17408\n" \
  "ds_read_b64_tr_b16 v[140:141], v225 offset:32768\n" \
  "ds_read_b64_tr_b16 v[142:143], v225 offset:33792\n" \
  "s_waitcnt lgkmcnt(4)\n" \
  "ds_read_b64_tr_b16 v[144:145], v226 offset:16384\n" \
  "ds_read_b64_tr_b16 v[146:147], v226 offset:17408\n" \
  "ds_read_b64_tr_b16 v[148:149], v226 offset:32768\n" \
  "ds_read_b64_tr_b16 v[150:151], v226 offset:33792\n" \
  "ds_read_b64_tr_b16 v[152:153], v227 offset:16384\n" \
  "ds_read_b64_tr_b16 v[154:155], v227 offset:17408\n" \
  "ds_read_b64_tr_b16 v[156:157], v227 offset:32768\n" \
  "ds_read_b64_tr_b16 v[158:159], v227 offset:33792\n" \
  "s_mov_b32 %[cnt], 9\n" \
  "s_waitcnt lgkmcnt(10)\n" \
  "v_mfma_f32_16x16x32_bf16 v[0:3], v[128:131], v[160:163], v[0:3]\n" \
  "ds_read_b64_tr_b16 v[176:177], v224 offset:24576\n" \
  "v_mfma_f32_16x16x32_bf16 v[16:19], v[132:135], v[160:163], v[16:19]\n" \
  "ds_read_b64_tr_b16 v[178:179], v224 offset:25600\n" \
  "v_mfma_f32_16x16x32_bf16 v[20:23], v[132:135], v[164:167], v[20:23]\n" \
  "ds_read_b128 v[208:211], v229 offset:0\n" \
  "v_mfma_f32_16x16x32_bf16 v[4:7], v[128:131], v[164:167], v[4:7]\n" \
  "ds_read_b128 v[212:215], v229 offset:2048\n" \
  "v_mfma_f32_16x16x32_bf16 v[8:11], v[128:131], v[168:171], v[8:11]\n" \
  "ds_read_b128 v[216:219], v229 offset:4096\n" \
  "v_mfma_f32_16x16x32_bf16 v[24:27], v[132:135], v[168:171], v[24:27]\n" \
  "ds_read_b128 v[220:223], v229 offset:6144\n" \
  "v_mfma_f32_16x16x32_bf16 v[28:31], v[132:135], v[172:175], v[28:31]\n" \
  "ds_read_b64_tr_b16 v[180:181], v224 offset:40960\n" \
  "v_mfma_f32_16x16x32_bf16 v[12:15], v[128:131], v[172:175], v[12:15]\n" \
  "ds_read_b64_tr_b16 v[182:183], v224 offset:41984\n" \
  "s_waitcnt lgkmcnt(8)\n" \
  "v_mfma_f32_16x16x32_bf16 v[32:35], v[136:139], v[160:163], v[32:35]\n" \
  "ds_read_b64_tr_b16 v[184:185], v225 offset:24576\n" \
  "v_mfma_f32_16x16x32_bf16 v[48:51], v[140:143], v[160:163], v[48:51]\n" \
  "ds_read_b64_tr_b16 v[186:187], v225 offset:25600\n" \
  "v_mfma_f32_16x16x32_bf16 v[52:55], v[140:143], v[164:167], v[52:55]\n" \
  "ds_read_b64_tr_b16 v[188:189], v225 offset:40960\n" \
  "v_mfma_f32_16x16x32_bf16 v[36:39], v[136:139], v[164:167], v[36:39]\n" \
  "ds_read_b64_tr_b16 v[190:191], v225 offset:41984\n" \
  "s_waitcnt lgkmcnt(12)\n" \
  "v_mfma_f32_16x16x32_bf16 v[40:43], v[136:139], v[168:171], v[40:43]\n" \
  "ds_read_b64_tr_b16 v[192:193], v226 offset:24576\n" \
  "v_mfma_f32_16x16x32_bf16 v[56:59], v[140:143], v[168:171], v[56:59]\n" \
  "ds_read_b64_tr_b16 v[194:195], v226 offset:25600\n" \
  "v_mfma_f32_16x16x32_bf16 v[60:63], v[140:143], v[172:175], v[60:63]\n" \
  "ds_read_b64_tr_b16 v[196:197], v226 offset:40960\n" \
  "v_mfma_f32_16x16x32_bf16 v[44:47], v[136:139], v[172:175], v[44:47]\n" \
  "ds_read_b64_tr_b16 v[198:199], v226 offset:41984\n" \
  "v_mfma_f32_16x16x32_bf16 v[64:67], v[144:147], v[160:163], v[64:67]\n" \
  "ds_read_b64_tr_b16 v[200:201], v227 offset:24576\n" \
  "v_mfma_f32_16x16x32_bf16 v[80:83], v[148:151], v[160:163], v[80:83]\n" \
  "ds_read_b64_tr_b16 v[202:203], v227 offset:25600\n" \
  "v_mfma_f32_16x16x32_bf16 v[84:87], v[148:151], v[164:167], v[84:87]\n" \
  "ds_read_b64_tr_b16 v[204:205], v227 offset:40960\n" \
  "v_mfma_f32_16x16x32_bf16 v[68:71], v[144:147], v[164:167], v[68:71]\n" \
  "ds_read_b64_tr_b16 v[206:207], v227 offset:41984\n" \
  "v_mfma_f32_16x16x32_bf16 v[72:75], v[144:147], v[168:171], v[72:75]\n" \
  "s_add_u32 %[sl], %[sl], 1\n" \
  "v_mfma_f32_16x16x32_bf16 v[88:91], v[148:151], v[168:171], v[88:91]\n" \
  "s_cmp_eq_u32 %[sl], 3\n" \
  "v_mfma_f32_16x16x32_bf16 v[92:95], v[148:151], v[172:175], v[92:95]\n" \
  "s_mov_b32 %[d], 49152\n" \
  "v_mfma_f32_16x16x32_bf16 v[76:79], v[144:147], v[172:175], v[76:79]\n" \
  "s_cselect_b32 %[d], 4294868992, %[d]\n" \
  "v_mfma_f32_16x16x32_bf16 v[96:99], v[152:155], v[160:163], v[96:99]\n" \
  "s_cselect_b32 %[sl], 0, %[sl]\n" \
  "v_mfma_f32_16x16x32_bf16 v[112:115], v[156:159], v[160:163], v[112:115]\n" \
  "v_mfma_f32_16x16x32_bf16 v[116:119], v[156:159], v[164:167], v[116:119]\n" \
  "s_bitcmp1_b32 %[p0], %[cnt]\n" \
  "v_mfma_f32_16x16x32_bf16 v[100:103], v[152:155], v[164:167], v[100:103]\n" \
  "s_cbranch_scc1 6f\n" \
  "7:\n" \
  "v_mfma_f32_16x16x32_bf16 v[104:107], v[152:155], v[168:171], v[104:107]\n" \
  "v_mfma_f32_16x16x32_bf16 v[120:123], v[156:159], v[168:171], v[120:123]\n" \
  "v_mfma_f32_16x16x32_bf16 v[124:127], v[156:159], v[172:175], v[124:127]\n" \
  "v_mfma_f32_16x16x32_bf16 v[108:111], v[152:155], v[172:175], v[108:111]\n" \
  "s_waitcnt lgkmcnt(0)\n" \
  "1:\n" \
  "s_barrier\n" \
  "v_mfma_f32_16x16x32_bf16 v[0:3], v[176:179], v[208:211], v[0:3]\n" \
  "v_add_u32 v224, %[d], v224\n" \
  "ds_read_b64_tr_b16 v[128:129], v224 offset:16384\n" \
  "v_mfma_f32_16x16x32_bf16 v[16:19], v[180:183], v[208:211], v[16:19]\n" \
  "v_add_u32 v228, %[d], v228\n" \
  "ds_read_b64_tr_b16 v[130:131], v224 offset:17408\n" \
  "v_mfma_f32_16x16x32_bf16 v[20:23], v[180:183], v[212:215], v[20:23]\n" \
  "ds_read_b128 v[160:163], v228 offset:0\n" \
  "v_mfma_f32_16x16x32_bf16 v[4:7], v[176:179], v[212:215], v[4:7]\n" \
  "ds_read_b128 v[164:167], v228 offset:2048\n" \
  "v_mfma_f32_16x16x32_bf16 v[8:11], v[176:179], v[216:219], v[8:11]\n" \
  "ds_read_b128 v[168:171], v228 offset:4096\n" \
  "v_mfma_f32_16x16x32_bf16 v[24:27], v[180:183], v[216:219], v[24:27]\n" \
  "v_add_u32 v229, %[d], v229\n" \
  "ds_read_b128 v[172:175], v228 offset:6144\n" \
  "v_mfma_f32_16x16x32_bf16 v[28:31], v[180:183], v[220:223], v[28:31]\n" \
  "ds_read_b64_tr_b16 v[132:133], v224 offset:32768\n" \
  "v_mfma_f32_16x16x32_bf16 v[12:15], v[176:179], v[220:223], v[12:15]\n" \
  "v_add_u32 v225, %[d], v225\n" \
  "ds_read_b64_tr_b16 v[134:135], v224 offset:33792\n" \
  "v_mfma_f32_16x16x32_bf16 v[32:35], v[184:187], v[208:211], v[32:35]\n" \
  "ds_read_b64_tr_b16 v[136:137], v225 offset:16384\n" \
  "v_mfma_f32_16x16x32_bf16 v[48:51], v[188:191], v[208:211], v[48:51]\n" \
  "ds_read_b64_tr_b16 v[138:139], v225 offset:17408\n" \
  "v_mfma_f32_16x16x32_bf16 v[52:55], v[188:191], v[212:215], v[52:55]\n" \
  "ds_read_b64_tr_b16 v[140:141], v225 offset:32768\n" \
  "v_mfma_f32_16x16x32_bf16 v[36:39], v[184:187], v[212:215], v[36:39]\n" \
  "v_add_u32 v226, %[d], v226\n" \
  "ds_read_b64_tr_b16 v[142:143], v225 offset:33792\n" \
  "v_mfma_f32_16x16x32_bf16 v[40:43], v[184:187], v[216:219], v[40:43]\n" \
  "ds_read_b64_tr_b16 v[144:145], v226 offset:16384\n" \
  "v_mfma_f32_16x16x32_bf16 v[56:59], v[188:191], v[216:219], v[56:59]\n" \
  "ds_read_b64_tr_b16 v[146:147], v226 offset:17408\n" \
  "v_mfma_f32_16x16x32_bf16 v[60:63], v[188:191], v[220:223], v[60:63]\n" \
  "ds_read_b64_tr_b16 v[148:149], v226 offset:32768\n" \
  "v_mfma_f32_16x16x32_bf16 v[44:47], v[184:187], v[220:223], v[44:47]\n" \
  "v_add_u32 v227, %[d], v227\n" \
  "ds_read_b64_tr_b16 v[150:151], v226 offset:33792\n" \
  "v_mfma_f32_16x16x32_bf16 v[64:67], v[192:195], v[208:211], v[64:67]\n" \
  "ds_read_b64_tr_b16 v[152:153], v227 offset:16384\n" \
  "v_mfma_f32_16x16x32_bf16 v[80:83], v[196:199], v[208:211], v[80:83]\n" \
  "ds_read_b64_tr_b16 v[154:155], v227 offset:17408\n" \
  "v_mfma_f32_16x16x32_bf16 v[84:87], v[196:199], v[212:215], v[84:87]\n" \
  "ds_read_b64_tr_b16 v[156:157], v227 offset:32768\n" \
  "v_mfma_f32_16x16x32_bf16 v[68:71], v[192:195], v[212:215], v[68:71]\n" \
  "ds_read_b64_tr_b16 v[158:159], v227 offset:33792\n" \
  "v_mfma_f32_16x16x32_bf16 v[72:75], v[192:195], v[216:219], v[72:75]\n" \
  "v_mfma_f32_16x16x32_bf16 v[88:91], v[196:199], v[216:219], v[88:91]\n" \
  "v_mfma_f32_16x16x32_bf16 v[92:95], v[196:199], v[220:223], v[92:95]\n" \
  "s_sub_u32 %[cnt], %[cnt], 1\n" \
  "v_mfma_f32_16x16x32_bf16 v[76:79], v[192:195], v[220:223], v[76:79]\n" \
  "v_mfma_f32_16x16x32_bf16 v[96:99], v[200:203], v[208:211], v[96:99]\n" \
  "v_mfma_f32_16x16x32_bf16 v[112:115], v[204:207], v[208:211], v[112:115]\n" \
  "v_mfma_f32_16x16x32_bf16 v[116:119], v[204:207], v[212:215], v[116:119]\n" \
  "v_mfma_f32_16x16x32_bf16 v[100:103], v[200:203], v[212:215], v[100:103]\n" \
  "v_mfma_f32_16x16x32_bf16 v[104:107], v[200:203], v[216:219], v[104:107]\n" \
  "v_mfma_f32_16x16x32_bf16 v[120:123], v[204:207], v[216:219], v[120:123]\n" \
  "v_mfma_f32_16x16x32_bf16 v[124:127], v[204:207], v[220:223], v[124:127]\n" \
  "v_mfma_f32_16x16x32_bf16 v[108:111], v[200:203], v[220:223], v[108:111]\n" \
  "s_waitcnt lgkmcnt(10)\n" \
  "v_mfma_f32_16x16x32_bf16 v[0:3], v[128:131], v[160:163], v[0:3]\n" \
  "ds_read_b64_tr_b16 v[176:177], v224 offset:24576\n" \
  "v_mfma_f32_16x16x32_bf16 v[16:19], v[132:135], v[160:163], v[16:19]\n" \
  "ds_read_b64_tr_b16 v[178:179], v224 offset:25600\n" \
  "v_mfma_f32_16x16x32_bf16 v[20:23], v[132:135], v[164:167], v[20:23]\n" \
  "ds_read_b128 v[208:211], v229 offset:0\n" \
  "v_mfma_f32_16x16x32_bf16 v[4:7], v[128:131], v[164:167], v[4:7]\n" \
  "ds_read_b128 v[212:215], v229 offset:2048\n" \
  "v_mfma_f32_16x16x32_bf16 v[8:11], v[128:131], v[168:171], v[8:11]\n" \
  "ds_read_b128 v[216:219], v229 offset:4096\n" \
  "v_mfma_f32_16x16x32_bf16 v[24:27], v[132:135], v[168:171], v[24:27]\n" \
  "ds_read_b128 v[220:223], v229 offset:6144\n" \
  "v_mfma_f32_16x16x32_bf16 v[28:31], v[132:135], v[172:175], v[28:31]\n" \
  "ds_read_b64_tr_b16 v[180:181], v224 offset:40960\n" \
  "v_mfma_f32_16x16x32_bf16 v[12:15], v[128:131], v[172:175], v[12:15]\n" \
  "ds_read_b64_tr_b16 v[182:183], v224 offset:41984\n" \
  "s_waitcnt lgkmcnt(8)\n" \
  "v_mfma_f32_16x16x32_bf16 v[32:35], v[136:139], v[160:163], v[32:35]\n" \
  "ds_read_b64_tr_b16 v[184:185], v225 offset:24576\n" \
  "v_mfma_f32_16x16x32_bf16 v[48:51], v[140:143], v[160:163], v[48:51]\n" \
  "ds_read_b64_tr_b16 v[186:187], v225 offset:25600\n" \
  "v_mfma_f32_16x16x32_bf16 v[52:55], v[140:143], v[164:167], v[52:55]\n" \
  "ds_read_b64_tr_b16 v[188:189], v225 offset:40960\n" \
  "v_mfma_f32_16x16x32_bf16 v[36:39], v[136:139], v[164:167], v[36:39]\n" \
  "ds_read_b64_tr_b16 v[190:191], v225 offset:41984\n" \
  "s_waitcnt lgkmcnt(12)\n" \
  "v_mfma_f32_16x16x32_bf16 v[40:43], v[136:139], v[168:171], v[40:43]\n" \
  "ds_read_b64_tr_b16 v[192:193], v226 offset:24576\n" \
  "v_mfma_f32_16x16x32_bf16 v[56:59], v[140:143], v[168:171], v[56:59]\n" \
  "ds_read_b64_tr_b16 v[194:195], v226 offset:25600\n" \
  "v_mfma_f32_16x16x32_bf16 v[60:63], v[140:143], v[172:175], v[60:63]\n" \
  "ds_read_b64_tr_b16 v[196:197], v226 offset:40960\n" \
  "v_mfma_f32_16x16x32_bf16 v[44:47], v[136:139], v[172:175], v[44:47]\n" \
  "ds_read_b64_tr_b16 v[198:199], v226 offset:41984\n" \
  "v_mfma_f32_16x16x32_bf16 v[64:67], v[144:147], v[160:163], v[64:67]\n" \
  "ds_read_b64_tr_b16 v[200:201], v227 offset:24576\n" \
  "v_mfma_f32_16x16x32_bf16 v[80:83], v[148:151], v[160:163], v[80:83]\n" \
  "ds_read_b64_tr_b16 v[202:203], v227 offset:25600\n" \
  "v_mfma_f32_16x16x32_bf16 v[84:87], v[148:151], v[164:167], v[84:87]\n" \
  "ds_read_b64_tr_b16 v[204:205], v227 offset:40960\n" \
  "v_mfma_f32_16x16x32_bf16 v[68:71], v[144:147], v[164:167], v[68:71]\n" \
  "ds_read_b64_tr_b16 v[206:207], v227 offset:41984\n" \
  "v_mfma_f32_16x16x32_bf16 v[72:75], v[144:147], v[168:171], v[72:75]\n" \
  "s_add_u32 %[sl], %[sl], 1\n" \
  "v_mfma_f32_16x16x32_bf16 v[88:91], v[148:151], v[168:171], v[88:91]\n" \
  "s_cmp_eq_u32 %[sl], 3\n" \
  "v_mfma_f32_16x16x32_bf16 v[92:95], v[148:151], v[172:175], v[92:95]\n" \
  "s_mov_b32 %[d], 49152\n" \
  "v_mfma_f32_16x16x32_bf16 v[76:79], v[144:147], v[172:175], v[76:79]\n" \
  "s_cselect_b32 %[d], 4294868992, %[d]\n" \
  "v_mfma_f32_16x16x32_bf16 v[96:99], v[152:155], v[160:163], v[96:99]\n" \
  "s_cselect_b32 %[sl], 0, %[sl]\n" \
  "v_mfma_f32_16x16x32_bf16 v[112:115], v[156:159], v[160:163], v[112:115]\n" \
  "v_mfma_f32_16x16x32_bf16 v[116:119], v[156:159], v[164:167], v[116:119]\n" \
  "s_bitcmp1_b32 %[p0], %[cnt]\n" \
  "v_mfma_f32_16x16x32_bf16 v[100:103], v[152:155], v[164:167], v[100:103]\n" \
  "s_cbranch_scc1 8f\n" \
  "9:\n" \
  "v_mfma_f32_16x16x32_bf16 v[104:107], v[152:155], v[168:171], v[104:107]\n" \
  "v_mfma_f32_16x16x32_bf16 v[120:123], v[156:159], v[168:171], v[120:123]\n" \
  "v_mfma_f32_16x16x32_bf16 v[124:127], v[156:159], v[172:175], v[124:127]\n" \
  "s_cmp_lg_u32 %[cnt], 0\n" \
  "v_mfma_f32_16x16x32_bf16 v[108:111], v[152:155], v[172:175], v[108:111]\n" \
  "s_waitcnt lgkmcnt(0)\n" \
  "s_cbranch_scc1 1b\n" \
  "s_barrier\n" \
  "v_mfma_f32_16x16x32_bf16 v[0:3], v[176:179], v[208:211], v[0:3]\n" \
  "v_mfma_f32_16x16x32_bf16 v[16:19], v[180:183], v[208:211], v[16:19]\n" \
  "v_mfma_f32_16x16x32_bf16 v[20:23], v[180:183], v[212:215], v[20:23]\n" \
  "v_mfma_f32_16x16x32_bf16 v[4:7], v[176:179], v[212:215], v[4:7]\n" \
  "v_mfma_f32_16x16x32_bf16 v[8:11], v[176:179], v[216:219], v[8:11]\n" \
  "v_mfma_f32_16x16x32_bf16 v[24:27], v[180:183], v[216:219], v[24:27]\n" \
  "v_mfma_f32_16x16x32_bf16 v[28:31], v[180:183], v[220:223], v[28:31]\n" \
  "v_mfma_f32_16x16x32_bf16 v[12:15], v[176:179], v[220:223], v[12:15]\n" \
  "v_mfma_f32_16x16x32_bf16 v[32:35], v[184:187], v[208:211], v[32:35]\n" \
  "v_mfma_f32_16x16x32_bf16 v[48:51], v[188:191], v[208:211], v[48:51]\n" \
  "v_mfma_f32_16x16x32_bf16 v[52:55], v[188:191], v[212:215], v[52:55]\n" \
  "v_mfma_f32_16x16x32_bf16 v[36:39], v[184:187], v[212:215], v[36:39]\n" \
  "v_mfma_f32_16x16x32_bf16 v[40:43], v[184:187], v[216:219], v[40:43]\n" \
  "v_mfma_f32_16x16x32_bf16 v[56:59], v[188:191], v[216:219], v[56:59]\n" \
  "v_mfma_f32_16x16x32_bf16 v[60:63], v[188:191], v[220:223], v[60:63]\n" \
  "v_mfma_f32_16x16x32_bf16 v[44:47], v[184:187], v[220:223], v[44:47]\n" \
  "v_mfma_f32_16x16x32_bf16 v[64:67], v[192:195], v[208:211], v[64:67]\n" \
  "v_mfma_f32_16x16x32_bf16 v[80:83], v[196:199], v[208:211], v[80:83]\n" \
  "v_mfma_f32_16x16x32_bf16 v[84:87], v[196:199], v[212:215], v[84:87]\n" \
  "v_mfma_f32_16x16x32_bf16 v[68:71], v[192:195], v[212:215], v[68:71]\n" \
  "v_mfma_f32_16x16x32_bf16 v[72:75], v[192:195], v[216:219], v[72:75]\n" \
  "v_mfma_f32_16x16x32_bf16 v[88:91], v[196:199], v[216:219], v[88:91]\n" \
  "v_mfma_f32_16x16x32_bf16 v[92:95], v[196:199], v[220:223], v[92:95]\n" \
  "v_mfma_f32_16x16x32_bf16 v[76:79], v[192:195], v[220:223], v[76:79]\n" \
  "v_mfma_f32_16x16x32_bf16 v[96:99], v[200:203], v[208:211], v[96:99]\n" \
  "v_mfma_f32_16x16x32_bf16 v[112:115], v[204:207], v[208:211], v[112:115]\n" \
  "v_mfma_f32_16x16x32_bf16 v[116:119], v[204:207], v[212:215], v[116:119]\n" \
  "v_mfma_f32_16x16x32_bf16 v[100:103], v[200:203], v[212:215], v[100:103]\n" \
  "v_mfma_f32_16x16x32_bf16 v[104:107], v[200:203], v[216:219], v[104:107]\n" \
  "v_mfma_f32_16x16x32_bf16 v[120:123], v[204:207], v[216:219], v[120:123]\n" \
  "v_mfma_f32_16x16x32_bf16 v[124:127], v[204:207], v[220:223], v[124:127]\n" \
  "v_mfma_f32_16x16x32_bf16 v[108:111], v[200:203], v[220:223], v[108:111]\n" \
  "s_branch 5f\n" \
  "6:\n" \
  "global_load_dword v235, v[232:233], off\n" \
  "v_add_co_u32 v232, vcc, v232, v234\n" \
  "s_nop 1\n" \
  "v_addc_co_u32 v233, vcc, 0, v233, vcc\n" \
  "s_branch 7b\n" \
  "8:\n" \
  "global_load_dword v235, v[232:233], off\n" \
  "v_add_co_u32 v232, vcc, v232, v234\n" \
  "s_nop 1\n" \
  "v_addc_co_u32 v233, vcc, 0, v233, vcc\n" \
  "s_branch 9b\n" \
  "5:\n" \
  "s_waitcnt vmcnt(0)\n" \
  "s_nop 7\n"
#define GEMM1_CLOB "v128", "v129", "v130", "v131", "v132", "v133", "v134", "v135", "v136", "v137", "v138", "v139", "v140", "v141", "v142", "v143", "v144", "v145", "v146", "v147", "v148", "v149", "v150", "v151", "v152", "v153", "v154", "v155", "v156", "v157", "v158", "v159", "v160", "v161", "v162", "v163", "v164", "v165", "v166", "v167", "v168", "v169", "v170", "v171", "v172", "v173", "v174", "v175", "v176", "v177", "v178", "v179", "v180", "v181", "v182", "v183", "v184", "v185", "v186", "v187", "v188", "v189", "v190", "v191", "v192", "v193", "v194", "v195", "v196", "v197", "v198", "v199", "v200", "v201", "v202", "v203", "v204", "v205", "v206", "v207", "v208", "v209", "v210", "v211", "v212", "v213", "v214", "v215", "v216", "v217", "v218", "v219", "v220", "v221", "v222", "v223", "v235"
#define GEMMD_ASM \
  "global_load_dword v235, v[238:239], off\n" \
  "s_mov_b32 %[sl], 0\n" \
  "s_waitcnt lgkmcnt(0)\n" \
  "s_barrier\n" \
  "s_cmp_eq_u32 %[role], 1\n" \
  "s_cbranch_scc1 2f\n" \
  "s_cmp_eq_u32 %[role], 2\n" \
  "s_cbranch_scc1 3f\n" \
  "s_cmp_eq_u32 %[role], 3\n" \
  "s_cbranch_scc1 5f\n" \
  "ds_read_b128 v[140:143], v224\n" \
  "ds_read_b64_tr_b16 v[128:129], v228 offset:16384\n" \
  "ds_read_b64_tr_b16 v[130:131], v228 offset:17664\n" \
  "ds_read_b128 v[144:147], v224 offset:4096\n" \
  "ds_read_b64_tr_b16 v[132:133], v228 offset:16448\n" \
  "ds_read_b64_tr_b16 v[134:135], v228 offset:17728\n" \
  "ds_read_b64_tr_b16 v[136:137], v229 offset:36864\n" \
  "ds_read_b64_tr_b16 v[138:139], v229 offset:37024\n" \
  "s_mov_b32 %[cnt], 8\n" \
  "s_waitcnt lgkmcnt(5)\n" \
  "v_mfma_f32_32x32x16_bf16 v[0:15], v[128:131], v[140:143], v[0:15]\n" \
  "ds_read_b128 v[160:163], v225\n" \
  "ds_read_b64_tr_b16 v[148:149], v228 offset:21504\n" \
  "s_waitcnt lgkmcnt(6)\n" \
  "v_mfma_f32_32x32x16_bf16 v[16:31], v[128:131], v[144:147], v[16:31]\n" \
  "ds_read_b64_tr_b16 v[150:151], v228 offset:22784\n" \
  "ds_read_b128 v[164:167], v225 offset:4096\n" \
  "s_waitcnt lgkmcnt(6)\n" \
  "v_mfma_f32_32x32x16_bf16 v[32:47], v[132:135], v[140:143], v[32:47]\n" \
  "ds_read_b64_tr_b16 v[152:153], v228 offset:21568\n" \
  "ds_read_b64_tr_b16 v[154:155], v228 offset:22848\n" \
  "v_mfma_f32_32x32x16_bf16 v[48:63], v[132:135], v[144:147], v[48:63]\n" \
  "ds_read_b64_tr_b16 v[156:157], v229 offset:37504\n" \
  "ds_read_b64_tr_b16 v[158:159], v229 offset:37664\n" \
  "s_waitcnt lgkmcnt(8)\n" \
  "v_mfma_f32_32x32x16_bf16 v[64:79], v[136:139], v[140:143], v[64:79]\n" \
  "s_add_u32 %[sl], %[sl], 1\n" \
  "s_cmp_eq_u32 %[sl], 3\n" \
  "s_mov_b32 %[d], 49152\n" \
  "v_mfma_f32_32x32x16_bf16 v[80:95], v[136:139], v[144:147], v[80:95]\n" \
  "s_cselect_b32 %[d], 4294868992, %[d]\n" \
  "s_cselect_b32 %[sl], 0, %[sl]\n" \
  "s_waitcnt lgkmcnt(5)\n" \
  "v_mfma_f32_32x32x16_bf16 v[0:15], v[148:151], v[160:163], v[0:15]\n" \
  "ds_read_b128 v[140:143], v226\n" \
  "ds_read_b64_tr_b16 v[128:129], v228 offset:26624\n" \
  "s_waitcnt lgkmcnt(6)\n" \
  "v_mfma_f32_32x32x16_bf16 v[16:31], v[148:151], v[164:167], v[16:31]\n" \
  "ds_read_b64_tr_b16 v[130:131], v228 offset:27904\n" \
  "ds_read_b128 v[144:147], v226 offset:4096\n" \
  "s_waitcnt lgkmcnt(6)\n" \
  "v_mfma_f32_32x32x16_bf16 v[32:47], v[152:155], v[160:163], v[32:47]\n" \
  "ds_read_b64_tr_b16 v[132:133], v228 offset:26688\n" \
  "ds_read_b64_tr_b16 v[134:135], v228 offset:27968\n" \
  "v_mfma_f32_32x32x16_bf16 v[48:63], v[152:155], v[164:167], v[48:63]\n" \
  "ds_read_b64_tr_b16 v[136:137], v229 offset:38144\n" \
  "ds_read_b64_tr_b16 v[138:139], v229 offset:38304\n" \
  "s_waitcnt lgkmcnt(8)\n" \
  "v_mfma_f32_32x32x16_bf16 v[64:79], v[156:159], v[160:163], v[64:79]\n" \
  "v_mfma_f32_32x32x16_bf16 v[80:95], v[156:159], v[164:167], v[80:95]\n" \
  "s_waitcnt lgkmcnt(5)\n" \
  "v_mfma_f32_32x32x16_bf16 v[0:15], v[128:131], v[140:143], v[0:15]\n" \
  "ds_read_b128 v[160:163], v227\n" \
  "ds_read_b64_tr_b16 v[148:149], v228 offset:31744\n" \
  "s_waitcnt lgkmcnt(6)\n" \
  "v_mfma_f32_32x32x16_bf16 v[16:31], v[128:131], v[144:147], v[16:31]\n" \
  "ds_read_b64_tr_b16 v[150:151], v228 offset:33024\n" \
  "ds_read_b128 v[164:167], v227 offset:4096\n" \
  "s_waitcnt lgkmcnt(6)\n" \
  "v_mfma_f32_32x32x16_bf16 v[32:47], v[132:135], v[140:143], v[32:47]\n" \
  "ds_read_b64_tr_b16 v[152:153], v228 offset:31808\n" \
  "ds_read_b64_tr_b16 v[154:155], v228 offset:33088\n" \
  "v_mfma_f32_32x32x16_bf16 v[48:63], v[132:135], v[144:147], v[48:63]\n" \
  "ds_read_b64_tr_b16 v[156:157], v229 offset:38784\n" \
  "ds_read_b64_tr_b16 v[158:159], v229 offset:38944\n" \
  "s_waitcnt lgkmcnt(8)\n" \
  "v_mfma_f32_32x32x16_bf16 v[64:79], v[136:139], v[140:143], v[64:79]\n" \
  "v_mfma_f32_32x32x16_bf16 v[80:95], v[136:139], v[144:147], v[80:95]\n" \
  "s_waitcnt lgkmcnt(0)\n" \
  "10:\n" \
  "s_barrier\n" \
  "v_mfma_f32_32x32x16_bf16 v[0:15], v[148:151], v[160:163], v[0:15]\n" \
  "v_add_u32 v224, %[d], v224\n" \
  "v_add_u32 v228, %[d], v228\n" \
  "ds_read_b128 v[140:143], v224\n" \
  "ds_read_b64_tr_b16 v[128:129], v228 offset:16384\n" \
  "v_mfma_f32_32x32x16_bf16 v[16:31], v[148:151], v[164:167], v[16:31]\n" \
  "v_add_u32 v229, %[d], v229\n" \
  "ds_read_b64_tr_b16 v[130:131], v228 offset:17664\n" \
  "ds_read_b128 v[144:147], v224 offset:4096\n" \
  "v_mfma_f32_32x32x16_bf16 v[32:47], v[152:155], v[160:163], v[32:47]\n" \
  "v_add_u32 v225, %[d], v225\n" \
  "ds_read_b64_tr_b16 v[132:133], v228 offset:16448\n" \
  "ds_read_b64_tr_b16 v[134:135], v228 offset:17728\n" \
  "v_mfma_f32_32x32x16_bf16 v[48:63], v[152:155], v[164:167], v[48:63]\n" \
  "v_add_u32 v226, %[d], v226\n" \
  "ds_read_b64_tr_b16 v[136:137], v229 offset:36864\n" \
  "ds_read_b64_tr_b16 v[138:139], v229 offset:37024\n" \
  "v_mfma_f32_32x32x16_bf16 v[64:79], v[156:159], v[160:163], v[64:79]\n" \
  "v_add_u32 v227, %[d], v227\n" \
  "v_mfma_f32_32x32x16_bf16 v[80:95], v[156:159], v[164:167], v[80:95]\n" \
  "s_waitcnt lgkmcnt(5)\n" \
  "v_mfma_f32_32x32x16_bf16 v[0:15], v[128:131], v[140:143], v[0:15]\n" \
  "ds_read_b128 v[160:163], v225\n" \
  "ds_read_b64_tr_b16 v[148:149], v228 offset:21504\n" \
  "s_waitcnt lgkmcnt(6)\n" \
  "v_mfma_f32_32x32x16_bf16 v[16:31], v[128:131], v[144:147], v[16:31]\n" \
  "ds_read_b64_tr_b16 v[150:151], v228 offset:22784\n" \
  "ds_read_b128 v[164:167], v225 offset:4096\n" \
  "s_waitcnt lgkmcnt(6)\n" \
  "v_mfma_f32_32x32x16_bf16 v[32:47], v[132:135], v[140:143], v[32:47]\n" \
  "ds_read_b64_tr_b16 v[152:153], v228 offset:21568\n" \
  "ds_read_b64_tr_b16 v[154:155], v228 offset:22848\n" \
  "v_mfma_f32_32x32x16_bf16 v[48:63], v[132:135], v[144:147], v[48:63]\n" \
  "ds_read_b64_tr_b16 v[156:157], v229 offset:37504\n" \
  "ds_read_b64_tr_b16 v[158:159], v229 offset:37664\n" \
  "s_waitcnt lgkmcnt(8)\n" \
  "v_mfma_f32_32x32x16_bf16 v[64:79], v[136:139], v[140:143], v[64:79]\n" \
  "s_add_u32 %[sl], %[sl], 1\n" \
  "s_cmp_eq_u32 %[sl], 3\n" \
  "s_mov_b32 %[d], 49152\n" \
  "v_mfma_f32_32x32x16_bf16 v[80:95], v[136:139], v[144:147], v[80:95]\n" \
  "s_cselect_b32 %[d], 4294868992, %[d]\n" \
  "s_cselect_b32 %[sl], 0, %[sl]\n" \
  "s_waitcnt lgkmcnt(5)\n" \
  "v_mfma_f32_32x32x16_bf16 v[0:15], v[148:151], v[160:163], v[0:15]\n" \
  "ds_read_b128 v[140:143], v226\n" \
  "ds_read_b64_tr_b16 v[128:129], v228 offset:26624\n" \
  "s_waitcnt lgkmcnt(6)\n" \
  "v_mfma_f32_32x32x16_bf16 v[16:31], v[148:151], v[164:167], v[16:31]\n" \
  "ds_read_b64_tr_b16 v[130:131], v228 offset:27904\n" \
  "ds_read_b128 v[144:147], v226 offset:4096\n" \
  "s_waitcnt lgkmcnt(6)\n" \
  "v_mfma_f32_32x32x16_bf16 v[32:47], v[152:155], v[160:163], v[32:47]\n" \
  "ds_read_b64_tr_b16 v[132:133], v228 offset:26688\n" \
  "ds_read_b64_tr_b16 v[134:135], v228 offset:27968\n" \
  "v_mfma_f32_32x32x16_bf16 v[48:63], v[152:155], v[164:167], v[48:63]\n" \
  "ds_read_b64_tr_b16 v[136:137], v229 offset:38144\n" \
  "ds_read_b64_tr_b16 v[138:139], v229 offset:38304\n" \
  "s_waitcnt lgkmcnt(8)\n" \
  "v_mfma_f32_32x32x16_bf16 v[64:79], v[156:159], v[160:163], v[64:79]\n" \
  "v_mfma_f32_32x32x16_bf16 v[80:95], v[156:159], v[164:167], v[80:95]\n" \
  "s_waitcnt lgkmcnt(5)\n" \
  "v_mfma_f32_32x32x16_bf16 v[0:15], v[128:131], v[140:143], v[0:15]\n" \
  "ds_read_b128 v[160:163], v227\n" \
  "ds_read_b64_tr_b16 v[148:149], v228 offset:31744\n" \
  "s_waitcnt lgkmcnt(6)\n" \
  "v_mfma_f32_32x32x16_bf16 v[16:31], v[128:131], v[144:147], v[16:31]\n" \
  "ds_read_b64_tr_b16 v[150:151], v228 offset:33024\n" \
  "ds_read_b128 v[164:167], v227 offset:4096\n" \
  "s_waitcnt lgkmcnt(6)\n" \
  "v_mfma_f32_32x32x16_bf16 v[32:47], v[132:135], v[140:143], v[32:47]\n" \
  "ds_read_b64_tr_b16 v[152:153], v228 offset:31808\n" \
  "ds_read_b64_tr_b16 v[154:155], v228 offset:33088\n" \
  "v_mfma_f32_32x32x16_bf16 v[48:63], v[132:135], v[144:147], v[48:63]\n" \
  "ds_read_b64_tr_b16 v[156:157], v229 offset:38784\n" \
  "ds_read_b64_tr_b16 v[158:159], v229 offset:38944\n" \
  "s_waitcnt lgkmcnt(8)\n" \
  "v_mfma_f32_32x32x16_bf16 v[64:79], v[136:139], v[140:143], v[64:79]\n" \
  "s_sub_u32 %[cnt], %[cnt], 1\n" \
  "s_cmp_lg_u32 %[cnt], 0\n" \
  "v_mfma_f32_32x32x16_bf16 v[80:95], v[136:139], v[144:147], v[80:95]\n" \
  "s_waitcnt vmcnt(0) lgkmcnt(0)\n" \
  "s_cbranch_scc1 10b\n" \
  "s_barrier\n" \
  "v_mfma_f32_32x32x16_bf16 v[0:15], v[148:151], v[160:163], v[0:15]\n" \
  "v_add_u32 v224, %[d], v224\n" \
  "v_add_u32 v228, %[d], v228\n" \
  "ds_read_b128 v[140:143], v224\n" \
  "ds_read_b64_tr_b16 v[128:129], v228 offset:16384\n" \
  "v_mfma_f32_32x32x16_bf16 v[16:31], v[148:151], v[164:167], v[16:31]\n" \
  "v_add_u32 v229, %[d], v229\n" \
  "ds_read_b64_tr_b16 v[130:131], v228 offset:17664\n" \
  "ds_read_b128 v[144:147], v224 offset:4096\n" \
  "v_mfma_f32_32x32x16_bf16 v[32:47], v[152:155], v[160:163], v[32:47]\n" \
  "v_add_u32 v225, %[d], v225\n" \
  "ds_read_b64_tr_b16 v[132:133], v228 offset:16448\n" \
  "ds_read_b64_tr_b16 v[134:135], v228 offset:17728\n" \
  "v_mfma_f32_32x32x16_bf16 v[48:63], v[152:155], v[164:167], v[48:63]\n" \
  "v_add_u32 v226, %[d], v226\n" \
  "ds_read_b64_tr_b16 v[136:137], v229 offset:36864\n" \
  "ds_read_b64_tr_b16 v[138:139], v229 offset:37024\n" \
  "v_mfma_f32_32x32x16_bf16 v[64:79], v[156:159], v[160:163], v[64:79]\n" \
  "v_add_u32 v227, %[d], v227\n" \
  "v_mfma_f32_32x32x16_bf16 v[80:95], v[156:159], v[164:167], v[80:95]\n" \
  "s_waitcnt lgkmcnt(5)\n" \
  "v_mfma_f32_32x32x16_bf16 v[0:15], v[128:131], v[140:143], v[0:15]\n" \
  "ds_read_b128 v[160:163], v225\n" \
  "ds_read_b64_tr_b16 v[156:157], v229 offset:37504\n" \
  "ds_read_b64_tr_b16 v[158:159], v229 offset:37664\n" \
  "s_waitcnt lgkmcnt(7)\n" \
  "v_mfma_f32_32x32x16_bf16 v[16:31], v[128:131], v[144:147], v[16:31]\n" \
  "ds_read_b128 v[164:167], v225 offset:4096\n" \
  "ds_read_b128 v[180:183], v226\n" \
  "ds_read_b64_tr_b16 v[176:177], v229 offset:38144\n" \
  "s_waitcnt lgkmcnt(8)\n" \
  "v_mfma_f32_32x32x16_bf16 v[32:47], v[132:135], v[140:143], v[32:47]\n" \
  "ds_read_b64_tr_b16 v[178:179], v229 offset:38304\n" \
  "ds_read_b128 v[184:187], v226 offset:4096\n" \
  "ds_read_b128 v[200:203], v227\n" \
  "v_mfma_f32_32x32x16_bf16 v[48:63], v[132:135], v[144:147], v[48:63]\n" \
  "ds_read_b64_tr_b16 v[196:197], v229 offset:38784\n" \
  "ds_read_b64_tr_b16 v[198:199], v229 offset:38944\n" \
  "ds_read_b128 v[204:207], v227 offset:4096\n" \
  "s_waitcnt lgkmcnt(12)\n" \
  "v_mfma_f32_32x32x16_bf16 v[64:79], v[136:139], v[140:143], v[64:79]\n" \
  "ds_read_b64_tr_b16 v[148:149], v228 offset:21504\n" \
  "ds_read_b64_tr_b16 v[150:151], v228 offset:22784\n" \
  "ds_read_b64_tr_b16 v[168:169], v228 offset:26624\n" \
  "v_mfma_f32_32x32x16_bf16 v[80:95], v[136:139], v[144:147], v[80:95]\n" \
  "ds_read_b64_tr_b16 v[170:171], v228 offset:27904\n" \
  "ds_read_b64_tr_b16 v[188:189], v228 offset:31744\n" \
  "ds_read_b64_tr_b16 v[190:191], v228 offset:33024\n" \
  "s_waitcnt lgkmcnt(15)\n" \
  "v_mfma_f32_32x32x16_bf16 v[64:79], v[156:159], v[160:163], v[64:79]\n" \
  "ds_read_b64_tr_b16 v[152:153], v228 offset:21568\n" \
  "ds_read_b64_tr_b16 v[154:155], v228 offset:22848\n" \
  "ds_read_b64_tr_b16 v[172:173], v228 offset:26688\n" \
  "s_waitcnt lgkmcnt(15)\n" \
  "v_mfma_f32_32x32x16_bf16 v[80:95], v[156:159], v[164:167], v[80:95]\n" \
  "ds_read_b64_tr_b16 v[174:175], v228 offset:27968\n" \
  "ds_read_b64_tr_b16 v[192:193], v228 offset:31808\n" \
  "ds_read_b64_tr_b16 v[194:195], v228 offset:33088\n" \
  "s_waitcnt lgkmcnt(15)\n" \
  "v_mfma_f32_32x32x16_bf16 v[64:79], v[176:179], v[180:183], v[64:79]\n" \
  "s_mov_b32 vcc_lo, 0\n" \
  "s_mov_b32 vcc_hi, -1\n" \
  "s_mov_b32 s84, 0xfffff\n" \
  "s_mov_b32 s85, 0xfffff\n" \
  "v_mfma_f32_32x32x16_bf16 v[80:95], v[176:179], v[184:187], v[80:95]\n" \
  "v_mov_b32 v218, 0x3f803f80\n" \
  "v_mov_b32 v219, 0x3f80\n" \
  "v_mov_b32 v220, 0\n" \
  "s_waitcnt lgkmcnt(13)\n" \
  "v_mfma_f32_32x32x16_bf16 v[64:79], v[196:199], v[200:203], v[64:79]\n" \
  "s_waitcnt lgkmcnt(12)\n" \
  "v_mfma_f32_32x32x16_bf16 v[80:95], v[196:199], v[204:207], v[80:95]\n" \
  "s_nop 4\n" \
  "s_waitcnt lgkmcnt(10)\n" \
  "v_mfma_f32_32x32x16_bf16 v[0:15], v[148:151], v[160:163], v[0:15]\n" \
  "s_nop 4\n" \
  "v_mfma_f32_32x32x16_bf16 v[16:31], v[148:151], v[164:167], v[16:31]\n" \
  "v_mul_f32 v64, %[qs], v64\n" \
  "v_mul_f32 v65, %[qs], v65\n" \
  "v_mul_f32 v66, %[qs], v66\n" \
  "v_mul_f32 v67, %[qs], v67\n" \
  "v_mul_f32 v68, %[qs], v68\n" \
  "s_waitcnt lgkmcnt(8)\n" \
  "v_mfma_f32_32x32x16_bf16 v[0:15], v[168:171], v[180:183], v[0:15]\n" \
  "v_mul_f32 v69, %[qs], v69\n" \
  "v_mul_f32 v70, %[qs], v70\n" \
  "v_mul_f32 v71, %[qs], v71\n" \
  "v_mul_f32 v72, %[qs], v72\n" \
  "v_mul_f32 v73, %[qs], v73\n" \
  "v_mfma_f32_32x32x16_bf16 v[16:31], v[168:171], v[184:187], v[16:31]\n" \
  "v_mul_f32 v74, %[qs], v74\n" \
  "v_mul_f32 v75, %[qs], v75\n" \
  "v_mul_f32 v214, v64, v64\n" \
  "v_mul_f32 v215, v72, v72\n" \
  "v_cvt_pk_bf16_f32 v96, v64, v65\n" \
  "s_waitcnt lgkmcnt(6)\n" \
  "v_mfma_f32_32x32x16_bf16 v[0:15], v[188:191], v[200:203], v[0:15]\n" \
  "v_cvt_pk_bf16_f32 v97, v66, v67\n" \
  "v_cvt_pk_bf16_f32 v98, v68, v69\n" \
  "v_cvt_pk_bf16_f32 v99, v70, v71\n" \
  "v_add_u32 v100, 0, v241\n" \
  "buffer_store_dwordx4 v[96:99], v100, %[rs], 0 offen sc1\n" \
  "v_mfma_f32_32x32x16_bf16 v[16:31], v[188:191], v[204:207], v[16:31]\n" \
  "v_cvt_pk_bf16_f32 v102, v72, v73\n" \
  "v_cvt_pk_bf16_f32 v103, v74, v75\n" \
  "v_cndmask_b32 v102, v102, v218, vcc\n" \
  "v_cndmask_b32 v103, v103, v219, vcc\n" \
  "v_add_u32 v106, 0, v242\n" \
  "s_waitcnt lgkmcnt(4)\n" \
  "v_mfma_f32_32x32x16_bf16 v[32:47], v[152:155], v[160:163], v[32:47]\n" \
  "buffer_store_dwordx2 v[102:103], v106, %[rs], 0 offen sc1\n" \
  "v_fmac_f32 v214, v65, v65\n" \
  "v_fmac_f32 v214, v66, v66\n" \
  "v_fmac_f32 v214, v67, v67\n" \
  "v_fmac_f32 v214, v68, v68\n" \
  "v_mfma_f32_32x32x16_bf16 v[48:63], v[152:155], v[164:167], v[48:63]\n" \
  "v_fmac_f32 v214, v69, v69\n" \
  "v_fmac_f32 v214, v70, v70\n" \
  "v_fmac_f32 v214, v71, v71\n" \
  "v_fmac_f32 v215, v73, v73\n" \
  "v_fmac_f32 v215, v74, v74\n" \
  "s_waitcnt lgkmcnt(2)\n" \
  "v_mfma_f32_32x32x16_bf16 v[32:47], v[172:175], v[180:183], v[32:47]\n" \
  "v_fmac_f32 v215, v75, v75\n" \
  "v_cndmask_b32 v215, v215, v220, vcc\n" \
  "v_add_f32 v214, v214, v215\n" \
  "v_mov_b32 v215, v214\n" \
  "s_nop 1\n" \
  "v_mfma_f32_32x32x16_bf16 v[48:63], v[172:175], v[184:187], v[48:63]\n" \
  "v_permlane32_swap_b32 v214, v215\n" \
  "s_nop 0\n" \
  "v_add_f32 v214, v214, v215\n" \
  "v_mul_f32 v80, %[qs], v80\n" \
  "v_mul_f32 v81, %[qs], v81\n" \
  "s_waitcnt lgkmcnt(0)\n" \
  "v_mfma_f32_32x32x16_bf16 v[32:47], v[192:195], v[200:203], v[32:47]\n" \
  "v_mul_f32 v82, %[qs], v82\n" \
  "v_mul_f32 v83, %[qs], v83\n" \
  "v_mul_f32 v84, %[qs], v84\n" \
  "v_mul_f32 v85, %[qs], v85\n" \
  "v_mul_f32 v86, %[qs], v86\n" \
  "v_mfma_f32_32x32x16_bf16 v[48:63], v[192:195], v[204:207], v[48:63]\n" \
  "v_mul_f32 v87, %[qs], v87\n" \
  "v_mul_f32 v88, %[qs], v88\n" \
  "v_mul_f32 v89, %[qs], v89\n" \
  "v_mul_f32 v90, %[qs], v90\n" \
  "v_mul_f32 v91, %[qs], v91\n" \
  "v_mul_f32 v216, v80, v80\n" \
  "v_mul_f32 v217, v88, v88\n" \
  "v_cvt_pk_bf16_f32 v108, v80, v81\n" \
  "v_cvt_pk_bf16_f32 v109, v82, v83\n" \
  "v_cvt_pk_bf16_f32 v110, v84, v85\n" \
  "v_cvt_pk_bf16_f32 v111, v86, v87\n" \
  "v_add_u32 v112, 3584, v241\n" \
  "buffer_store_dwordx4 v[108:111], v112, %[rs], 0 offen sc1\n" \
  "v_cvt_pk_bf16_f32 v114, v88, v89\n" \
  "v_cvt_pk_bf16_f32 v115, v90, v91\n" \
  "v_cndmask_b32 v114, v114, v218, vcc\n" \
  "v_cndmask_b32 v115, v115, v219, vcc\n" \
  "v_add_u32 v118, 3584, v242\n" \
  "buffer_store_dwordx2 v[114:115], v118, %[rs], 0 offen sc1\n" \
  "v_fmac_f32 v216, v81, v81\n" \
  "v_fmac_f32 v216, v82, v82\n" \
  "v_fmac_f32 v216, v83, v83\n" \
  "v_fmac_f32 v216, v84, v84\n" \
  "v_fmac_f32 v216, v85, v85\n" \
  "v_fmac_f32 v216, v86, v86\n" \
  "v_fmac_f32 v216, v87, v87\n" \
  "v_fmac_f32 v217, v89, v89\n" \
  "v_fmac_f32 v217, v90, v90\n" \
  "v_fmac_f32 v217, v91, v91\n" \
  "v_cndmask_b32 v217, v217, v220, vcc\n" \
  "v_add_f32 v216, v216, v217\n" \
  "v_mov_b32 v217, v216\n" \
  "s_nop 1\n" \
  "v_permlane32_swap_b32 v216, v217\n" \
  "s_nop 0\n" \
  "v_add_f32 v216, v216, v217\n" \
  "v_max_f32 v214, v214, v216\n" \
  "s_nop 1\n" \
  "v_max_f32_dpp v214, v214, v214 row_ror:8 row_mask:0xf bank_mask:0xf\n" \
  "s_nop 1\n" \
  "v_max_f32_dpp v214, v214, v214 row_ror:4 row_mask:0xf bank_mask:0xf\n" \
  "s_nop 1\n" \
  "v_max_f32_dpp v214, v214, v214 row_ror:2 row_mask:0xf bank_mask:0xf\n" \
  "s_nop 1\n" \
  "v_max_f32_dpp v214, v214, v214 row_ror:1 row_mask:0xf bank_mask:0xf\n" \
  "s_nop 1\n" \
  "v_mov_b32 v215, v214\n" \
  "s_nop 1\n" \
  "v_permlane16_swap_b32 v214, v215\n" \
  "s_nop 0\n" \
  "v_max_f32 v214, v214, v215\n" \
  "s_mov_b64 exec, 1\n" \
  "buffer_store_dword v214, v243, %[rs], 0 offen sc1\n" \
  "s_mov_b64 exec, -1\n" \
  "v_cvt_pk_bf16_f32 v208, v0, v1\n" \
  "v_cvt_pk_bf16_f32 v209, v2, v3\n" \
  "v_cvt_pk_bf16_f32 v210, v4, v5\n" \
  "v_cvt_pk_bf16_f32 v211, v6, v7\n" \
  "v_cvt_pk_bf16_f32 v212, v8, v9\n" \
  "v_cvt_pk_bf16_f32 v213, v10, v11\n" \
  "v_cndmask_b32 v96, v210, v208, vcc\n" \
  "v_cndmask_b32 v97, v211, v209, vcc\n" \
  "v_cndmask_b32 v98, v212, v210, vcc\n" \
  "v_cndmask_b32 v99, v213, v211, vcc\n" \
  "v_cndmask_b32 v120, v208, v212, vcc\n" \
  "v_cndmask_b32 v121, v209, v213, vcc\n" \
  "v_add_u32 v100, 20480, v231\n" \
  "buffer_store_dwordx4 v[96:99], v100, %[rs], 0 offen sc1\n" \
  "v_cvt_pk_bf16_f32 v208, v16, v17\n" \
  "v_cvt_pk_bf16_f32 v209, v18, v19\n" \
  "v_cvt_pk_bf16_f32 v210, v20, v21\n" \
  "v_cvt_pk_bf16_f32 v211, v22, v23\n" \
  "v_cvt_pk_bf16_f32 v212, v24, v25\n" \
  "v_cvt_pk_bf16_f32 v213, v26, v27\n" \
  "v_cndmask_b32 v102, v210, v208, vcc\n" \
  "v_cndmask_b32 v103, v211, v209, vcc\n" \
  "v_cndmask_b32 v104, v212, v210, vcc\n" \
  "v_cndmask_b32 v105, v213, v211, vcc\n" \
  "v_cndmask_b32 v124, v208, v212, vcc\n" \
  "v_cndmask_b32 v125, v209, v213, vcc\n" \
  "v_add_u32 v106, 21504, v231\n" \
  "buffer_store_dwordx4 v[102:105], v106, %[rs], 0 offen sc1\n" \
  "v_cvt_pk_bf16_f32 v108, v12, v13\n" \
  "v_cvt_pk_bf16_f32 v109, v14, v15\n" \
  "v_cvt_pk_bf16_f32 v110, v32, v33\n" \
  "v_cvt_pk_bf16_f32 v111, v34, v35\n" \
  "v_add_u32 v112, 24576, v230\n" \
  "buffer_store_dwordx4 v[108:111], v112, %[rs], 0 offen sc1\n" \
  "v_cvt_pk_bf16_f32 v208, v36, v37\n" \
  "v_cvt_pk_bf16_f32 v209, v38, v39\n" \
  "v_cvt_pk_bf16_f32 v210, v40, v41\n" \
  "v_cvt_pk_bf16_f32 v211, v42, v43\n" \
  "v_cvt_pk_bf16_f32 v212, v44, v45\n" \
  "v_cvt_pk_bf16_f32 v213, v46, v47\n" \
  "v_cndmask_b32 v114, v210, v208, vcc\n" \
  "v_cndmask_b32 v115, v211, v209, vcc\n" \
  "v_cndmask_b32 v116, v212, v210, vcc\n" \
  "v_cndmask_b32 v117, v213, v211, vcc\n" \
  "v_cndmask_b32 v122, v208, v212, vcc\n" \
  "v_cndmask_b32 v123, v209, v213, vcc\n" \
  "v_add_u32 v118, 28672, v231\n" \
  "buffer_store_dwordx4 v[114:117], v118, %[rs], 0 offen sc1\n" \
  "v_add_u32 v96, 4096, v240\n" \
  "buffer_store_dwordx4 v[120:123], v96, %[rs], 0 offen sc1\n" \
  "v_cvt_pk_bf16_f32 v102, v28, v29\n" \
  "v_cvt_pk_bf16_f32 v103, v30, v31\n" \
  "v_cvt_pk_bf16_f32 v104, v48, v49\n" \
  "v_cvt_pk_bf16_f32 v105, v50, v51\n" \
  "v_add_u32 v106, 25600, v230\n" \
  "buffer_store_dwordx4 v[102:105], v106, %[rs], 0 offen sc1\n" \
  "v_cvt_pk_bf16_f32 v208, v52, v53\n" \
  "v_cvt_pk_bf16_f32 v209, v54, v55\n" \
  "v_cvt_pk_bf16_f32 v210, v56, v57\n" \
  "v_cvt_pk_bf16_f32 v211, v58, v59\n" \
  "v_cvt_pk_bf16_f32 v212, v60, v61\n" \
  "v_cvt_pk_bf16_f32 v213, v62, v63\n" \
  "v_cndmask_b32 v108, v210, v208, vcc\n" \
  "v_cndmask_b32 v109, v211, v209, vcc\n" \
  "v_cndmask_b32 v110, v212, v210, vcc\n" \
  "v_cndmask_b32 v111, v213, v211, vcc\n" \
  "v_cndmask_b32 v126, v208, v212, vcc\n" \
  "v_cndmask_b32 v127, v209, v213, vcc\n" \
  "v_add_u32 v112, 29696, v231\n" \
  "buffer_store_dwordx4 v[108:111], v112, %[rs], 0 offen sc1\n" \
  "v_add_u32 v114, 5120, v240\n" \
  "buffer_store_dwordx4 v[124:127], v114, %[rs], 0 offen sc1\n" \
  "s_branch 4f\n" \
  "2:\n" \
  "ds_read_b128 v[140:143], v224\n" \
  "ds_read_b64_tr_b16 v[128:129], v228 offset:16384\n" \
  "ds_read_b64_tr_b16 v[130:131], v228 offset:17664\n" \
  "ds_read_b128 v[144:147], v224 offset:4096\n" \
  "ds_read_b64_tr_b16 v[132:133], v228 offset:16448\n" \
  "ds_read_b64_tr_b16 v[134:135], v228 offset:17728\n" \
  "ds_read_b64_tr_b16 v[136:137], v229 offset:36864\n" \
  "ds_read_b64_tr_b16 v[138:139], v229 offset:37024\n" \
  "s_mov_b32 %[cnt], 8\n" \
  "s_waitcnt lgkmcnt(5)\n" \
  "v_mfma_f32_32x32x16_bf16 v[0:15], v[128:131], v[140:143], v[0:15]\n" \
  "ds_read_b128 v[160:163], v225\n" \
  "ds_read_b64_tr_b16 v[148:149], v228 offset:21504\n" \
  "s_waitcnt lgkmcnt(6)\n" \
  "v_mfma_f32_32x32x16_bf16 v[16:31], v[128:131], v[144:147], v[16:31]\n" \
  "ds_read_b64_tr_b16 v[150:151], v228 offset:22784\n" \
  "ds_read_b128 v[164:167], v225 offset:4096\n" \
  "s_waitcnt lgkmcnt(6)\n" \
  "v_mfma_f32_32x32x16_bf16 v[32:47], v[132:135], v[140:143], v[32:47]\n" \
  "ds_read_b64_tr_b16 v[152:153], v228 offset:21568\n" \
  "ds_read_b64_tr_b16 v[154:155], v228 offset:22848\n" \
  "v_mfma_f32_32x32x16_bf16 v[48:63], v[132:135], v[144:147], v[48:63]\n" \
  "ds_read_b64_tr_b16 v[156:157], v229 offset:37504\n" \
  "ds_read_b64_tr_b16 v[158:159], v229 offset:37664\n" \
  "s_waitcnt lgkmcnt(8)\n" \
  "v_mfma_f32_32x32x16_bf16 v[64:79], v[140:143], v[136:139], v[64:79]\n" \
  "s_add_u32 %[sl], %[sl], 1\n" \
  "s_cmp_eq_u32 %[sl], 3\n" \
  "s_mov_b32 %[d], 49152\n" \
  "v_mfma_f32_32x32x16_bf16 v[80:95], v[144:147], v[136:139], v[80:95]\n" \
  "s_cselect_b32 %[d], 4294868992, %[d]\n" \
  "s_cselect_b32 %[sl], 0, %[sl]\n" \
  "s_waitcnt lgkmcnt(5)\n" \
  "v_mfma_f32_32x32x16_bf16 v[0:15], v[148:151], v[160:163], v[0:15]\n" \
  "ds_read_b128 v[140:143], v226\n" \
  "ds_read_b64_tr_b16 v[128:129], v228 offset:26624\n" \
  "s_waitcnt lgkmcnt(6)\n" \
  "v_mfma_f32_32x32x16_bf16 v[16:31], v[148:151], v[164:167], v[16:31]\n" \
  "ds_read_b64_tr_b16 v[130:131], v228 offset:27904\n" \
  "ds_read_b128 v[144:147], v226 offset:4096\n" \
  "s_waitcnt lgkmcnt(6)\n" \
  "v_mfma_f32_32x32x16_bf16 v[32:47], v[152:155], v[160:163], v[32:47]\n" \
  "ds_read_b64_tr_b16 v[132:133], v228 offset:26688\n" \
  "ds_read_b64_tr_b16 v[134:135], v228 offset:27968\n" \
  "v_mfma_f32_32x32x16_bf16 v[48:63], v[152:155], v[164:167], v[48:63]\n" \
  "ds_read_b64_tr_b16 v[136:137], v229 offset:38144\n" \
  "ds_read_b64_tr_b16 v[138:139], v229 offset:38304\n" \
  "s_waitcnt lgkmcnt(8)\n" \
  "v_mfma_f32_32x32x16_bf16 v[64:79], v[160:163], v[156:159], v[64:79]\n" \
  "v_mfma_f32_32x32x16_bf16 v[80:95], v[164:167], v[156:159], v[80:95]\n" \
  "s_waitcnt lgkmcnt(5)\n" \
  "v_mfma_f32_32x32x16_bf16 v[0:15], v[128:131], v[140:143], v[0:15]\n" \
  "ds_read_b128 v[160:163], v227\n" \
  "ds_read_b64_tr_b16 v[148:149], v228 offset:31744\n" \
  "s_waitcnt lgkmcnt(6)\n" \
  "v_mfma_f32_32x32x16_bf16 v[16:31], v[128:131], v[144:147], v[16:31]\n" \
  "ds_read_b64_tr_b16 v[150:151], v228 offset:33024\n" \
  "ds_read_b128 v[164:167], v227 offset:4096\n" \
  "s_waitcnt lgkmcnt(6)\n" \
  "v_mfma_f32_32x32x16_bf16 v[32:47], v[132:135], v[140:143], v[32:47]\n" \
  "ds_read_b64_tr_b16 v[152:153], v228 offset:31808\n" \
  "ds_read_b64_tr_b16 v[154:155], v228 offset:33088\n" \
  "v_mfma_f32_32x32x16_bf16 v[48:63], v[132:135], v[144:147], v[48:63]\n" \
  "ds_read_b64_tr_b16 v[156:157], v229 offset:38784\n" \
  "ds_read_b64_tr_b16 v[158:159], v229 offset:38944\n" \
  "s_waitcnt lgkmcnt(8)\n" \
  "v_mfma_f32_32x32x16_bf16 v[64:79], v[140:143], v[136:139], v[64:79]\n" \
  "v_mfma_f32_32x32x16_bf16 v[80:95], v[144:147], v[136:139], v[80:95]\n" \
  "s_waitcnt lgkmcnt(0)\n" \
  "11:\n" \
  "s_barrier\n" \
  "v_mfma_f32_32x32x16_bf16 v[0:15], v[148:151], v[160:163], v[0:15]\n" \
  "v_add_u32 v224, %[d], v224\n" \
  "v_add_u32 v228, %[d], v228\n" \
  "ds_read_b128 v[140:143], v224\n" \
  "ds_read_b64_tr_b16 v[128:129], v228 offset:16384\n" \
  "v_mfma_f32_32x32x16_bf16 v[16:31], v[148:151], v[164:167], v[16:31]\n" \
  "v_add_u32 v229, %[d], v229\n" \
  "ds_read_b64_tr_b16 v[130:131], v228 offset:17664\n" \
  "ds_read_b128 v[144:147], v224 offset:4096\n" \
  "v_mfma_f32_32x32x16_bf16 v[32:47], v[152:155], v[160:163], v[32:47]\n" \
  "v_add_u32 v225, %[d], v225\n" \
  "ds_read_b64_tr_b16 v[132:133], v228 offset:16448\n" \
  "ds_read_b64_tr_b16 v[134:135], v228 offset:17728\n" \
  "v_mfma_f32_32x32x16_bf16 v[48:63], v[152:155], v[164:167], v[48:63]\n" \
  "v_add_u32 v226, %[d], v226\n" \
  "ds_read_b64_tr_b16 v[136:137], v229 offset:36864\n" \
  "ds_read_b64_tr_b16 v[138:139], v229 offset:37024\n" \
  "v_mfma_f32_32x32x16_bf16 v[64:79], v[160:163], v[156:159], v[64:79]\n" \
  "v_add_u32 v227, %[d], v227\n" \
  "v_mfma_f32_32x32x16_bf16 v[80:95], v[164:167], v[156:159], v[80:95]\n" \
  "s_waitcnt lgkmcnt(5)\n" \
  "v_mfma_f32_32x32x16_bf16 v[0:15], v[128:131], v[140:143], v[0:15]\n" \
  "ds_read_b128 v[160:163], v225\n" \
  "ds_read_b64_tr_b16 v[148:149], v228 offset:21504\n" \
  "s_waitcnt lgkmcnt(6)\n" \
  "v_mfma_f32_32x32x16_bf16 v[16:31], v[128:131], v[144:147], v[16:31]\n" \
  "ds_read_b64_tr_b16 v[150:151], v228 offset:22784\n" \
  "ds_read_b128 v[164:167], v225 offset:4096\n" \
  "s_waitcnt lgkmcnt(6)\n" \
  "v_mfma_f32_32x32x16_bf16 v[32:47], v[132:135], v[140:143], v[32:47]\n" \
  "ds_read_b64_tr_b16 v[152:153], v228 offset:21568\n" \
  "ds_read_b64_tr_b16 v[154:155], v228 offset:22848\n" \
  "v_mfma_f32_32x32x16_bf16 v[48:63], v[132:135], v[144:147], v[48:63]\n" \
  "ds_read_b64_tr_b16 v[156:157], v229 offset:37504\n" \
  "ds_read_b64_tr_b16 v[158:159], v229 offset:37664\n" \
  "s_waitcnt lgkmcnt(8)\n" \
  "v_mfma_f32_32x32x16_bf16 v[64:79], v[140:143], v[136:139], v[64:79]\n" \
  "s_add_u32 %[sl], %[sl], 1\n" \
  "s_cmp_eq_u32 %[sl], 3\n" \
  "s_mov_b32 %[d], 49152\n" \
  "v_mfma_f32_32x32x16_bf16 v[80:95], v[144:147], v[136:139], v[80:95]\n" \
  "s_cselect_b32 %[d], 4294868992, %[d]\n" \
  "s_cselect_b32 %[sl], 0, %[sl]\n" \
  "s_waitcnt lgkmcnt(5)\n" \
  "v_mfma_f32_32x32x16_bf16 v[0:15], v[148:151], v[160:163], v[0:15]\n" \
  "ds_read_b128 v[140:143], v226\n" \
  "ds_read_b64_tr_b16 v[128:129], v228 offset:26624\n" \
  "s_waitcnt lgkmcnt(6)\n" \
  "v_mfma_f32_32x32x16_bf16 v[16:31], v[148:151], v[164:167], v[16:31]\n" \
  "ds_read_b64_tr_b16 v[130:131], v228 offset:27904\n" \
  "ds_read_b128 v[144:147], v226 offset:4096\n" \
  "s_waitcnt lgkmcnt(6)\n" \
  "v_mfma_f32_32x32x16_bf16 v[32:47], v[152:155], v[160:163], v[32:47]\n" \
  "ds_read_b64_tr_b16 v[132:133], v228 offset:26688\n" \
  "ds_read_b64_tr_b16 v[134:135], v228 offset:27968\n" \
  "v_mfma_f32_32x32x16_bf16 v[48:63], v[152:155], v[164:167], v[48:63]\n" \
  "ds_read_b64_tr_b16 v[136:137], v229 offset:38144\n" \
  "ds_read_b64_tr_b16 v[138:139], v229 offset:38304\n" \
  "s_waitcnt lgkmcnt(8)\n" \
  "v_mfma_f32_32x32x16_bf16 v[64:79], v[160:163], v[156:159], v[64:79]\n" \
  "v_mfma_f32_32x32x16_bf16 v[80:95], v[164:167], v[156:159], v[80:95]\n" \
  "s_waitcnt lgkmcnt(5)\n" \
  "v_mfma_f32_32x32x16_bf16 v[0:15], v[128:131], v[140:143], v[0:15]\n" \
  "ds_read_b128 v[160:163], v227\n" \
  "ds_read_b64_tr_b16 v[148:149], v228 offset:31744\n" \
  "s_waitcnt lgkmcnt(6)\n" \
  "v_mfma_f32_32x32x16_bf16 v[16:31], v[128:131], v[144:147], v[16:31]\n" \
  "ds_read_b64_tr_b16 v[150:151], v228 offset:33024\n" \
  "ds_read_b128 v[164:167], v227 offset:4096\n" \
  "s_waitcnt lgkmcnt(6)\n" \
  "v_mfma_f32_32x32x16_bf16 v[32:47], v[132:135], v[140:143], v[32:47]\n" \
  "ds_read_b64_tr_b16 v[152:153], v228 offset:31808\n" \
  "ds_read_b64_tr_b16 v[154:155], v228 offset:33088\n" \
  "v_mfma_f32_32x32x16_bf16 v[48:63], v[132:135], v[144:147], v[48:63]\n" \
  "ds_read_b64_tr_b16 v[156:157], v229 offset:38784\n" \
  "ds_read_b64_tr_b16 v[158:159], v229 offset:38944\n" \
  "s_waitcnt lgkmcnt(8)\n" \
  "v_mfma_f32_32x32x16_bf16 v[64:79], v[140:143], v[136:139], v[64:79]\n" \
  "s_sub_u32 %[cnt], %[cnt], 1\n" \
  "s_cmp_lg_u32 %[cnt], 0\n" \
  "v_mfma_f32_32x32x16_bf16 v[80:95], v[144:147], v[136:139], v[80:95]\n" \
  "s_waitcnt vmcnt(0) lgkmcnt(0)\n" \
  "s_cbranch_scc1 11b\n" \
  "s_barrier\n" \
  "v_mfma_f32_32x32x16_bf16 v[0:15], v[148:151], v[160:163], v[0:15]\n" \
  "v_add_u32 v224, %[d], v224\n" \
  "v_add_u32 v228, %[d], v228\n" \
  "ds_read_b128 v[140:143], v224\n" \
  "ds_read_b64_tr_b16 v[128:129], v228 offset:16384\n" \
  "v_mfma_f32_32x32x16_bf16 v[16:31], v[148:151], v[164:167], v[16:31]\n" \
  "v_add_u32 v229, %[d], v229\n" \
  "ds_read_b64_tr_b16 v[130:131], v228 offset:17664\n" \
  "ds_read_b128 v[144:147], v224 offset:4096\n" \
  "v_mfma_f32_32x32x16_bf16 v[32:47], v[152:155], v[160:163], v[32:47]\n" \
  "v_add_u32 v225, %[d], v225\n" \
  "ds_read_b64_tr_b16 v[132:133], v228 offset:16448\n" \
  "ds_read_b64_tr_b16 v[134:135], v228 offset:17728\n" \
  "v_mfma_f32_32x32x16_bf16 v[48:63], v[152:155], v[164:167], v[48:63]\n" \
  "v_add_u32 v226, %[d], v226\n" \
  "ds_read_b64_tr_b16 v[136:137], v229 offset:36864\n" \
  "ds_read_b64_tr_b16 v[138:139], v229 offset:37024\n" \
  "v_mfma_f32_32x32x16_bf16 v[64:79], v[160:163], v[156:159], v[64:79]\n" \
  "v_add_u32 v227, %[d], v227\n" \
  "v_mfma_f32_32x32x16_bf16 v[80:95], v[164:167], v[156:159], v[80:95]\n" \
  "s_waitcnt lgkmcnt(5)\n" \
  "v_mfma_f32_32x32x16_bf16 v[0:15], v[128:131], v[140:143], v[0:15]\n" \
  "ds_read_b128 v[160:163], v225\n" \
  "ds_read_b64_tr_b16 v[156:157], v229 offset:37504\n" \
  "ds_read_b64_tr_b16 v[158:159], v229 offset:37664\n" \
  "s_waitcnt lgkmcnt(7)\n" \
  "v_mfma_f32_32x32x16_bf16 v[16:31], v[128:131], v[144:147], v[16:31]\n" \
  "ds_read_b128 v[164:167], v225 offset:4096\n" \
  "ds_read_b128 v[180:183], v226\n" \
  "ds_read_b64_tr_b16 v[176:177], v229 offset:38144\n" \
  "s_waitcnt lgkmcnt(8)\n" \
  "v_mfma_f32_32x32x16_bf16 v[32:47], v[132:135], v[140:143], v[32:47]\n" \
  "ds_read_b64_tr_b16 v[178:179], v229 offset:38304\n" \
  "ds_read_b128 v[184:187], v226 offset:4096\n" \
  "ds_read_b128 v[200:203], v227\n" \
  "v_mfma_f32_32x32x16_bf16 v[48:63], v[132:135], v[144:147], v[48:63]\n" \
  "ds_read_b64_tr_b16 v[196:197], v229 offset:38784\n" \
  "ds_read_b64_tr_b16 v[198:199], v229 offset:38944\n" \
  "ds_read_b128 v[204:207], v227 offset:4096\n" \
  "s_waitcnt lgkmcnt(12)\n" \
  "v_mfma_f32_32x32x16_bf16 v[64:79], v[140:143], v[136:139], v[64:79]\n" \
  "ds_read_b64_tr_b16 v[148:149], v228 offset:21504\n" \
  "ds_read_b64_tr_b16 v[150:151], v228 offset:22784\n" \
  "ds_read_b64_tr_b16 v[168:169], v228 offset:26624\n" \
  "v_mfma_f32_32x32x16_bf16 v[80:95], v[144:147], v[136:139], v[80:95]\n" \
  "ds_read_b64_tr_b16 v[170:171], v228 offset:27904\n" \
  "ds_read_b64_tr_b16 v[188:189], v228 offset:31744\n" \
  "ds_read_b64_tr_b16 v[190:191], v228 offset:33024\n" \
  "s_waitcnt lgkmcnt(15)\n" \
  "v_mfma_f32_32x32x16_bf16 v[64:79], v[160:163], v[156:159], v[64:79]\n" \
  "ds_read_b64_tr_b16 v[152:153], v228 offset:21568\n" \
  "ds_read_b64_tr_b16 v[154:155], v228 offset:22848\n" \
  "ds_read_b64_tr_b16 v[172:173], v228 offset:26688\n" \
  "s_waitcnt lgkmcnt(15)\n" \
  "v_mfma_f32_32x32x16_bf16 v[80:95], v[164:167], v[156:159], v[80:95]\n" \
  "ds_read_b64_tr_b16 v[174:175], v228 offset:27968\n" \
  "ds_read_b64_tr_b16 v[192:193], v228 offset:31808\n" \
  "ds_read_b64_tr_b16 v[194:195], v228 offset:33088\n" \
  "s_waitcnt lgkmcnt(15)\n" \
  "v_mfma_f32_32x32x16_bf16 v[64:79], v[180:183], v[176:179], v[64:79]\n" \
  "s_mov_b32 vcc_lo, 0\n" \
  "s_mov_b32 vcc_hi, -1\n" \
  "s_mov_b32 s84, 0xfffff\n" \
  "s_mov_b32 s85, 0xfffff\n" \
  "v_mfma_f32_32x32x16_bf16 v[80:95], v[184:187], v[176:179], v[80:95]\n" \
  "s_waitcnt lgkmcnt(13)\n" \
  "v_mfma_f32_32x32x16_bf16 v[64:79], v[200:203], v[196:199], v[64:79]\n" \
  "s_waitcnt lgkmcnt(12)\n" \
  "v_mfma_f32_32x32x16_bf16 v[80:95], v[204:207], v[196:199], v[80:95]\n" \
  "s_nop 4\n" \
  "s_waitcnt lgkmcnt(10)\n" \
  "v_mfma_f32_32x32x16_bf16 v[0:15], v[148:151], v[160:163], v[0:15]\n" \
  "s_nop 4\n" \
  "v_mfma_f32_32x32x16_bf16 v[16:31], v[148:151], v[164:167], v[16:31]\n" \
  "v_cvt_pk_bf16_f32 v96, v64, v65\n" \
  "v_cvt_pk_bf16_f32 v97, v66, v67\n" \
  "v_cvt_pk_bf16_f32 v98, v68, v69\n" \
  "v_cvt_pk_bf16_f32 v99, v70, v71\n" \
  "v_cndmask_b32 v96, v243, v96, s[84:85]\n" \
  "s_waitcnt lgkmcnt(8)\n" \
  "v_mfma_f32_32x32x16_bf16 v[0:15], v[168:171], v[180:183], v[0:15]\n" \
  "v_cndmask_b32 v97, v243, v97, s[84:85]\n" \
  "v_cndmask_b32 v98, v243, v98, s[84:85]\n" \
  "v_cndmask_b32 v99, v243, v99, s[84:85]\n" \
  "v_add_u32 v100, 0, v241\n" \
  "buffer_store_dwordx4 v[96:99], v100, %[rs], 0 offen sc1\n" \
  "v_mfma_f32_32x32x16_bf16 v[16:31], v[168:171], v[184:187], v[16:31]\n" \
  "v_cvt_pk_bf16_f32 v102, v72, v73\n" \
  "v_cvt_pk_bf16_f32 v103, v74, v75\n" \
  "v_cvt_pk_bf16_f32 v104, v76, v77\n" \
  "v_cvt_pk_bf16_f32 v105, v78, v79\n" \
  "v_cndmask_b32 v102, v243, v102, s[84:85]\n" \
  "s_waitcnt lgkmcnt(6)\n" \
  "v_mfma_f32_32x32x16_bf16 v[0:15], v[188:191], v[200:203], v[0:15]\n" \
  "v_cndmask_b32 v103, v243, v103, s[84:85]\n" \
  "v_cndmask_b32 v104, v243, v104, s[84:85]\n" \
  "v_cndmask_b32 v105, v243, v105, s[84:85]\n" \
  "v_add_u32 v106, 1024, v241\n" \
  "buffer_store_dwordx4 v[102:105], v106, %[rs], 0 offen sc1\n" \
  "v_mfma_f32_32x32x16_bf16 v[16:31], v[188:191], v[204:207], v[16:31]\n" \
  "v_cvt_pk_bf16_f32 v108, v80, v81\n" \
  "v_cvt_pk_bf16_f32 v109, v82, v83\n" \
  "v_cvt_pk_bf16_f32 v110, v84, v85\n" \
  "v_cvt_pk_bf16_f32 v111, v86, v87\n" \
  "v_cndmask_b32 v108, v243, v108, s[84:85]\n" \
  "s_waitcnt lgkmcnt(4)\n" \
  "v_mfma_f32_32x32x16_bf16 v[32:47], v[152:155], v[160:163], v[32:47]\n" \
  "v_cndmask_b32 v109, v243, v109, s[84:85]\n" \
  "v_cndmask_b32 v110, v243, v110, s[84:85]\n" \
  "v_cndmask_b32 v111, v243, v111, s[84:85]\n" \
  "v_add_u32 v112, 3584, v241\n" \
  "buffer_store_dwordx4 v[108:111], v112, %[rs], 0 offen sc1\n" \
  "v_mfma_f32_32x32x16_bf16 v[48:63], v[152:155], v[164:167], v[48:63]\n" \
  "v_cvt_pk_bf16_f32 v114, v88, v89\n" \
  "v_cvt_pk_bf16_f32 v115, v90, v91\n" \
  "v_cvt_pk_bf16_f32 v116, v92, v93\n" \
  "v_cvt_pk_bf16_f32 v117, v94, v95\n" \
  "v_cndmask_b32 v114, v243, v114, s[84:85]\n" \
  "s_waitcnt lgkmcnt(2)\n" \
  "v_mfma_f32_32x32x16_bf16 v[32:47], v[172:175], v[180:183], v[32:47]\n" \
  "v_cndmask_b32 v115, v243, v115, s[84:85]\n" \
  "v_cndmask_b32 v116, v243, v116, s[84:85]\n" \
  "v_cndmask_b32 v117, v243, v117, s[84:85]\n" \
  "v_add_u32 v118, 4608, v241\n" \
  "buffer_store_dwordx4 v[114:117], v118, %[rs], 0 offen sc1\n" \
  "v_mfma_f32_32x32x16_bf16 v[48:63], v[172:175], v[184:187], v[48:63]\n" \
  "v_cvt_pk_bf16_f32 v208, v0, v1\n" \
  "v_cvt_pk_bf16_f32 v209, v2, v3\n" \
  "v_cvt_pk_bf16_f32 v210, v4, v5\n" \
  "v_cvt_pk_bf16_f32 v211, v6, v7\n" \
  "v_cvt_pk_bf16_f32 v212, v8, v9\n" \
  "s_waitcnt lgkmcnt(0)\n" \
  "v_mfma_f32_32x32x16_bf16 v[32:47], v[192:195], v[200:203], v[32:47]\n" \
  "v_cvt_pk_bf16_f32 v213, v10, v11\n" \
  "v_cndmask_b32 v96, v210, v208, vcc\n" \
  "v_cndmask_b32 v97, v211, v209, vcc\n" \
  "v_cndmask_b32 v98, v212, v210, vcc\n" \
  "v_cndmask_b32 v99, v213, v211, vcc\n" \
  "v_mfma_f32_32x32x16_bf16 v[48:63], v[192:195], v[204:207], v[48:63]\n" \
  "v_cndmask_b32 v120, v208, v212, vcc\n" \
  "v_cndmask_b32 v121, v209, v213, vcc\n" \
  "v_add_u32 v100, 20480, v231\n" \
  "buffer_store_dwordx4 v[96:99], v100, %[rs], 0 offen sc1\n" \
  "v_cvt_pk_bf16_f32 v208, v16, v17\n" \
  "v_cvt_pk_bf16_f32 v209, v18, v19\n" \
  "v_cvt_pk_bf16_f32 v210, v20, v21\n" \
  "v_cvt_pk_bf16_f32 v211, v22, v23\n" \
  "v_cvt_pk_bf16_f32 v212, v24, v25\n" \
  "v_cvt_pk_bf16_f32 v213, v26, v27\n" \
  "v_cndmask_b32 v102, v210, v208, vcc\n" \
  "v_cndmask_b32 v103, v211, v209, vcc\n" \
  "v_cndmask_b32 v104, v212, v210, vcc\n" \
  "v_cndmask_b32 v105, v213, v211, vcc\n" \
  "v_cndmask_b32 v124, v208, v212, vcc\n" \
  "v_cndmask_b32 v125, v209, v213, vcc\n" \
  "v_add_u32 v106, 21504, v231\n" \
  "buffer_store_dwordx4 v[102:105], v106, %[rs], 0 offen sc1\n" \
  "v_cvt_pk_bf16_f32 v108, v12, v13\n" \
  "v_cvt_pk_bf16_f32 v109, v14, v15\n" \
  "v_cvt_pk_bf16_f32 v110, v32, v33\n" \
  "v_cvt_pk_bf16_f32 v111, v34, v35\n" \
  "v_add_u32 v112, 24576, v230\n" \
  "buffer_store_dwordx4 v[108:111], v112, %[rs], 0 offen sc1\n" \
  "v_cvt_pk_bf16_f32 v208, v36, v37\n" \
  "v_cvt_pk_bf16_f32 v209, v38, v39\n" \
  "v_cvt_pk_bf16_f32 v210, v40, v41\n" \
  "v_cvt_pk_bf16_f32 v211, v42, v43\n" \
  "v_cvt_pk_bf16_f32 v212, v44, v45\n" \
  "v_cvt_pk_bf16_f32 v213, v46, v47\n" \
  "v_cndmask_b32 v114, v210, v208, vcc\n" \
  "v_cndmask_b32 v115, v211, v209, vcc\n" \
  "v_cndmask_b32 v116, v212, v210, vcc\n" \
  "v_cndmask_b32 v117, v213, v211, vcc\n" \
  "v_cndmask_b32 v122, v208, v212, vcc\n" \
  "v_cndmask_b32 v123, v209, v213, vcc\n" \
  "v_add_u32 v118, 28672, v231\n" \
  "buffer_store_dwordx4 v[114:117], v118, %[rs], 0 offen sc1\n" \
  "v_add_u32 v96, 4096, v240\n" \
  "buffer_store_dwordx4 v[120:123], v96, %[rs], 0 offen sc1\n" \
  "v_cvt_pk_bf16_f32 v102, v28, v29\n" \
  "v_cvt_pk_bf16_f32 v103, v30, v31\n" \
  "v_cvt_pk_bf16_f32 v104, v48, v49\n" \
  "v_cvt_pk_bf16_f32 v105, v50, v51\n" \
  "v_add_u32 v106, 25600, v230\n" \
  "buffer_store_dwordx4 v[102:105], v106, %[rs], 0 offen sc1\n" \
  "v_cvt_pk_bf16_f32 v208, v52, v53\n" \
  "v_cvt_pk_bf16_f32 v209, v54, v55\n" \
  "v_cvt_pk_bf16_f32 v210, v56, v57\n" \
  "v_cvt_pk_bf16_f32 v211, v58, v59\n" \
  "v_cvt_pk_bf16_f32 v212, v60, v61\n" \
  "v_cvt_pk_bf16_f32 v213, v62, v63\n" \
  "v_cndmask_b32 v108, v210, v208, vcc\n" \
  "v_cndmask_b32 v109, v211, v209, vcc\n" \
  "v_cndmask_b32 v110, v212, v210, vcc\n" \
  "v_cndmask_b32 v111, v213, v211, vcc\n" \
  "v_cndmask_b32 v126, v208, v212, vcc\n" \
  "v_cndmask_b32 v127, v209, v213, vcc\n" \
  "v_add_u32 v112, 29696, v231\n" \
  "buffer_store_dwordx4 v[108:111], v112, %[rs], 0 offen sc1\n" \
  "v_add_u32 v114, 5120, v240\n" \
  "buffer_store_dwordx4 v[124:127], v114, %[rs], 0 offen sc1\n" \
  "s_branch 4f\n" \
  "3:\n" \
  "ds_read_b128 v[140:143], v224\n" \
  "ds_read_b64_tr_b16 v[128:129], v228 offset:16384\n" \
  "ds_read_b64_tr_b16 v[130:131], v228 offset:17664\n" \
  "ds_read_b128 v[144:147], v224 offset:4096\n" \
  "ds_read_b64_tr_b16 v[132:133], v228 offset:16448\n" \
  "ds_read_b64_tr_b16 v[134:135], v228 offset:17728\n" \
  "ds_read_b128 v[136:139], v229 offset:36864\n" \
  "s_mov_b32 %[cnt], 8\n" \
  "s_waitcnt lgkmcnt(4)\n" \
  "v_mfma_f32_32x32x16_bf16 v[0:15], v[128:131], v[140:143], v[0:15]\n" \
  "ds_read_b128 v[160:163], v225\n" \
  "ds_read_b64_tr_b16 v[148:149], v228 offset:21504\n" \
  "s_waitcnt lgkmcnt(5)\n" \
  "v_mfma_f32_32x32x16_bf16 v[16:31], v[128:131], v[144:147], v[16:31]\n" \
  "ds_read_b64_tr_b16 v[150:151], v228 offset:22784\n" \
  "ds_read_b128 v[164:167], v225 offset:4096\n" \
  "s_waitcnt lgkmcnt(5)\n" \
  "v_mfma_f32_32x32x16_bf16 v[32:47], v[132:135], v[140:143], v[32:47]\n" \
  "ds_read_b64_tr_b16 v[152:153], v228 offset:21568\n" \
  "ds_read_b64_tr_b16 v[154:155], v228 offset:22848\n" \
  "v_mfma_f32_32x32x16_bf16 v[48:63], v[132:135], v[144:147], v[48:63]\n" \
  "ds_read_b128 v[156:159], v229 offset:36896\n" \
  "s_waitcnt lgkmcnt(7)\n" \
  "v_mfma_f32_32x32x16_bf16 v[64:79], v[136:139], v[140:143], v[64:79]\n" \
  "s_add_u32 %[sl], %[sl], 1\n" \
  "s_cmp_eq_u32 %[sl], 3\n" \
  "s_mov_b32 %[d], 49152\n" \
  "v_mfma_f32_32x32x16_bf16 v[80:95], v[136:139], v[144:147], v[80:95]\n" \
  "s_cselect_b32 %[d], 4294868992, %[d]\n" \
  "s_cselect_b32 %[sl], 0, %[sl]\n" \
  "s_waitcnt lgkmcnt(4)\n" \
  "v_mfma_f32_32x32x16_bf16 v[0:15], v[148:151], v[160:163], v[0:15]\n" \
  "ds_read_b128 v[140:143], v226\n" \
  "ds_read_b64_tr_b16 v[128:129], v228 offset:26624\n" \
  "s_waitcnt lgkmcnt(5)\n" \
  "v_mfma_f32_32x32x16_bf16 v[16:31], v[148:151], v[164:167], v[16:31]\n" \
  "ds_read_b64_tr_b16 v[130:131], v228 offset:27904\n" \
  "ds_read_b128 v[144:147], v226 offset:4096\n" \
  "s_waitcnt lgkmcnt(5)\n" \
  "v_mfma_f32_32x32x16_bf16 v[32:47], v[152:155], v[160:163], v[32:47]\n" \
  "ds_read_b64_tr_b16 v[132:133], v228 offset:26688\n" \
  "ds_read_b64_tr_b16 v[134:135], v228 offset:27968\n" \
  "v_mfma_f32_32x32x16_bf16 v[48:63], v[152:155], v[164:167], v[48:63]\n" \
  "ds_read_b128 v[136:139], v229 offset:36928\n" \
  "s_waitcnt lgkmcnt(7)\n" \
  "v_mfma_f32_32x32x16_bf16 v[64:79], v[156:159], v[160:163], v[64:79]\n" \
  "v_mfma_f32_32x32x16_bf16 v[80:95], v[156:159], v[164:167], v[80:95]\n" \
  "s_waitcnt lgkmcnt(4)\n" \
  "v_mfma_f32_32x32x16_bf16 v[0:15], v[128:131], v[140:143], v[0:15]\n" \
  "ds_read_b128 v[160:163], v227\n" \
  "ds_read_b64_tr_b16 v[148:149], v228 offset:31744\n" \
  "s_waitcnt lgkmcnt(5)\n" \
  "v_mfma_f32_32x32x16_bf16 v[16:31], v[128:131], v[144:147], v[16:31]\n" \
  "ds_read_b64_tr_b16 v[150:151], v228 offset:33024\n" \
  "ds_read_b128 v[164:167], v227 offset:4096\n" \
  "s_waitcnt lgkmcnt(5)\n" \
  "v_mfma_f32_32x32x16_bf16 v[32:47], v[132:135], v[140:143], v[32:47]\n" \
  "ds_read_b64_tr_b16 v[152:153], v228 offset:31808\n" \
  "ds_read_b64_tr_b16 v[154:155], v228 offset:33088\n" \
  "v_mfma_f32_32x32x16_bf16 v[48:63], v[132:135], v[144:147], v[48:63]\n" \
  "ds_read_b128 v[156:159], v229 offset:36960\n" \
  "s_waitcnt lgkmcnt(7)\n" \
  "v_mfma_f32_32x32x16_bf16 v[64:79], v[136:139], v[140:143], v[64:79]\n" \
  "v_mfma_f32_32x32x16_bf16 v[80:95], v[136:139], v[144:147], v[80:95]\n" \
  "s_waitcnt lgkmcnt(0)\n" \
  "12:\n" \
  "s_barrier\n" \
  "v_mfma_f32_32x32x16_bf16 v[0:15], v[148:151], v[160:163], v[0:15]\n" \
  "v_add_u32 v224, %[d], v224\n" \
  "v_add_u32 v228, %[d], v228\n" \
  "ds_read_b128 v[140:143], v224\n" \
  "ds_read_b64_tr_b16 v[128:129], v228 offset:16384\n" \
  "v_mfma_f32_32x32x16_bf16 v[16:31], v[148:151], v[164:167], v[16:31]\n" \
  "v_add_u32 v229, %[d], v229\n" \
  "ds_read_b64_tr_b16 v[130:131], v228 offset:17664\n" \
  "ds_read_b128 v[144:147], v224 offset:4096\n" \
  "v_mfma_f32_32x32x16_bf16 v[32:47], v[152:155], v[160:163], v[32:47]\n" \
  "v_add_u32 v225, %[d], v225\n" \
  "ds_read_b64_tr_b16 v[132:133], v228 offset:16448\n" \
  "ds_read_b64_tr_b16 v[134:135], v228 offset:17728\n" \
  "v_mfma_f32_32x32x16_bf16 v[48:63], v[152:155], v[164:167], v[48:63]\n" \
  "v_add_u32 v226, %[d], v226\n" \
  "ds_read_b128 v[136:139], v229 offset:36864\n" \
  "v_mfma_f32_32x32x16_bf16 v[64:79], v[156:159], v[160:163], v[64:79]\n" \
  "v_add_u32 v227, %[d], v227\n" \
  "v_mfma_f32_32x32x16_bf16 v[80:95], v[156:159], v[164:167], v[80:95]\n" \
  "s_waitcnt lgkmcnt(4)\n" \
  "v_mfma_f32_32x32x16_bf16 v[0:15], v[128:131], v[140:143], v[0:15]\n" \
  "ds_read_b128 v[160:163], v225\n" \
  "ds_read_b64_tr_b16 v[148:149], v228 offset:21504\n" \
  "s_waitcnt lgkmcnt(5)\n" \
  "v_mfma_f32_32x32x16_bf16 v[16:31], v[128:131], v[144:147], v[16:31]\n" \
  "ds_read_b64_tr_b16 v[150:151], v228 offset:22784\n" \
  "ds_read_b128 v[164:167], v225 offset:4096\n" \
  "s_waitcnt lgkmcnt(5)\n" \
  "v_mfma_f32_32x32x16_bf16 v[32:47], v[132:135], v[140:143], v[32:47]\n" \
  "ds_read_b64_tr_b16 v[152:153], v228 offset:21568\n" \
  "ds_read_b64_tr_b16 v[154:155], v228 offset:22848\n" \
  "v_mfma_f32_32x32x16_bf16 v[48:63], v[132:135], v[144:147], v[48:63]\n" \
  "ds_read_b128 v[156:159], v229 offset:36896\n" \
  "s_waitcnt lgkmcnt(7)\n" \
  "v_mfma_f32_32x32x16_bf16 v[64:79], v[136:139], v[140:143], v[64:79]\n" \
  "s_add_u32 %[sl], %[sl], 1\n" \
  "s_cmp_eq_u32 %[sl], 3\n" \
  "s_mov_b32 %[d], 49152\n" \
  "v_mfma_f32_32x32x16_bf16 v[80:95], v[136:139], v[144:147], v[80:95]\n" \
  "s_cselect_b32 %[d], 4294868992, %[d]\n" \
  "s_cselect_b32 %[sl], 0, %[sl]\n" \
  "s_waitcnt lgkmcnt(4)\n" \
  "v_mfma_f32_32x32x16_bf16 v[0:15], v[148:151], v[160:163], v[0:15]\n" \
  "ds_read_b128 v[140:143], v226\n" \
  "ds_read_b64_tr_b16 v[128:129], v228 offset:26624\n" \
  "s_waitcnt lgkmcnt(5)\n" \
  "v_mfma_f32_32x32x16_bf16 v[16:31], v[148:151], v[164:167], v[16:31]\n" \
  "ds_read_b64_tr_b16 v[130:131], v228 offset:27904\n" \
  "ds_read_b128 v[144:147], v226 offset:4096\n" \
  "s_waitcnt lgkmcnt(5)\n" \
  "v_mfma_f32_32x32x16_bf16 v[32:47], v[152:155], v[160:163], v[32:47]\n" \
  "ds_read_b64_tr_b16 v[132:133], v228 offset:26688\n" \
  "ds_read_b64_tr_b16 v[134:135], v228 offset:27968\n" \
  "v_mfma_f32_32x32x16_bf16 v[48:63], v[152:155], v[164:167], v[48:63]\n" \
  "ds_read_b128 v[136:139], v229 offset:36928\n" \
  "s_waitcnt lgkmcnt(7)\n" \
  "v_mfma_f32_32x32x16_bf16 v[64:79], v[156:159], v[160:163], v[64:79]\n" \
  "v_mfma_f32_32x32x16_bf16 v[80:95], v[156:159], v[164:167], v[80:95]\n" \
  "s_waitcnt lgkmcnt(4)\n" \
  "v_mfma_f32_32x32x16_bf16 v[0:15], v[128:131], v[140:143], v[0:15]\n" \
  "ds_read_b128 v[160:163], v227\n" \
  "ds_read_b64_tr_b16 v[148:149], v228 offset:31744\n" \
  "s_waitcnt lgkmcnt(5)\n" \
  "v_mfma_f32_32x32x16_bf16 v[16:31], v[128:131], v[144:147], v[16:31]\n" \
  "ds_read_b64_tr_b16 v[150:151], v228 offset:33024\n" \
  "ds_read_b128 v[164:167], v227 offset:4096\n" \
  "s_waitcnt lgkmcnt(5)\n" \
  "v_mfma_f32_32x32x16_bf16 v[32:47], v[132:135], v[140:143], v[32:47]\n" \
  "ds_read_b64_tr_b16 v[152:153], v228 offset:31808\n" \
  "ds_read_b64_tr_b16 v[154:155], v228 offset:33088\n" \
  "v_mfma_f32_32x32x16_bf16 v[48:63], v[132:135], v[144:147], v[48:63]\n" \
  "ds_read_b128 v[156:159], v229 offset:36960\n" \
  "s_waitcnt lgkmcnt(7)\n" \
  "v_mfma_f32_32x32x16_bf16 v[64:79], v[136:139], v[140:143], v[64:79]\n" \
  "s_sub_u32 %[cnt], %[cnt], 1\n" \
  "s_cmp_lg_u32 %[cnt], 0\n" \
  "v_mfma_f32_32x32x16_bf16 v[80:95], v[136:139], v[144:147], v[80:95]\n" \
  "s_waitcnt vmcnt(0) lgkmcnt(0)\n" \
  "s_cbranch_scc1 12b\n" \
  "s_barrier\n" \
  "v_mfma_f32_32x32x16_bf16 v[0:15], v[148:151], v[160:163], v[0:15]\n" \
  "v_add_u32 v224, %[d], v224\n" \
  "v_add_u32 v228, %[d], v228\n" \
  "ds_read_b128 v[140:143], v224\n" \
  "ds_read_b64_tr_b16 v[128:129], v228 offset:16384\n" \
  "v_mfma_f32_32x32x16_bf16 v[16:31], v[148:151], v[164:167], v[16:31]\n" \
  "v_add_u32 v229, %[d], v229\n" \
  "ds_read_b64_tr_b16 v[130:131], v228 offset:17664\n" \
  "ds_read_b128 v[144:147], v224 offset:4096\n" \
  "v_mfma_f32_32x32x16_bf16 v[32:47], v[152:155], v[160:163], v[32:47]\n" \
  "v_add_u32 v225, %[d], v225\n" \
  "ds_read_b64_tr_b16 v[132:133], v228 offset:16448\n" \
  "ds_read_b64_tr_b16 v[134:135], v228 offset:17728\n" \
  "v_mfma_f32_32x32x16_bf16 v[48:63], v[152:155], v[164:167], v[48:63]\n" \
  "v_add_u32 v226, %[d], v226\n" \
  "ds_read_b128 v[136:139], v229 offset:36864\n" \
  "v_mfma_f32_32x32x16_bf16 v[64:79], v[156:159], v[160:163], v[64:79]\n" \
  "v_add_u32 v227, %[d], v227\n" \
  "v_mfma_f32_32x32x16_bf16 v[80:95], v[156:159], v[164:167], v[80:95]\n" \
  "s_waitcnt lgkmcnt(4)\n" \
  "v_mfma_f32_32x32x16_bf16 v[0:15], v[128:131], v[140:143], v[0:15]\n" \
  "ds_read_b128 v[160:163], v225\n" \
  "ds_read_b128 v[156:159], v229 offset:36896\n" \
  "ds_read_b128 v[164:167], v225 offset:4096\n" \
  "s_waitcnt lgkmcnt(6)\n" \
  "v_mfma_f32_32x32x16_bf16 v[16:31], v[128:131], v[144:147], v[16:31]\n" \
  "ds_read_b128 v[180:183], v226\n" \
  "ds_read_b128 v[176:179], v229 offset:36928\n" \
  "ds_read_b128 v[184:187], v226 offset:4096\n" \
  "s_waitcnt lgkmcnt(7)\n" \
  "v_mfma_f32_32x32x16_bf16 v[32:47], v[132:135], v[140:143], v[32:47]\n" \
  "ds_read_b128 v[200:203], v227\n" \
  "ds_read_b128 v[196:199], v229 offset:36960\n" \
  "ds_read_b128 v[204:207], v227 offset:4096\n" \
  "v_mfma_f32_32x32x16_bf16 v[48:63], v[132:135], v[144:147], v[48:63]\n" \
  "ds_read_b64_tr_b16 v[148:149], v228 offset:21504\n" \
  "ds_read_b64_tr_b16 v[150:151], v228 offset:22784\n" \
  "ds_read_b64_tr_b16 v[168:169], v228 offset:26624\n" \
  "s_waitcnt lgkmcnt(12)\n" \
  "v_mfma_f32_32x32x16_bf16 v[64:79], v[136:139], v[140:143], v[64:79]\n" \
  "ds_read_b64_tr_b16 v[170:171], v228 offset:27904\n" \
  "ds_read_b64_tr_b16 v[188:189], v228 offset:31744\n" \
  "ds_read_b64_tr_b16 v[190:191], v228 offset:33024\n" \
  "v_mfma_f32_32x32x16_bf16 v[80:95], v[136:139], v[144:147], v[80:95]\n" \
  "ds_read_b64_tr_b16 v[152:153], v228 offset:21568\n" \
  "ds_read_b64_tr_b16 v[154:155], v228 offset:22848\n" \
  "ds_read_b64_tr_b16 v[172:173], v228 offset:26688\n" \
  "s_waitcnt lgkmcnt(15)\n" \
  "v_mfma_f32_32x32x16_bf16 v[64:79], v[156:159], v[160:163], v[64:79]\n" \
  "ds_read_b64_tr_b16 v[174:175], v228 offset:27968\n" \
  "ds_read_b64_tr_b16 v[192:193], v228 offset:31808\n" \
  "ds_read_b64_tr_b16 v[194:195], v228 offset:33088\n" \
  "v_mfma_f32_32x32x16_bf16 v[80:95], v[156:159], v[164:167], v[80:95]\n" \
  "s_waitcnt lgkmcnt(15)\n" \
  "v_mfma_f32_32x32x16_bf16 v[64:79], v[176:179], v[180:183], v[64:79]\n" \
  "s_mov_b32 vcc_lo, 0\n" \
  "s_mov_b32 vcc_hi, -1\n" \
  "s_mov_b32 s84, 0xfffff\n" \
  "s_mov_b32 s85, 0xfffff\n" \
  "v_mfma_f32_32x32x16_bf16 v[80:95], v[176:179], v[184:187], v[80:95]\n" \
  "s_waitcnt lgkmcnt(13)\n" \
  "v_mfma_f32_32x32x16_bf16 v[64:79], v[196:199], v[200:203], v[64:79]\n" \
  "s_waitcnt lgkmcnt(12)\n" \
  "v_mfma_f32_32x32x16_bf16 v[80:95], v[196:199], v[204:207], v[80:95]\n" \
  "s_nop 4\n" \
  "s_waitcnt lgkmcnt(10)\n" \
  "v_mfma_f32_32x32x16_bf16 v[0:15], v[148:151], v[160:163], v[0:15]\n" \
  "s_nop 4\n" \
  "v_mfma_f32_32x32x16_bf16 v[16:31], v[148:151], v[164:167], v[16:31]\n" \
  "v_cndmask_b32 v214, v64, v80, vcc\n" \
  "v_mul_f32 v214, 0xbfb8aa3b, v214\n" \
  "v_exp_f32 v214, v214\n" \
  "s_nop 0\n" \
  "v_add_f32 v214, 1.0, v214\n" \
  "s_waitcnt lgkmcnt(8)\n" \
  "v_mfma_f32_32x32x16_bf16 v[0:15], v[168:171], v[180:183], v[0:15]\n" \
  "v_rcp_f32 v214, v214\n" \
  "s_nop 0\n" \
  "buffer_store_dword v214, v241, %[rs], 0 offen sc1\n" \
  "s_nop 1\n" \
  "v_mfma_f32_32x32x16_bf16 v[16:31], v[168:171], v[184:187], v[16:31]\n" \
  "s_nop 4\n" \
  "s_waitcnt lgkmcnt(6)\n" \
  "v_mfma_f32_32x32x16_bf16 v[0:15], v[188:191], v[200:203], v[0:15]\n" \
  "s_nop 4\n" \
  "v_mfma_f32_32x32x16_bf16 v[16:31], v[188:191], v[204:207], v[16:31]\n" \
  "s_nop 4\n" \
  "s_waitcnt lgkmcnt(4)\n" \
  "v_mfma_f32_32x32x16_bf16 v[32:47], v[152:155], v[160:163], v[32:47]\n" \
  "s_nop 4\n" \
  "v_mfma_f32_32x32x16_bf16 v[48:63], v[152:155], v[164:167], v[48:63]\n" \
  "v_cvt_pk_bf16_f32 v208, v0, v1\n" \
  "v_cvt_pk_bf16_f32 v209, v2, v3\n" \
  "v_cvt_pk_bf16_f32 v210, v4, v5\n" \
  "v_cvt_pk_bf16_f32 v211, v6, v7\n" \
  "v_cvt_pk_bf16_f32 v212, v8, v9\n" \
  "s_waitcnt lgkmcnt(2)\n" \
  "v_mfma_f32_32x32x16_bf16 v[32:47], v[172:175], v[180:183], v[32:47]\n" \
  "v_cvt_pk_bf16_f32 v213, v10, v11\n" \
  "v_cndmask_b32 v96, v210, v208, vcc\n" \
  "v_cndmask_b32 v97, v211, v209, vcc\n" \
  "v_cndmask_b32 v98, v212, v210, vcc\n" \
  "v_cndmask_b32 v99, v213, v211, vcc\n" \
  "v_mfma_f32_32x32x16_bf16 v[48:63], v[172:175], v[184:187], v[48:63]\n" \
  "v_cndmask_b32 v120, v208, v212, vcc\n" \
  "v_cndmask_b32 v121, v209, v213, vcc\n" \
  "v_add_u32 v100, 20480, v231\n" \
  "buffer_store_dwordx4 v[96:99], v100, %[rs], 0 offen sc1\n" \
  "v_cvt_pk_bf16_f32 v208, v16, v17\n" \
  "s_waitcnt lgkmcnt(0)\n" \
  "v_mfma_f32_32x32x16_bf16 v[32:47], v[192:195], v[200:203], v[32:47]\n" \
  "v_cvt_pk_bf16_f32 v209, v18, v19\n" \
  "v_cvt_pk_bf16_f32 v210, v20, v21\n" \
  "v_cvt_pk_bf16_f32 v211, v22, v23\n" \
  "v_cvt_pk_bf16_f32 v212, v24, v25\n" \
  "v_cvt_pk_bf16_f32 v213, v26, v27\n" \
  "v_mfma_f32_32x32x16_bf16 v[48:63], v[192:195], v[204:207], v[48:63]\n" \
  "v_cndmask_b32 v102, v210, v208, vcc\n" \
  "v_cndmask_b32 v103, v211, v209, vcc\n" \
  "v_cndmask_b32 v104, v212, v210, vcc\n" \
  "v_cndmask_b32 v105, v213, v211, vcc\n" \
  "v_cndmask_b32 v124, v208, v212, vcc\n" \
  "v_cndmask_b32 v125, v209, v213, vcc\n" \
  "v_add_u32 v106, 21504, v231\n" \
  "buffer_store_dwordx4 v[102:105], v106, %[rs], 0 offen sc1\n" \
  "s_nop 3\n" \
  "v_cvt_pk_bf16_f32 v108, v12, v13\n" \
  "v_cvt_pk_bf16_f32 v109, v14, v15\n" \
  "v_cvt_pk_bf16_f32 v110, v32, v33\n" \
  "v_cvt_pk_bf16_f32 v111, v34, v35\n" \
  "v_add_u32 v112, 24576, v230\n" \
  "buffer_store_dwordx4 v[108:111], v112, %[rs], 0 offen sc1\n" \
  "v_cvt_pk_bf16_f32 v208, v36, v37\n" \
  "v_cvt_pk_bf16_f32 v209, v38, v39\n" \
  "v_cvt_pk_bf16_f32 v210, v40, v41\n" \
  "v_cvt_pk_bf16_f32 v211, v42, v43\n" \
  "v_cvt_pk_bf16_f32 v212, v44, v45\n" \
  "v_cvt_pk_bf16_f32 v213, v46, v47\n" \
  "v_cndmask_b32 v114, v210, v208, vcc\n" \
  "v_cndmask_b32 v115, v211, v209, vcc\n" \
  "v_cndmask_b32 v116, v212, v210, vcc\n" \
  "v_cndmask_b32 v117, v213, v211, vcc\n" \
  "v_cndmask_b32 v122, v208, v212, vcc\n" \
  "v_cndmask_b32 v123, v209, v213, vcc\n" \
  "v_add_u32 v118, 28672, v231\n" \
  "buffer_store_dwordx4 v[114:117], v118, %[rs], 0 offen sc1\n" \
  "v_add_u32 v96, 4096, v240\n" \
  "buffer_store_dwordx4 v[120:123], v96, %[rs], 0 offen sc1\n" \
  "v_cvt_pk_bf16_f32 v102, v28, v29\n" \
  "v_cvt_pk_bf16_f32 v103, v30, v31\n" \
  "v_cvt_pk_bf16_f32 v104, v48, v49\n" \
  "v_cvt_pk_bf16_f32 v105, v50, v51\n" \
  "v_add_u32 v106, 25600, v230\n" \
  "buffer_store_dwordx4 v[102:105], v106, %[rs], 0 offen sc1\n" \
  "v_cvt_pk_bf16_f32 v208, v52, v53\n" \
  "v_cvt_pk_bf16_f32 v209, v54, v55\n" \
  "v_cvt_pk_bf16_f32 v210, v56, v57\n" \
  "v_cvt_pk_bf16_f32 v211, v58, v59\n" \
  "v_cvt_pk_bf16_f32 v212, v60, v61\n" \
  "v_cvt_pk_bf16_f32 v213, v62, v63\n" \
  "v_cndmask_b32 v108, v210, v208, vcc\n" \
  "v_cndmask_b32 v109, v211, v209, vcc\n" \
  "v_cndmask_b32 v110, v212, v210, vcc\n" \
  "v_cndmask_b32 v111, v213, v211, vcc\n" \
  "v_cndmask_b32 v126, v208, v212, vcc\n" \
  "v_cndmask_b32 v127, v209, v213, vcc\n" \
  "v_add_u32 v112, 29696, v231\n" \
  "buffer_store_dwordx4 v[108:111], v112, %[rs], 0 offen sc1\n" \
  "v_add_u32 v114, 5120, v240\n" \
  "buffer_store_dwordx4 v[124:127], v114, %[rs], 0 offen sc1\n" \
  "s_branch 4f\n" \
  "5:\n" \
  "ds_read_b128 v[140:143], v224\n" \
  "ds_read_b64_tr_b16 v[128:129], v228 offset:16384\n" \
  "ds_read_b64_tr_b16 v[130:131], v228 offset:17664\n" \
  "ds_read_b128 v[144:147], v224 offset:4096\n" \
  "ds_read_b64_tr_b16 v[132:133], v228 offset:16448\n" \
  "ds_read_b64_tr_b16 v[134:135], v228 offset:17728\n" \
  "ds_read_b64_tr_b16 v[136:137], v228 offset:16512\n" \
  "ds_read_b64_tr_b16 v[138:139], v228 offset:17792\n" \
  "s_mov_b32 %[cnt], 8\n" \
  "s_waitcnt lgkmcnt(5)\n" \
  "v_mfma_f32_32x32x16_bf16 v[0:15], v[128:131], v[140:143], v[0:15]\n" \
  "ds_read_b128 v[160:163], v225\n" \
  "ds_read_b64_tr_b16 v[148:149], v228 offset:21504\n" \
  "s_waitcnt lgkmcnt(6)\n" \
  "v_mfma_f32_32x32x16_bf16 v[16:31], v[128:131], v[144:147], v[16:31]\n" \
  "ds_read_b64_tr_b16 v[150:151], v228 offset:22784\n" \
  "ds_read_b128 v[164:167], v225 offset:4096\n" \
  "s_waitcnt lgkmcnt(6)\n" \
  "v_mfma_f32_32x32x16_bf16 v[32:47], v[132:135], v[140:143], v[32:47]\n" \
  "ds_read_b64_tr_b16 v[152:153], v228 offset:21568\n" \
  "ds_read_b64_tr_b16 v[154:155], v228 offset:22848\n" \
  "v_mfma_f32_32x32x16_bf16 v[48:63], v[132:135], v[144:147], v[48:63]\n" \
  "ds_read_b64_tr_b16 v[156:157], v228 offset:21632\n" \
  "ds_read_b64_tr_b16 v[158:159], v228 offset:22912\n" \
  "s_waitcnt lgkmcnt(8)\n" \
  "v_mfma_f32_32x32x16_bf16 v[64:79], v[136:139], v[140:143], v[64:79]\n" \
  "s_add_u32 %[sl], %[sl], 1\n" \
  "s_cmp_eq_u32 %[sl], 3\n" \
  "s_mov_b32 %[d], 49152\n" \
  "v_mfma_f32_32x32x16_bf16 v[80:95], v[136:139], v[144:147], v[80:95]\n" \
  "s_cselect_b32 %[d], 4294868992, %[d]\n" \
  "s_cselect_b32 %[sl], 0, %[sl]\n" \
  "s_waitcnt lgkmcnt(5)\n" \
  "v_mfma_f32_32x32x16_bf16 v[0:15], v[148:151], v[160:163], v[0:15]\n" \
  "ds_read_b128 v[140:143], v226\n" \
  "ds_read_b64_tr_b16 v[128:129], v228 offset:26624\n" \
  "s_waitcnt lgkmcnt(6)\n" \
  "v_mfma_f32_32x32x16_bf16 v[16:31], v[148:151], v[164:167], v[16:31]\n" \
  "ds_read_b64_tr_b16 v[130:131], v228 offset:27904\n" \
  "ds_read_b128 v[144:147], v226 offset:4096\n" \
  "s_waitcnt lgkmcnt(6)\n" \
  "v_mfma_f32_32x32x16_bf16 v[32:47], v[152:155], v[160:163], v[32:47]\n" \
  "ds_read_b64_tr_b16 v[132:133], v228 offset:26688\n" \
  "ds_read_b64_tr_b16 v[134:135], v228 offset:27968\n" \
  "v_mfma_f32_32x32x16_bf16 v[48:63], v[152:155], v[164:167], v[48:63]\n" \
  "ds_read_b64_tr_b16 v[136:137], v228 offset:26752\n" \
  "ds_read_b64_tr_b16 v[138:139], v228 offset:28032\n" \
  "s_waitcnt lgkmcnt(8)\n" \
  "v_mfma_f32_32x32x16_bf16 v[64:79], v[156:159], v[160:163], v[64:79]\n" \
  "v_mfma_f32_32x32x16_bf16 v[80:95], v[156:159], v[164:167], v[80:95]\n" \
  "s_waitcnt lgkmcnt(5)\n" \
  "v_mfma_f32_32x32x16_bf16 v[0:15], v[128:131], v[140:143], v[0:15]\n" \
  "ds_read_b128 v[160:163], v227\n" \
  "ds_read_b64_tr_b16 v[148:149], v228 offset:31744\n" \
  "s_waitcnt lgkmcnt(6)\n" \
  "v_mfma_f32_32x32x16_bf16 v[16:31], v[128:131], v[144:147], v[16:31]\n" \
  "ds_read_b64_tr_b16 v[150:151], v228 offset:33024\n" \
  "ds_read_b128 v[164:167], v227 offset:4096\n" \
  "s_waitcnt lgkmcnt(6)\n" \
  "v_mfma_f32_32x32x16_bf16 v[32:47], v[132:135], v[140:143], v[32:47]\n" \
  "ds_read_b64_tr_b16 v[152:153], v228 offset:31808\n" \
  "ds_read_b64_tr_b16 v[154:155], v228 offset:33088\n" \
  "v_mfma_f32_32x32x16_bf16 v[48:63], v[132:135], v[144:147], v[48:63]\n" \
  "ds_read_b64_tr_b16 v[156:157], v228 offset:31872\n" \
  "ds_read_b64_tr_b16 v[158:159], v228 offset:33152\n" \
  "s_waitcnt lgkmcnt(8)\n" \
  "v_mfma_f32_32x32x16_bf16 v[64:79], v[136:139], v[140:143], v[64:79]\n" \
  "v_mfma_f32_32x32x16_bf16 v[80:95], v[136:139], v[144:147], v[80:95]\n" \
  "s_waitcnt lgkmcnt(0)\n" \
  "13:\n" \
  "s_barrier\n" \
  "v_mfma_f32_32x32x16_bf16 v[0:15], v[148:151], v[160:163], v[0:15]\n" \
  "v_add_u32 v224, %[d], v224\n" \
  "v_add_u32 v228, %[d], v228\n" \
  "ds_read_b128 v[140:143], v224\n" \
  "ds_read_b64_tr_b16 v[128:129], v228 offset:16384\n" \
  "v_mfma_f32_32x32x16_bf16 v[16:31], v[148:151], v[164:167], v[16:31]\n" \
  "v_add_u32 v229, %[d], v229\n" \
  "ds_read_b64_tr_b16 v[130:131], v228 offset:17664\n" \
  "ds_read_b128 v[144:147], v224 offset:4096\n" \
  "v_mfma_f32_32x32x16_bf16 v[32:47], v[152:155], v[160:163], v[32:47]\n" \
  "v_add_u32 v225, %[d], v225\n" \
  "ds_read_b64_tr_b16 v[132:133], v228 offset:16448\n" \
  "ds_read_b64_tr_b16 v[134:135], v228 offset:17728\n" \
  "v_mfma_f32_32x32x16_bf16 v[48:63], v[152:155], v[164:167], v[48:63]\n" \
  "v_add_u32 v226, %[d], v226\n" \
  "ds_read_b64_tr_b16 v[136:137], v228 offset:16512\n" \
  "ds_read_b64_tr_b16 v[138:139], v228 offset:17792\n" \
  "v_mfma_f32_32x32x16_bf16 v[64:79], v[156:159], v[160:163], v[64:79]\n" \
  "v_add_u32 v227, %[d], v227\n" \
  "v_mfma_f32_32x32x16_bf16 v[80:95], v[156:159], v[164:167], v[80:95]\n" \
  "s_waitcnt lgkmcnt(5)\n" \
  "v_mfma_f32_32x32x16_bf16 v[0:15], v[128:131], v[140:143], v[0:15]\n" \
  "ds_read_b128 v[160:163], v225\n" \
  "ds_read_b64_tr_b16 v[148:149], v228 offset:21504\n" \
  "s_waitcnt lgkmcnt(6)\n" \
  "v_mfma_f32_32x32x16_bf16 v[16:31], v[128:131], v[144:147], v[16:31]\n" \
  "ds_read_b64_tr_b16 v[150:151], v228 offset:22784\n" \
  "ds_read_b128 v[164:167], v225 offset:4096\n" \
  "s_waitcnt lgkmcnt(6)\n" \
  "v_mfma_f32_32x32x16_bf16 v[32:47], v[132:135], v[140:143], v[32:47]\n" \
  "ds_read_b64_tr_b16 v[152:153], v228 offset:21568\n" \
  "ds_read_b64_tr_b16 v[154:155], v228 offset:22848\n" \
  "v_mfma_f32_32x32x16_bf16 v[48:63], v[132:135], v[144:147], v[48:63]\n" \
  "ds_read_b64_tr_b16 v[156:157], v228 offset:21632\n" \
  "ds_read_b64_tr_b16 v[158:159], v228 offset:22912\n" \
  "s_waitcnt lgkmcnt(8)\n" \
  "v_mfma_f32_32x32x16_bf16 v[64:79], v[136:139], v[140:143], v[64:79]\n" \
  "s_add_u32 %[sl], %[sl], 1\n" \
  "s_cmp_eq_u32 %[sl], 3\n" \
  "s_mov_b32 %[d], 49152\n" \
  "v_mfma_f32_32x32x16_bf16 v[80:95], v[136:139], v[144:147], v[80:95]\n" \
  "s_cselect_b32 %[d], 4294868992, %[d]\n" \
  "s_cselect_b32 %[sl], 0, %[sl]\n" \
  "s_waitcnt lgkmcnt(5)\n" \
  "v_mfma_f32_32x32x16_bf16 v[0:15], v[148:151], v[160:163], v[0:15]\n" \
  "ds_read_b128 v[140:143], v226\n" \
  "ds_read_b64_tr_b16 v[128:129], v228 offset:26624\n" \
  "s_waitcnt lgkmcnt(6)\n" \
  "v_mfma_f32_32x32x16_bf16 v[16:31], v[148:151], v[164:167], v[16:31]\n" \
  "ds_read_b64_tr_b16 v[130:131], v228 offset:27904\n" \
  "ds_read_b128 v[144:147], v226 offset:4096\n" \
  "s_waitcnt lgkmcnt(6)\n" \
  "v_mfma_f32_32x32x16_bf16 v[32:47], v[152:155], v[160:163], v[32:47]\n" \
  "ds_read_b64_tr_b16 v[132:133], v228 offset:26688\n" \
  "ds_read_b64_tr_b16 v[134:135], v228 offset:27968\n" \
  "v_mfma_f32_32x32x16_bf16 v[48:63], v[152:155], v[164:167], v[48:63]\n" \
  "ds_read_b64_tr_b16 v[136:137], v228 offset:26752\n" \
  "ds_read_b64_tr_b16 v[138:139], v228 offset:28032\n" \
  "s_waitcnt lgkmcnt(8)\n" \
  "v_mfma_f32_32x32x16_bf16 v[64:79], v[156:159], v[160:163], v[64:79]\n" \
  "v_mfma_f32_32x32x16_bf16 v[80:95], v[156:159], v[164:167], v[80:95]\n" \
  "s_waitcnt lgkmcnt(5)\n" \
  "v_mfma_f32_32x32x16_bf16 v[0:15], v[128:131], v[140:143], v[0:15]\n" \
  "ds_read_b128 v[160:163], v227\n" \
  "ds_read_b64_tr_b16 v[148:149], v228 offset:31744\n" \
  "s_waitcnt lgkmcnt(6)\n" \
  "v_mfma_f32_32x32x16_bf16 v[16:31], v[128:131], v[144:147], v[16:31]\n" \
  "ds_read_b64_tr_b16 v[150:151], v228 offset:33024\n" \
  "ds_read_b128 v[164:167], v227 offset:4096\n" \
  "s_waitcnt lgkmcnt(6)\n" \
  "v_mfma_f32_32x32x16_bf16 v[32:47], v[132:135], v[140:143], v[32:47]\n" \
  "ds_read_b64_tr_b16 v[152:153], v228 offset:31808\n" \
  "ds_read_b64_tr_b16 v[154:155], v228 offset:33088\n" \
  "v_mfma_f32_32x32x16_bf16 v[48:63], v[132:135], v[144:147], v[48:63]\n" \
  "ds_read_b64_tr_b16 v[156:157], v228 offset:31872\n" \
  "ds_read_b64_tr_b16 v[158:159], v228 offset:33152\n" \
  "s_waitcnt lgkmcnt(8)\n" \
  "v_mfma_f32_32x32x16_bf16 v[64:79], v[136:139], v[140:143], v[64:79]\n" \
  "s_sub_u32 %[cnt], %[cnt], 1\n" \
  "s_cmp_lg_u32 %[cnt], 0\n" \
  "v_mfma_f32_32x32x16_bf16 v[80:95], v[136:139], v[144:147], v[80:95]\n" \
  "s_waitcnt vmcnt(0) lgkmcnt(0)\n" \
  "s_cbranch_scc1 13b\n" \
  "s_barrier\n" \
  "v_mfma_f32_32x32x16_bf16 v[0:15], v[148:151], v[160:163], v[0:15]\n" \
  "v_add_u32 v224, %[d], v224\n" \
  "v_add_u32 v228, %[d], v228\n" \
  "ds_read_b128 v[140:143], v224\n" \
  "ds_read_b64_tr_b16 v[128:129], v228 offset:16384\n" \
  "v_mfma_f32_32x32x16_bf16 v[16:31], v[148:151], v[164:167], v[16:31]\n" \
  "v_add_u32 v229, %[d], v229\n" \
  "ds_read_b64_tr_b16 v[130:131], v228 offset:17664\n" \
  "ds_read_b128 v[144:147], v224 offset:4096\n" \
  "v_mfma_f32_32x32x16_bf16 v[32:47], v[152:155], v[160:163], v[32:47]\n" \
  "v_add_u32 v225, %[d], v225\n" \
  "ds_read_b64_tr_b16 v[132:133], v228 offset:16448\n" \
  "ds_read_b64_tr_b16 v[134:135], v228 offset:17728\n" \
  "v_mfma_f32_32x32x16_bf16 v[48:63], v[152:155], v[164:167], v[48:63]\n" \
  "v_add_u32 v226, %[d], v226\n" \
  "ds_read_b64_tr_b16 v[136:137], v228 offset:16512\n" \
  "ds_read_b64_tr_b16 v[138:139], v228 offset:17792\n" \
  "v_mfma_f32_32x32x16_bf16 v[64:79], v[156:159], v[160:163], v[64:79]\n" \
  "v_add_u32 v227, %[d], v227\n" \
  "v_mfma_f32_32x32x16_bf16 v[80:95], v[156:159], v[164:167], v[80:95]\n" \
  "s_waitcnt lgkmcnt(5)\n" \
  "v_mfma_f32_32x32x16_bf16 v[0:15], v[128:131], v[140:143], v[0:15]\n" \
  "ds_read_b128 v[160:163], v225\n" \
  "ds_read_b64_tr_b16 v[148:149], v228 offset:21504\n" \
  "ds_read_b64_tr_b16 v[150:151], v228 offset:22784\n" \
  "s_waitcnt lgkmcnt(7)\n" \
  "v_mfma_f32_32x32x16_bf16 v[16:31], v[128:131], v[144:147], v[16:31]\n" \
  "ds_read_b128 v[164:167], v225 offset:4096\n" \
  "ds_read_b128 v[180:183], v226\n" \
  "ds_read_b64_tr_b16 v[168:169], v228 offset:26624\n" \
  "s_waitcnt lgkmcnt(8)\n" \
  "v_mfma_f32_32x32x16_bf16 v[32:47], v[132:135], v[140:143], v[32:47]\n" \
  "ds_read_b64_tr_b16 v[170:171], v228 offset:27904\n" \
  "ds_read_b128 v[184:187], v226 offset:4096\n" \
  "ds_read_b128 v[200:203], v227\n" \
  "v_mfma_f32_32x32x16_bf16 v[48:63], v[132:135], v[144:147], v[48:63]\n" \
  "ds_read_b64_tr_b16 v[188:189], v228 offset:31744\n" \
  "ds_read_b64_tr_b16 v[190:191], v228 offset:33024\n" \
  "ds_read_b128 v[204:207], v227 offset:4096\n" \
  "s_waitcnt lgkmcnt(12)\n" \
  "v_mfma_f32_32x32x16_bf16 v[64:79], v[136:139], v[140:143], v[64:79]\n" \
  "ds_read_b64_tr_b16 v[152:153], v228 offset:21568\n" \
  "ds_read_b64_tr_b16 v[154:155], v228 offset:22848\n" \
  "ds_read_b64_tr_b16 v[172:173], v228 offset:26688\n" \
  "v_mfma_f32_32x32x16_bf16 v[80:95], v[136:139], v[144:147], v[80:95]\n" \
  "ds_read_b64_tr_b16 v[174:175], v228 offset:27968\n" \
  "ds_read_b64_tr_b16 v[192:193], v228 offset:31808\n" \
  "ds_read_b64_tr_b16 v[194:195], v228 offset:33088\n" \
  "s_waitcnt lgkmcnt(15)\n" \
  "v_mfma_f32_32x32x16_bf16 v[0:15], v[148:151], v[160:163], v[0:15]\n" \
  "ds_read_b64_tr_b16 v[156:157], v228 offset:21632\n" \
  "ds_read_b64_tr_b16 v[158:159], v228 offset:22912\n" \
  "ds_read_b64_tr_b16 v[176:177], v228 offset:26752\n" \
  "s_waitcnt lgkmcnt(15)\n" \
  "v_mfma_f32_32x32x16_bf16 v[16:31], v[148:151], v[164:167], v[16:31]\n" \
  "ds_read_b64_tr_b16 v[178:179], v228 offset:28032\n" \
  "ds_read_b64_tr_b16 v[196:197], v228 offset:31872\n" \
  "ds_read_b64_tr_b16 v[198:199], v228 offset:33152\n" \
  "s_waitcnt lgkmcnt(15)\n" \
  "v_mfma_f32_32x32x16_bf16 v[0:15], v[168:171], v[180:183], v[0:15]\n" \
  "s_mov_b32 vcc_lo, 0\n" \
  "s_mov_b32 vcc_hi, -1\n" \
  "s_mov_b32 s84, 0xfffff\n" \
  "s_mov_b32 s85, 0xfffff\n" \
  "v_mfma_f32_32x32x16_bf16 v[16:31], v[168:171], v[184:187], v[16:31]\n" \
  "s_waitcnt lgkmcnt(13)\n" \
  "v_mfma_f32_32x32x16_bf16 v[0:15], v[188:191], v[200:203], v[0:15]\n" \
  "s_waitcnt lgkmcnt(12)\n" \
  "v_mfma_f32_32x32x16_bf16 v[16:31], v[188:191], v[204:207], v[16:31]\n" \
  "s_nop 4\n" \
  "s_waitcnt lgkmcnt(10)\n" \
  "v_mfma_f32_32x32x16_bf16 v[32:47], v[152:155], v[160:163], v[32:47]\n" \
  "s_nop 4\n" \
  "v_mfma_f32_32x32x16_bf16 v[48:63], v[152:155], v[164:167], v[48:63]\n" \
  "v_cvt_pk_bf16_f32 v96, v0, v1\n" \
  "v_cvt_pk_bf16_f32 v97, v2, v3\n" \
  "v_cvt_pk_bf16_f32 v98, v4, v5\n" \
  "v_cvt_pk_bf16_f32 v99, v6, v7\n" \
  "v_add_u32 v100, 0, v230\n" \
  "s_waitcnt lgkmcnt(8)\n" \
  "v_mfma_f32_32x32x16_bf16 v[32:47], v[172:175], v[180:183], v[32:47]\n" \
  "buffer_store_dwordx4 v[96:99], v100, %[rs], 0 offen sc1\n" \
  "v_cvt_pk_bf16_f32 v102, v16, v17\n" \
  "v_cvt_pk_bf16_f32 v103, v18, v19\n" \
  "v_cvt_pk_bf16_f32 v104, v20, v21\n" \
  "v_cvt_pk_bf16_f32 v105, v22, v23\n" \
  "v_mfma_f32_32x32x16_bf16 v[48:63], v[172:175], v[184:187], v[48:63]\n" \
  "v_add_u32 v106, 1024, v230\n" \
  "buffer_store_dwordx4 v[102:105], v106, %[rs], 0 offen sc1\n" \
  "s_nop 2\n" \
  "s_waitcnt lgkmcnt(6)\n" \
  "v_mfma_f32_32x32x16_bf16 v[32:47], v[192:195], v[200:203], v[32:47]\n" \
  "s_nop 4\n" \
  "v_mfma_f32_32x32x16_bf16 v[48:63], v[192:195], v[204:207], v[48:63]\n" \
  "s_nop 4\n" \
  "s_waitcnt lgkmcnt(4)\n" \
  "v_mfma_f32_32x32x16_bf16 v[64:79], v[156:159], v[160:163], v[64:79]\n" \
  "s_nop 4\n" \
  "v_mfma_f32_32x32x16_bf16 v[80:95], v[156:159], v[164:167], v[80:95]\n" \
  "v_cvt_pk_bf16_f32 v208, v8, v9\n" \
  "v_cvt_pk_bf16_f32 v209, v10, v11\n" \
  "v_cvt_pk_bf16_f32 v210, v12, v13\n" \
  "v_cvt_pk_bf16_f32 v211, v14, v15\n" \
  "v_cvt_pk_bf16_f32 v212, v32, v33\n" \
  "s_waitcnt lgkmcnt(2)\n" \
  "v_mfma_f32_32x32x16_bf16 v[64:79], v[176:179], v[180:183], v[64:79]\n" \
  "v_cvt_pk_bf16_f32 v213, v34, v35\n" \
  "v_cndmask_b32 v108, v210, v208, vcc\n" \
  "v_cndmask_b32 v109, v211, v209, vcc\n" \
  "v_cndmask_b32 v110, v212, v210, vcc\n" \
  "v_cndmask_b32 v111, v213, v211, vcc\n" \
  "v_mfma_f32_32x32x16_bf16 v[80:95], v[176:179], v[184:187], v[80:95]\n" \
  "v_cndmask_b32 v120, v208, v212, vcc\n" \
  "v_cndmask_b32 v121, v209, v213, vcc\n" \
  "v_add_u32 v112, 4096, v231\n" \
  "buffer_store_dwordx4 v[108:111], v112, %[rs], 0 offen sc1\n" \
  "v_cvt_pk_bf16_f32 v114, v36, v37\n" \
  "s_waitcnt lgkmcnt(0)\n" \
  "v_mfma_f32_32x32x16_bf16 v[64:79], v[196:199], v[200:203], v[64:79]\n" \
  "v_cvt_pk_bf16_f32 v115, v38, v39\n" \
  "v_cvt_pk_bf16_f32 v116, v40, v41\n" \
  "v_cvt_pk_bf16_f32 v117, v42, v43\n" \
  "v_add_u32 v118, 8192, v230\n" \
  "buffer_store_dwordx4 v[114:117], v118, %[rs], 0 offen sc1\n" \
  "v_mfma_f32_32x32x16_bf16 v[80:95], v[196:199], v[204:207], v[80:95]\n" \
  "v_cvt_pk_bf16_f32 v208, v24, v25\n" \
  "v_cvt_pk_bf16_f32 v209, v26, v27\n" \
  "v_cvt_pk_bf16_f32 v210, v28, v29\n" \
  "v_cvt_pk_bf16_f32 v211, v30, v31\n" \
  "v_cvt_pk_bf16_f32 v212, v48, v49\n" \
  "v_cvt_pk_bf16_f32 v213, v50, v51\n" \
  "v_cndmask_b32 v96, v210, v208, vcc\n" \
  "v_cndmask_b32 v97, v211, v209, vcc\n" \
  "v_cndmask_b32 v98, v212, v210, vcc\n" \
  "v_cndmask_b32 v99, v213, v211, vcc\n" \
  "v_cndmask_b32 v124, v208, v212, vcc\n" \
  "v_cndmask_b32 v125, v209, v213, vcc\n" \
  "v_add_u32 v100, 5120, v231\n" \
  "buffer_store_dwordx4 v[96:99], v100, %[rs], 0 offen sc1\n" \
  "v_cvt_pk_bf16_f32 v102, v52, v53\n" \
  "v_cvt_pk_bf16_f32 v103, v54, v55\n" \
  "v_cvt_pk_bf16_f32 v104, v56, v57\n" \
  "v_cvt_pk_bf16_f32 v105, v58, v59\n" \
  "v_add_u32 v106, 9216, v230\n" \
  "buffer_store_dwordx4 v[102:105], v106, %[rs], 0 offen sc1\n" \
  "v_cvt_pk_bf16_f32 v208, v44, v45\n" \
  "v_cvt_pk_bf16_f32 v209, v46, v47\n" \
  "v_cvt_pk_bf16_f32 v210, v64, v65\n" \
  "v_cvt_pk_bf16_f32 v211, v66, v67\n" \
  "v_cvt_pk_bf16_f32 v212, v68, v69\n" \
  "v_cvt_pk_bf16_f32 v213, v70, v71\n" \
  "v_cndmask_b32 v108, v210, v208, vcc\n" \
  "v_cndmask_b32 v109, v211, v209, vcc\n" \
  "v_cndmask_b32 v110, v212, v210, vcc\n" \
  "v_cndmask_b32 v111, v213, v211, vcc\n" \
  "v_cndmask_b32 v122, v208, v212, vcc\n" \
  "v_cndmask_b32 v123, v209, v213, vcc\n" \
  "v_add_u32 v112, 12288, v231\n" \
  "buffer_store_dwordx4 v[108:111], v112, %[rs], 0 offen sc1\n" \
  "v_cvt_pk_bf16_f32 v114, v72, v73\n" \
  "v_cvt_pk_bf16_f32 v115, v74, v75\n" \
  "v_cvt_pk_bf16_f32 v116, v76, v77\n" \
  "v_cvt_pk_bf16_f32 v117, v78, v79\n" \
  "v_add_u32 v118, 16384, v230\n" \
  "buffer_store_dwordx4 v[114:117], v118, %[rs], 0 offen sc1\n" \
  "v_add_u32 v96, 0, v240\n" \
  "buffer_store_dwordx4 v[120:123], v96, %[rs], 0 offen sc1\n" \
  "v_cvt_pk_bf16_f32 v208, v60, v61\n" \
  "v_cvt_pk_bf16_f32 v209, v62, v63\n" \
  "v_cvt_pk_bf16_f32 v210, v80, v81\n" \
  "v_cvt_pk_bf16_f32 v211, v82, v83\n" \
  "v_cvt_pk_bf16_f32 v212, v84, v85\n" \
  "v_cvt_pk_bf16_f32 v213, v86, v87\n" \
  "v_cndmask_b32 v102, v210, v208, vcc\n" \
  "v_cndmask_b32 v103, v211, v209, vcc\n" \
  "v_cndmask_b32 v104, v212, v210, vcc\n" \
  "v_cndmask_b32 v105, v213, v211, vcc\n" \
  "v_cndmask_b32 v126, v208, v212, vcc\n" \
  "v_cndmask_b32 v127, v209, v213, vcc\n" \
  "v_add_u32 v106, 13312, v231\n" \
  "buffer_store_dwordx4 v[102:105], v106, %[rs], 0 offen sc1\n" \
  "v_cvt_pk_bf16_f32 v108, v88, v89\n" \
  "v_cvt_pk_bf16_f32 v109, v90, v91\n" \
  "v_cvt_pk_bf16_f32 v110, v92, v93\n" \
  "v_cvt_pk_bf16_f32 v111, v94, v95\n" \
  "v_add_u32 v112, 17408, v230\n" \
  "buffer_store_dwordx4 v[108:111], v112, %[rs], 0 offen sc1\n" \
  "v_add_u32 v114, 1024, v240\n" \
  "buffer_store_dwordx4 v[124:127], v114, %[rs], 0 offen sc1\n" \
  "4:\n" \
  "s_barrier\n"
#define GEMMD_CLOB "v0", "v1", "v2", "v3", "v4", "v5", "v6", "v7", "v8", "v9", "v10", "v11", "v12", "v13", "v14", "v15", "v16", "v17", "v18", "v19", "v20", "v21", "v22", "v23", "v24", "v25", "v26", "v27", "v28", "v29", "v30", "v31", "v32", "v33", "v34", "v35", "v36", "v37", "v38", "v39", "v40", "v41", "v42", "v43", "v44", "v45", "v46", "v47", "v48", "v49", "v50", "v51", "v52", "v53", "v54", "v55", "v56", "v57", "v58", "v59", "v60", "v61", "v62", "v63", "v64", "v65", "v66", "v67", "v68", "v69", "v70", "v71", "v72", "v73", "v74", "v75", "v76", "v77", "v78", "v79", "v80", "v81", "v82", "v83", "v84", "v85", "v86", "v87", "v88", "v89", "v90", "v91", "v92", "v93", "v94", "v95", "v96", "v97", "v98", "v99", "v100", "v101", "v102", "v103", "v104", "v105", "v106", "v107", "v108", "v109", "v110", "v111", "v112", "v113", "v114", "v115", "v116", "v117", "v118", "v119", "v120", "v121", "v122", "v123", "v124", "v125", "v126", "v127", "v128", "v129", "v130", "v131", "v132", "v133", "v134", "v135", "v136", "v137", "v138", "v139", "v140", "v141", "v142", "v143", "v144", "v145", "v146", "v147", "v148", "v149", "v150", "v151", "v152", "v153", "v154", "v155", "v156", "v157", "v158", "v159", "v160", "v161", "v162", "v163", "v164", "v165", "v166", "v167", "v168", "v169", "v170", "v171", "v172", "v173", "v174", "v175", "v176", "v177", "v178", "v179", "v180", "v181", "v182", "v183", "v184", "v185", "v186", "v187", "v188", "v189", "v190", "v191", "v192", "v193", "v194", "v195", "v196", "v197", "v198", "v199", "v200", "v201", "v202", "v203", "v204", "v205", "v206", "v207", "v208", "v209", "v210", "v211", "v212", "v213", "v214", "v215", "v216", "v217", "v218", "v219", "v220", "v221", "v222", "v223", "v235"
#define HEPI_ASM \
  "v_pk_mul_f32 v[128:129], v[0:1], %[c]\n" \
  "v_pk_mul_f32 v[130:131], v[2:3], %[c]\n" \
  "v_pk_mul_f32 v[132:133], v[32:33], %[c]\n" \
  "v_pk_mul_f32 v[134:135], v[34:35], %[c]\n" \
  "v_exp_f32 v128, v128\n" \
  "v_exp_f32 v129, v129\n" \
  "v_exp_f32 v130, v130\n" \
  "v_exp_f32 v131, v131\n" \
  "v_exp_f32 v132, v132\n" \
  "v_exp_f32 v133, v133\n" \
  "v_exp_f32 v134, v134\n" \
  "v_exp_f32 v135, v135\n" \
  "v_pk_mul_f32 v[0:1], v[0:1], v[16:17]\n" \
  "v_pk_mul_f32 v[2:3], v[2:3], v[18:19]\n" \
  "v_pk_mul_f32 v[32:33], v[32:33], v[48:49]\n" \
  "v_pk_mul_f32 v[34:35], v[34:35], v[50:51]\n" \
  "v_pk_add_f32 v[128:129], v[128:129], %[one]\n" \
  "v_pk_add_f32 v[130:131], v[130:131], %[one]\n" \
  "v_pk_add_f32 v[132:133], v[132:133], %[one]\n" \
  "v_pk_add_f32 v[134:135], v[134:135], %[one]\n" \
  "v_rcp_f32 v128, v128\n" \
  "v_rcp_f32 v129, v129\n" \
  "v_rcp_f32 v130, v130\n" \
  "v_rcp_f32 v131, v131\n" \
  "v_rcp_f32 v132, v132\n" \
  "v_rcp_f32 v133, v133\n" \
  "v_rcp_f32 v134, v134\n" \
  "v_rcp_f32 v135, v135\n" \
  "s_nop 0\n" \
  "v_pk_mul_f32 v[0:1], v[0:1], v[128:129]\n" \
  "v_pk_mul_f32 v[2:3], v[2:3], v[130:131]\n" \
  "v_pk_mul_f32 v[32:33], v[32:33], v[132:133]\n" \
  "v_pk_mul_f32 v[34:35], v[34:35], v[134:135]\n" \
  "v_cvt_pk_bf16_f32 v144, v0, v1\n" \
  "v_cvt_pk_bf16_f32 v145, v2, v3\n" \
  "v_cvt_pk_bf16_f32 v146, v32, v33\n" \
  "v_cvt_pk_bf16_f32 v147, v34, v35\n" \
  "ds_write_b128 %[vo], v[144:147] offset:0\n" \
  "v_pk_mul_f32 v[136:137], v[4:5], %[c]\n" \
  "v_pk_mul_f32 v[138:139], v[6:7], %[c]\n" \
  "v_pk_mul_f32 v[140:141], v[36:37], %[c]\n" \
  "v_pk_mul_f32 v[142:143], v[38:39], %[c]\n" \
  "v_exp_f32 v136, v136\n" \
  "v_exp_f32 v137, v137\n" \
  "v_exp_f32 v138, v138\n" \
  "v_exp_f32 v139, v139\n" \
  "v_exp_f32 v140, v140\n" \
  "v_exp_f32 v141, v141\n" \
  "v_exp_f32 v142, v142\n" \
  "v_exp_f32 v143, v143\n" \
  "v_pk_mul_f32 v[4:5], v[4:5], v[20:21]\n" \
  "v_pk_mul_f32 v[6:7], v[6:7], v[22:23]\n" \
  "v_pk_mul_f32 v[36:37], v[36:37], v[52:53]\n" \
  "v_pk_mul_f32 v[38:39], v[38:39], v[54:55]\n" \
  "v_pk_add_f32 v[136:137], v[136:137], %[one]\n" \
  "v_pk_add_f32 v[138:139], v[138:139], %[one]\n" \
  "v_pk_add_f32 v[140:141], v[140:141], %[one]\n" \
  "v_pk_add_f32 v[142:143], v[142:143], %[one]\n" \
  "v_rcp_f32 v136, v136\n" \
  "v_rcp_f32 v137, v137\n" \
  "v_rcp_f32 v138, v138\n" \
  "v_rcp_f32 v139, v139\n" \
  "v_rcp_f32 v140, v140\n" \
  "v_rcp_f32 v141, v141\n" \
  "v_rcp_f32 v142, v142\n" \
  "v_rcp_f32 v143, v143\n" \
  "s_nop 0\n" \
  "v_pk_mul_f32 v[4:5], v[4:5], v[136:137]\n" \
  "v_pk_mul_f32 v[6:7], v[6:7], v[138:139]\n" \
  "v_pk_mul_f32 v[36:37], v[36:37], v[140:141]\n" \
  "v_pk_mul_f32 v[38:39], v[38:39], v[142:143]\n" \
  "v_cvt_pk_bf16_f32 v148, v4, v5\n" \
  "v_cvt_pk_bf16_f32 v149, v6, v7\n" \
  "v_cvt_pk_bf16_f32 v150, v36, v37\n" \
  "v_cvt_pk_bf16_f32 v151, v38, v39\n" \
  "ds_write_b128 %[vo], v[148:151] offset:1024\n" \
  "v_pk_mul_f32 v[128:129], v[8:9], %[c]\n" \
  "v_pk_mul_f32 v[130:131], v[10:11], %[c]\n" \
  "v_pk_mul_f32 v[132:133], v[40:41], %[c]\n" \
  "v_pk_mul_f32 v[134:135], v[42:43], %[c]\n" \
  "v_exp_f32 v128, v128\n" \
  "v_exp_f32 v129, v129\n" \
  "v_exp_f32 v130, v130\n" \
  "v_exp_f32 v131, v131\n" \
  "v_exp_f32 v132, v132\n" \
  "v_exp_f32 v133, v133\n" \
  "v_exp_f32 v134, v134\n" \
  "v_exp_f32 v135, v135\n" \
  "v_pk_mul_f32 v[8:9], v[8:9], v[24:25]\n" \
  "v_pk_mul_f32 v[10:11], v[10:11], v[26:27]\n" \
  "v_pk_mul_f32 v[40:41], v[40:41], v[56:57]\n" \
  "v_pk_mul_f32 v[42:43], v[42:43], v[58:59]\n" \
  "v_pk_add_f32 v[128:129], v[128:129], %[one]\n" \
  "v_pk_add_f32 v[130:131], v[130:131], %[one]\n" \
  "v_pk_add_f32 v[132:133], v[132:133], %[one]\n" \
  "v_pk_add_f32 v[134:135], v[134:135], %[one]\n" \
  "v_rcp_f32 v128, v128\n" \
  "v_rcp_f32 v129, v129\n" \
  "v_rcp_f32 v130, v130\n" \
  "v_rcp_f32 v131, v131\n" \
  "v_rcp_f32 v132, v132\n" \
  "v_rcp_f32 v133, v133\n" \
  "v_rcp_f32 v134, v134\n" \
  "v_rcp_f32 v135, v135\n" \
  "s_nop 0\n" \
  "v_pk_mul_f32 v[8:9], v[8:9], v[128:129]\n" \
  "v_pk_mul_f32 v[10:11], v[10:11], v[130:131]\n" \
  "v_pk_mul_f32 v[40:41], v[40:41], v[132:133]\n" \
  "v_pk_mul_f32 v[42:43], v[42:43], v[134:135]\n" \
  "v_cvt_pk_bf16_f32 v144, v8, v9\n" \
  "v_cvt_pk_bf16_f32 v145, v10, v11\n" \
  "v_cvt_pk_bf16_f32 v146, v40, v41\n" \
  "v_cvt_pk_bf16_f32 v147, v42, v43\n" \
  "ds_write_b128 %[vo], v[144:147] offset:2048\n" \
  "v_pk_mul_f32 v[136:137], v[12:13], %[c]\n" \
  "v_pk_mul_f32 v[138:139], v[14:15], %[c]\n" \
  "v_pk_mul_f32 v[140:141], v[44:45], %[c]\n" \
  "v_pk_mul_f32 v[142:143], v[46:47], %[c]\n" \
  "v_exp_f32 v136, v136\n" \
  "v_exp_f32 v137, v137\n" \
  "v_exp_f32 v138, v138\n" \
  "v_exp_f32 v139, v139\n" \
  "v_exp_f32 v140, v140\n" \
  "v_exp_f32 v141, v141\n" \
  "v_exp_f32 v142, v142\n" \
  "v_exp_f32 v143, v143\n" \
  "v_pk_mul_f32 v[12:13], v[12:13], v[28:29]\n" \
  "v_pk_mul_f32 v[14:15], v[14:15], v[30:31]\n" \
  "v_pk_mul_f32 v[44:45], v[44:45], v[60:61]\n" \
  "v_pk_mul_f32 v[46:47], v[46:47], v[62:63]\n" \
  "v_pk_add_f32 v[136:137], v[136:137], %[one]\n" \
  "v_pk_add_f32 v[138:139], v[138:139], %[one]\n" \
  "v_pk_add_f32 v[140:141], v[140:141], %[one]\n" \
  "v_pk_add_f32 v[142:143], v[142:143], %[one]\n" \
  "v_rcp_f32 v136, v136\n" \
  "v_rcp_f32 v137, v137\n" \
  "v_rcp_f32 v138, v138\n" \
  "v_rcp_f32 v139, v139\n" \
  "v_rcp_f32 v140, v140\n" \
  "v_rcp_f32 v141, v141\n" \
  "v_rcp_f32 v142, v142\n" \
  "v_rcp_f32 v143, v143\n" \
  "s_nop 0\n" \
  "v_pk_mul_f32 v[12:13], v[12:13], v[136:137]\n" \
  "v_pk_mul_f32 v[14:15], v[14:15], v[138:139]\n" \
  "v_pk_mul_f32 v[44:45], v[44:45], v[140:141]\n" \
  "v_pk_mul_f32 v[46:47], v[46:47], v[142:143]\n" \
  "v_cvt_pk_bf16_f32 v148, v12, v13\n" \
  "v_cvt_pk_bf16_f32 v149, v14, v15\n" \
  "v_cvt_pk_bf16_f32 v150, v44, v45\n" \
  "v_cvt_pk_bf16_f32 v151, v46, v47\n" \
  "ds_write_b128 %[vo], v[148:151] offset:3072\n" \
  "v_pk_mul_f32 v[128:129], v[64:65], %[c]\n" \
  "v_pk_mul_f32 v[130:131], v[66:67], %[c]\n" \
  "v_pk_mul_f32 v[132:133], v[96:97], %[c]\n" \
  "v_pk_mul_f32 v[134:135], v[98:99], %[c]\n" \
  "v_exp_f32 v128, v128\n" \
  "v_exp_f32 v129, v129\n" \
  "v_exp_f32 v130, v130\n" \
  "v_exp_f32 v131, v131\n" \
  "v_exp_f32 v132, v132\n" \
  "v_exp_f32 v133, v133\n" \
  "v_exp_f32 v134, v134\n" \
  "v_exp_f32 v135, v135\n" \
  "v_pk_mul_f32 v[64:65], v[64:65], v[80:81]\n" \
  "v_pk_mul_f32 v[66:67], v[66:67], v[82:83]\n" \
  "v_pk_mul_f32 v[96:97], v[96:97], v[112:113]\n" \
  "v_pk_mul_f32 v[98:99], v[98:99], v[114:115]\n" \
  "v_pk_add_f32 v[128:129], v[128:129], %[one]\n" \
  "v_pk_add_f32 v[130:131], v[130:131], %[one]\n" \
  "v_pk_add_f32 v[132:133], v[132:133], %[one]\n" \
  "v_pk_add_f32 v[134:135], v[134:135], %[one]\n" \
  "v_rcp_f32 v128, v128\n" \
  "v_rcp_f32 v129, v129\n" \
  "v_rcp_f32 v130, v130\n" \
  "v_rcp_f32 v131, v131\n" \
  "v_rcp_f32 v132, v132\n" \
  "v_rcp_f32 v133, v133\n" \
  "v_rcp_f32 v134, v134\n" \
  "v_rcp_f32 v135, v135\n" \
  "s_nop 0\n" \
  "v_pk_mul_f32 v[64:65], v[64:65], v[128:129]\n" \
  "v_pk_mul_f32 v[66:67], v[66:67], v[130:131]\n" \
  "v_pk_mul_f32 v[96:97], v[96:97], v[132:133]\n" \
  "v_pk_mul_f32 v[98:99], v[98:99], v[134:135]\n" \
  "v_cvt_pk_bf16_f32 v144, v64, v65\n" \
  "v_cvt_pk_bf16_f32 v145, v66, v67\n" \
  "v_cvt_pk_bf16_f32 v146, v96, v97\n" \
  "v_cvt_pk_bf16_f32 v147, v98, v99\n" \
  "ds_write_b128 %[vo], v[144:147] offset:4096\n" \
  "v_pk_mul_f32 v[136:137], v[68:69], %[c]\n" \
  "v_pk_mul_f32 v[138:139], v[70:71], %[c]\n" \
  "v_pk_mul_f32 v[140:141], v[100:101], %[c]\n" \
  "v_pk_mul_f32 v[142:143], v[102:103], %[c]\n" \
  "v_exp_f32 v136, v136\n" \
  "v_exp_f32 v137, v137\n" \
  "v_exp_f32 v138, v138\n" \
  "v_exp_f32 v139, v139\n" \
  "v_exp_f32 v140, v140\n" \
  "v_exp_f32 v141, v141\n" \
  "v_exp_f32 v142, v142\n" \
  "v_exp_f32 v143, v143\n" \
  "v_pk_mul_f32 v[68:69], v[68:69], v[84:85]\n" \
  "v_pk_mul_f32 v[70:71], v[70:71], v[86:87]\n" \
  "v_pk_mul_f32 v[100:101], v[100:101], v[116:117]\n" \
  "v_pk_mul_f32 v[102:103], v[102:103], v[118:119]\n" \
  "v_pk_add_f32 v[136:137], v[136:137], %[one]\n" \
  "v_pk_add_f32 v[138:139], v[138:139], %[one]\n" \
  "v_pk_add_f32 v[140:141], v[140:141], %[one]\n" \
  "v_pk_add_f32 v[142:143], v[142:143], %[one]\n" \
  "v_rcp_f32 v136, v136\n" \
  "v_rcp_f32 v137, v137\n" \
  "v_rcp_f32 v138, v138\n" \
  "v_rcp_f32 v139, v139\n" \
  "v_rcp_f32 v140, v140\n" \
  "v_rcp_f32 v141, v141\n" \
  "v_rcp_f32 v142, v142\n" \
  "v_rcp_f32 v143, v143\n" \
  "s_nop 0\n" \
  "v_pk_mul_f32 v[68:69], v[68:69], v[136:137]\n" \
  "v_pk_mul_f32 v[70:71], v[70:71], v[138:139]\n" \
  "v_pk_mul_f32 v[100:101], v[100:101], v[140:141]\n" \
  "v_pk_mul_f32 v[102:103], v[102:103], v[142:143]\n" \
  "v_cvt_pk_bf16_f32 v148, v68, v69\n" \
  "v_cvt_pk_bf16_f32 v149, v70, v71\n" \
  "v_cvt_pk_bf16_f32 v150, v100, v101\n" \
  "v_cvt_pk_bf16_f32 v151, v102, v103\n" \
  "ds_write_b128 %[vo], v[148:151] offset:5120\n" \
  "v_pk_mul_f32 v[128:129], v[72:73], %[c]\n" \
  "v_pk_mul_f32 v[130:131], v[74:75], %[c]\n" \
  "v_pk_mul_f32 v[132:133], v[104:105], %[c]\n" \
  "v_pk_mul_f32 v[134:135], v[106:107], %[c]\n" \
  "v_exp_f32 v128, v128\n" \
  "v_exp_f32 v129, v129\n" \
  "v_exp_f32 v130, v130\n" \
  "v_exp_f32 v131, v131\n" \
  "v_exp_f32 v132, v132\n" \
  "v_exp_f32 v133, v133\n" \
  "v_exp_f32 v134, v134\n" \
  "v_exp_f32 v135, v135\n" \
  "v_pk_mul_f32 v[72:73], v[72:73], v[88:89]\n" \
  "v_pk_mul_f32 v[74:75], v[74:75], v[90:91]\n" \
  "v_pk_mul_f32 v[104:105], v[104:105], v[120:121]\n" \
  "v_pk_mul_f32 v[106:107], v[106:107], v[122:123]\n" \
  "v_pk_add_f32 v[128:129], v[128:129], %[one]\n" \
  "v_pk_add_f32 v[130:131], v[130:131], %[one]\n" \
  "v_pk_add_f32 v[132:133], v[132:133], %[one]\n" \
  "v_pk_add_f32 v[134:135], v[134:135], %[one]\n" \
  "v_rcp_f32 v128, v128\n" \
  "v_rcp_f32 v129, v129\n" \
  "v_rcp_f32 v130, v130\n" \
  "v_rcp_f32 v131, v131\n" \
  "v_rcp_f32 v132, v132\n" \
  "v_rcp_f32 v133, v133\n" \
  "v_rcp_f32 v134, v134\n" \
  "v_rcp_f32 v135, v135\n" \
  "s_nop 0\n" \
  "v_pk_mul_f32 v[72:73], v[72:73], v[128:129]\n" \
  "v_pk_mul_f32 v[74:75], v[74:75], v[130:131]\n" \
  "v_pk_mul_f32 v[104:105], v[104:105], v[132:133]\n" \
  "v_pk_mul_f32 v[106:107], v[106:107], v[134:135]\n" \
  "v_cvt_pk_bf16_f32 v144, v72, v73\n" \
  "v_cvt_pk_bf16_f32 v145, v74, v75\n" \
  "v_cvt_pk_bf16_f32 v146, v104, v105\n" \
  "v_cvt_pk_bf16_f32 v147, v106, v107\n" \
  "ds_write_b128 %[vo], v[144:147] offset:6144\n" \
  "v_pk_mul_f32 v[136:137], v[76:77], %[c]\n" \
  "v_pk_mul_f32 v[138:139], v[78:79], %[c]\n" \
  "v_pk_mul_f32 v[140:141], v[108:109], %[c]\n" \
  "v_pk_mul_f32 v[142:143], v[110:111], %[c]\n" \
  "v_exp_f32 v136, v136\n" \
  "v_exp_f32 v137, v137\n" \
  "v_exp_f32 v138, v138\n" \
  "v_exp_f32 v139, v139\n" \
  "v_exp_f32 v140, v140\n" \
  "v_exp_f32 v141, v141\n" \
  "v_exp_f32 v142, v142\n" \
  "v_exp_f32 v143, v143\n" \
  "v_pk_mul_f32 v[76:77], v[76:77], v[92:93]\n" \
  "v_pk_mul_f32 v[78:79], v[78:79], v[94:95]\n" \
  "v_pk_mul_f32 v[108:109], v[108:109], v[124:125]\n" \
  "v_pk_mul_f32 v[110:111], v[110:111], v[126:127]\n" \
  "v_pk_add_f32 v[136:137], v[136:137], %[one]\n" \
  "v_pk_add_f32 v[138:139], v[138:139], %[one]\n" \
  "v_pk_add_f32 v[140:141], v[140:141], %[one]\n" \
  "v_pk_add_f32 v[142:143], v[142:143], %[one]\n" \
  "v_rcp_f32 v136, v136\n" \
  "v_rcp_f32 v137, v137\n" \
  "v_rcp_f32 v138, v138\n" \
  "v_rcp_f32 v139, v139\n" \
  "v_rcp_f32 v140, v140\n" \
  "v_rcp_f32 v141, v141\n" \
  "v_rcp_f32 v142, v142\n" \
  "v_rcp_f32 v143, v143\n" \
  "s_nop 0\n" \
  "v_pk_mul_f32 v[76:77], v[76:77], v[136:137]\n" \
  "v_pk_mul_f32 v[78:79], v[78:79], v[138:139]\n" \
  "v_pk_mul_f32 v[108:109], v[108:109], v[140:141]\n" \
  "v_pk_mul_f32 v[110:111], v[110:111], v[142:143]\n" \
  "v_cvt_pk_bf16_f32 v148, v76, v77\n" \
  "v_cvt_pk_bf16_f32 v149, v78, v79\n" \
  "v_cvt_pk_bf16_f32 v150, v108, v109\n" \
  "v_cvt_pk_bf16_f32 v151, v110, v111\n" \
  "ds_write_b128 %[vo], v[148:151] offset:7168\n" \
  "s_waitcnt vmcnt(0)\n"
#define HEPI_CLOB "v128", "v129", "v130", "v131", "v132", "v133", "v134", "v135", "v136", "v137", "v138", "v139", "v140", "v141", "v142", "v143", "v144", "v145", "v146", "v147", "v148", "v149", "v150", "v151", "v235"
#define ATTN_ASM \
  "s_mov_b32 %[d], 28672\n" \
  "s_mov_b32 %[cnt], 4194304\n" \
  "20:\n" \
  "ds_read_b32 v46, v176\n" \
  "ds_read_b128 v[32:35], v177\n" \
  "ds_read_b128 v[36:39], v177 offset:1024\n" \
  "ds_read_b64 v[40:41], v178\n" \
  "ds_read_b64 v[42:43], v178 offset:512\n" \
  "ds_read_b32 v47, v179\n" \
  "s_waitcnt lgkmcnt(0)\n" \
  "v_cmp_eq_u32 vcc, 0, v46\n" \
  "s_sub_u32 %[cnt], %[cnt], 1\n" \
  "s_cbranch_scc1 21f\n" \
  "s_nop 1\n" \
  "s_cbranch_vccz 21f\n" \
  "s_sleep 2\n" \
  "s_branch 20b\n" \
  "21:\n" \
  "s_barrier\n" \
  "ds_read_b128 v[156:159], v44 offset:0\n" \
  "ds_read_b64 v[160:161], v45 offset:0\n" \
  "s_mov_b32 vcc_lo, -1\n" \
  "s_mov_b32 vcc_hi, 0\n" \
  "v_mov_b32 v48, 0\n" \
  "v_mov_b32 v49, 0\n" \
  "v_max_f32_dpp v47, v47, v47 row_ror:8 row_mask:0xf bank_mask:0xf\n" \
  "v_cndmask_b32 v40, 0, v40, vcc\n" \
  "v_cndmask_b32 v41, 0, v41, vcc\n" \
  "v_cndmask_b32 v42, 0, v42, vcc\n" \
  "v_cndmask_b32 v43, 0, v43, vcc\n" \
  "v_max_f32_dpp v47, v47, v47 row_ror:4 row_mask:0xf bank_mask:0xf\n" \
  "v_dot2c_f32_bf16 v48, v32, v32\n" \
  "v_dot2c_f32_bf16 v49, v36, v36\n" \
  "v_max_f32_dpp v47, v47, v47 row_ror:2 row_mask:0xf bank_mask:0xf\n" \
  "v_dot2c_f32_bf16 v48, v33, v33\n" \
  "v_dot2c_f32_bf16 v49, v37, v37\n" \
  "v_max_f32_dpp v47, v47, v47 row_ror:1 row_mask:0xf bank_mask:0xf\n" \
  "v_dot2c_f32_bf16 v48, v34, v34\n" \
  "v_dot2c_f32_bf16 v49, v38, v38\n" \
  "v_mov_b32 v46, v47\n" \
  "v_dot2c_f32_bf16 v48, v35, v35\n" \
  "v_dot2c_f32_bf16 v49, v39, v39\n" \
  "v_permlane16_swap_b32 v46, v47\n" \
  "v_dot2c_f32_bf16 v48, v40, v40\n" \
  "v_dot2c_f32_bf16 v49, v42, v42\n" \
  "v_max_f32 v47, v46, v47\n" \
  "v_dot2c_f32_bf16 v48, v41, v41\n" \
  "v_dot2c_f32_bf16 v49, v43, v43\n" \
  "s_nop 2\n" \
  "v_mov_b32 v50, v48\n" \
  "v_mov_b32 v51, v49\n" \
  "s_nop 0\n" \
  "v_permlane32_swap_b32 v48, v50\n" \
  "v_permlane32_swap_b32 v49, v51\n" \
  "s_nop 0\n" \
  "v_add_f32 v48, v48, v50\n" \
  "v_add_f32 v49, v49, v51\n" \
  "v_mul_f32 v48, v48, v47\n" \
  "v_mul_f32 v49, v49, v47\n" \
  "v_sqrt_f32 v48, v48\n" \
  "v_sqrt_f32 v49, v49\n" \
  "s_nop 0\n" \
  "v_mul_f32 v48, 0xbf8147ae, v48\n" \
  "v_mul_f32 v49, 0xbf8147ae, v49\n" \
  "v_cmp_le_f32 s[86:87], %[ms], v48\n" \
  "v_cmp_le_f32 s[88:89], %[ms], v49\n" \
  "v_cvt_pk_bf16_f32 v50, v48, 0\n" \
  "v_cvt_pk_bf16_f32 v51, v49, 0\n" \
  "s_and_b64 s[86:87], s[86:87], s[88:89]\n" \
  "s_cmp_eq_u64 s[86:87], exec\n" \
  "s_cbranch_scc0 22f\n" \
  "v_cndmask_b32 v40, v50, v40, vcc\n" \
  "v_cndmask_b32 v42, v51, v42, vcc\n" \
  "23:\n" \
  "s_mov_b32 %[sp], 0x07060302\n" \
  "s_waitcnt lgkmcnt(0)\n" \
  "v_mfma_f32_32x32x8bf16_1k v[78:93], v[160:161], v[40:41], 0\n" \
  "ds_read_b128 v[142:145], v44 offset:3584\n" \
  "ds_read_b64 v[146:147], v45 offset:3584\n" \
  "v_mfma_f32_32x32x8bf16_1k v[94:109], v[160:161], v[42:43], 0\n" \
  "ds_read_b128 v[148:151], v44 offset:5120\n" \
  "ds_read_b128 v[152:155], v44 offset:6144\n" \
  "v_mfma_f32_32x32x16_bf16 v[78:93], v[156:159], v[32:35], v[78:93]\n" \
  "v_mfma_f32_32x32x16_bf16 v[94:109], v[156:159], v[36:39], v[94:109]\n" \
  "s_nop 7\n" \
  "s_nop 7\n" \
  "s_waitcnt lgkmcnt(0)\n" \
  "v_mfma_f32_32x32x8bf16_1k v[46:61], v[146:147], v[40:41], 0\n" \
  "ds_read_b128 v[156:159], v44 offset:7168\n" \
  "ds_read_b64 v[160:161], v45 offset:7168\n" \
  "v_exp_f32 v78, v78\n" \
  "v_exp_f32 v79, v79\n" \
  "v_exp_f32 v80, v80\n" \
  "v_exp_f32 v81, v81\n" \
  "v_perm_b32 v126, v79, v78, %[sp]\n" \
  "v_perm_b32 v127, v81, v80, %[sp]\n" \
  "v_mfma_f32_32x32x8bf16_1k v[62:77], v[146:147], v[42:43], 0\n" \
  "ds_read_b128 v[162:165], v44 offset:8704\n" \
  "ds_read_b128 v[166:169], v44 offset:9728\n" \
  "v_exp_f32 v82, v82\n" \
  "v_exp_f32 v83, v83\n" \
  "v_exp_f32 v84, v84\n" \
  "v_exp_f32 v85, v85\n" \
  "v_perm_b32 v128, v83, v82, %[sp]\n" \
  "v_perm_b32 v129, v85, v84, %[sp]\n" \
  "v_mfma_f32_32x32x16_bf16 v[46:61], v[142:145], v[32:35], v[46:61]\n" \
  "v_exp_f32 v86, v86\n" \
  "v_exp_f32 v87, v87\n" \
  "v_exp_f32 v88, v88\n" \
  "v_exp_f32 v89, v89\n" \
  "v_perm_b32 v130, v87, v86, %[sp]\n" \
  "v_perm_b32 v131, v89, v88, %[sp]\n" \
  "v_mfma_f32_32x32x16_bf16 v[62:77], v[142:145], v[36:39], v[62:77]\n" \
  "v_exp_f32 v90, v90\n" \
  "v_exp_f32 v91, v91\n" \
  "v_exp_f32 v92, v92\n" \
  "v_exp_f32 v93, v93\n" \
  "v_perm_b32 v132, v91, v90, %[sp]\n" \
  "v_perm_b32 v133, v93, v92, %[sp]\n" \
  "v_exp_f32 v94, v94\n" \
  "v_exp_f32 v95, v95\n" \
  "v_exp_f32 v96, v96\n" \
  "v_exp_f32 v97, v97\n" \
  "v_perm_b32 v134, v95, v94, %[sp]\n" \
  "v_perm_b32 v135, v97, v96, %[sp]\n" \
  "v_exp_f32 v98, v98\n" \
  "v_exp_f32 v99, v99\n" \
  "v_exp_f32 v100, v100\n" \
  "v_exp_f32 v101, v101\n" \
  "v_perm_b32 v136, v99, v98, %[sp]\n" \
  "v_perm_b32 v137, v101, v100, %[sp]\n" \
  "v_exp_f32 v102, v102\n" \
  "v_exp_f32 v103, v103\n" \
  "v_exp_f32 v104, v104\n" \
  "v_exp_f32 v105, v105\n" \
  "v_perm_b32 v138, v103, v102, %[sp]\n" \
  "v_perm_b32 v139, v105, v104, %[sp]\n" \
  "v_exp_f32 v106, v106\n" \
  "v_exp_f32 v107, v107\n" \
  "v_exp_f32 v108, v108\n" \
  "v_exp_f32 v109, v109\n" \
  "v_perm_b32 v140, v107, v106, %[sp]\n" \
  "v_perm_b32 v141, v109, v108, %[sp]\n" \
  "s_waitcnt lgkmcnt(0)\n" \
  "v_mfma_f32_32x32x8bf16_1k v[78:93], v[160:161], v[40:41], 0\n" \
  "ds_read_b128 v[142:145], v44 offset:10752\n" \
  "ds_read_b64 v[146:147], v45 offset:10752\n" \
  "v_exp_f32 v46, v46\n" \
  "v_exp_f32 v47, v47\n" \
  "v_exp_f32 v48, v48\n" \
  "v_exp_f32 v49, v49\n" \
  "v_perm_b32 v110, v47, v46, %[sp]\n" \
  "v_perm_b32 v111, v49, v48, %[sp]\n" \
  "v_mfma_f32_32x32x8bf16_1k v[94:109], v[160:161], v[42:43], 0\n" \
  "ds_read_b128 v[148:151], v44 offset:12288\n" \
  "ds_read_b128 v[152:155], v44 offset:13312\n" \
  "v_exp_f32 v50, v50\n" \
  "v_exp_f32 v51, v51\n" \
  "v_exp_f32 v52, v52\n" \
  "v_exp_f32 v53, v53\n" \
  "v_perm_b32 v112, v51, v50, %[sp]\n" \
  "v_perm_b32 v113, v53, v52, %[sp]\n" \
  "v_mfma_f32_32x32x16_bf16 v[78:93], v[156:159], v[32:35], v[78:93]\n" \
  "v_exp_f32 v54, v54\n" \
  "v_exp_f32 v55, v55\n" \
  "v_exp_f32 v56, v56\n" \
  "v_exp_f32 v57, v57\n" \
  "v_perm_b32 v114, v55, v54, %[sp]\n" \
  "v_perm_b32 v115, v57, v56, %[sp]\n" \
  "v_mfma_f32_32x32x16_bf16 v[94:109], v[156:159], v[36:39], v[94:109]\n" \
  "v_exp_f32 v58, v58\n" \
  "v_exp_f32 v59, v59\n" \
  "v_exp_f32 v60, v60\n" \
  "v_exp_f32 v61, v61\n" \
  "v_perm_b32 v116, v59, v58, %[sp]\n" \
  "v_perm_b32 v117, v61, v60, %[sp]\n" \
  "v_mfma_f32_32x32x16_bf16 v[0:15], v[162:165], v[126:129], 0\n" \
  "v_exp_f32 v62, v62\n" \
  "v_exp_f32 v63, v63\n" \
  "v_exp_f32 v64, v64\n" \
  "v_exp_f32 v65, v65\n" \
  "v_perm_b32 v118, v63, v62, %[sp]\n" \
  "v_perm_b32 v119, v65, v64, %[sp]\n" \
  "v_mfma_f32_32x32x16_bf16 v[16:31], v[162:165], v[134:137], 0\n" \
  "v_exp_f32 v66, v66\n" \
  "v_exp_f32 v67, v67\n" \
  "v_exp_f32 v68, v68\n" \
  "v_exp_f32 v69, v69\n" \
  "v_perm_b32 v120, v67, v66, %[sp]\n" \
  "v_perm_b32 v121, v69, v68, %[sp]\n" \
  "v_mfma_f32_32x32x16_bf16 v[0:15], v[166:169], v[130:133], v[0:15]\n" \
  "v_exp_f32 v70, v70\n" \
  "v_exp_f32 v71, v71\n" \
  "v_exp_f32 v72, v72\n" \
  "v_exp_f32 v73, v73\n" \
  "v_perm_b32 v122, v71, v70, %[sp]\n" \
  "v_perm_b32 v123, v73, v72, %[sp]\n" \
  "v_mfma_f32_32x32x16_bf16 v[16:31], v[166:169], v[138:141], v[16:31]\n" \
  "v_exp_f32 v74, v74\n" \
  "v_exp_f32 v75, v75\n" \
  "v_exp_f32 v76, v76\n" \
  "v_exp_f32 v77, v77\n" \
  "v_perm_b32 v124, v75, v74, %[sp]\n" \
  "v_perm_b32 v125, v77, v76, %[sp]\n" \
  "s_waitcnt lgkmcnt(0)\n" \
  "v_mfma_f32_32x32x8bf16_1k v[46:61], v[146:147], v[40:41], 0\n" \
  "ds_read_b128 v[156:159], v44 offset:14336\n" \
  "ds_read_b64 v[160:161], v45 offset:14336\n" \
  "v_exp_f32 v78, v78\n" \
  "v_exp_f32 v79, v79\n" \
  "v_exp_f32 v80, v80\n" \
  "v_exp_f32 v81, v81\n" \
  "v_perm_b32 v126, v79, v78, %[sp]\n" \
  "v_perm_b32 v127, v81, v80, %[sp]\n" \
  "v_mfma_f32_32x32x8bf16_1k v[62:77], v[146:147], v[42:43], 0\n" \
  "ds_read_b128 v[162:165], v44 offset:15872\n" \
  "ds_read_b128 v[166:169], v44 offset:16896\n" \
  "v_exp_f32 v82, v82\n" \
  "v_exp_f32 v83, v83\n" \
  "v_exp_f32 v84, v84\n" \
  "v_exp_f32 v85, v85\n" \
  "v_perm_b32 v128, v83, v82, %[sp]\n" \
  "v_perm_b32 v129, v85, v84, %[sp]\n" \
  "v_mfma_f32_32x32x16_bf16 v[46:61], v[142:145], v[32:35], v[46:61]\n" \
  "v_exp_f32 v86, v86\n" \
  "v_exp_f32 v87, v87\n" \
  "v_exp_f32 v88, v88\n" \
  "v_exp_f32 v89, v89\n" \
  "v_perm_b32 v130, v87, v86, %[sp]\n" \
  "v_perm_b32 v131, v89, v88, %[sp]\n" \
  "v_mfma_f32_32x32x16_bf16 v[62:77], v[142:145], v[36:39], v[62:77]\n" \
  "v_exp_f32 v90, v90\n" \
  "v_exp_f32 v91, v91\n" \
  "v_exp_f32 v92, v92\n" \
  "v_exp_f32 v93, v93\n" \
  "v_perm_b32 v132, v91, v90, %[sp]\n" \
  "v_perm_b32 v133, v93, v92, %[sp]\n" \
  "v_mfma_f32_32x32x16_bf16 v[0:15], v[148:151], v[110:113], v[0:15]\n" \
  "v_exp_f32 v94, v94\n" \
  "v_exp_f32 v95, v95\n" \
  "v_exp_f32 v96, v96\n" \
  "v_exp_f32 v97, v97\n" \
  "v_perm_b32 v134, v95, v94, %[sp]\n" \
  "v_perm_b32 v135, v97, v96, %[sp]\n" \
  "v_mfma_f32_32x32x16_bf16 v[16:31], v[148:151], v[118:121], v[16:31]\n" \
  "v_exp_f32 v98, v98\n" \
  "v_exp_f32 v99, v99\n" \
  "v_exp_f32 v100, v100\n" \
  "v_exp_f32 v101, v101\n" \
  "v_perm_b32 v136, v99, v98, %[sp]\n" \
  "v_perm_b32 v137, v101, v100, %[sp]\n" \
  "v_mfma_f32_32x32x16_bf16 v[0:15], v[152:155], v[114:117], v[0:15]\n" \
  "v_exp_f32 v102, v102\n" \
  "v_exp_f32 v103, v103\n" \
  "v_exp_f32 v104, v104\n" \
  "v_exp_f32 v105, v105\n" \
  "v_perm_b32 v138, v103, v102, %[sp]\n" \
  "v_perm_b32 v139, v105, v104, %[sp]\n" \
  "v_mfma_f32_32x32x16_bf16 v[16:31], v[152:155], v[122:125], v[16:31]\n" \
  "v_exp_f32 v106, v106\n" \
  "v_exp_f32 v107, v107\n" \
  "v_exp_f32 v108, v108\n" \
  "v_exp_f32 v109, v109\n" \
  "v_perm_b32 v140, v107, v106, %[sp]\n" \
  "v_perm_b32 v141, v109, v108, %[sp]\n" \
  "s_waitcnt lgkmcnt(0)\n" \
  "v_mfma_f32_32x32x8bf16_1k v[78:93], v[160:161], v[40:41], 0\n" \
  "ds_read_b128 v[142:145], v44 offset:17920\n" \
  "ds_read_b64 v[146:147], v45 offset:17920\n" \
  "v_exp_f32 v46, v46\n" \
  "v_exp_f32 v47, v47\n" \
  "v_exp_f32 v48, v48\n" \
  "v_exp_f32 v49, v49\n" \
  "v_perm_b32 v110, v47, v46, %[sp]\n" \
  "v_perm_b32 v111, v49, v48, %[sp]\n" \
  "v_mfma_f32_32x32x8bf16_1k v[94:109], v[160:161], v[42:43], 0\n" \
  "ds_read_b128 v[148:151], v44 offset:19456\n" \
  "ds_read_b128 v[152:155], v44 offset:20480\n" \
  "v_exp_f32 v50, v50\n" \
  "v_exp_f32 v51, v51\n" \
  "v_exp_f32 v52, v52\n" \
  "v_exp_f32 v53, v53\n" \
  "v_perm_b32 v112, v51, v50, %[sp]\n" \
  "v_perm_b32 v113, v53, v52, %[sp]\n" \
  "v_mfma_f32_32x32x16_bf16 v[78:93], v[156:159], v[32:35], v[78:93]\n" \
  "v_exp_f32 v54, v54\n" \
  "v_exp_f32 v55, v55\n" \
  "v_exp_f32 v56, v56\n" \
  "v_exp_f32 v57, v57\n" \
  "v_perm_b32 v114, v55, v54, %[sp]\n" \
  "v_perm_b32 v115, v57, v56, %[sp]\n" \
  "v_mfma_f32_32x32x16_bf16 v[94:109], v[156:159], v[36:39], v[94:109]\n" \
  "v_exp_f32 v58, v58\n" \
  "v_exp_f32 v59, v59\n" \
  "v_exp_f32 v60, v60\n" \
  "v_exp_f32 v61, v61\n" \
  "v_perm_b32 v116, v59, v58, %[sp]\n" \
  "v_perm_b32 v117, v61, v60, %[sp]\n" \
  "v_mfma_f32_32x32x16_bf16 v[0:15], v[162:165], v[126:129], v[0:15]\n" \
  "v_exp_f32 v62, v62\n" \
  "v_exp_f32 v63, v63\n" \
  "v_exp_f32 v64, v64\n" \
  "v_exp_f32 v65, v65\n" \
  "v_perm_b32 v118, v63, v62, %[sp]\n" \
  "v_perm_b32 v119, v65, v64, %[sp]\n" \
  "v_mfma_f32_32x32x16_bf16 v[16:31], v[162:165], v[134:137], v[16:31]\n" \
  "v_exp_f32 v66, v66\n" \
  "v_exp_f32 v67, v67\n" \
  "v_exp_f32 v68, v68\n" \
  "v_exp_f32 v69, v69\n" \
  "v_perm_b32 v120, v67, v66, %[sp]\n" \
  "v_perm_b32 v121, v69, v68, %[sp]\n" \
  "v_mfma_f32_32x32x16_bf16 v[0:15], v[166:169], v[130:133], v[0:15]\n" \
  "v_exp_f32 v70, v70\n" \
  "v_exp_f32 v71, v71\n" \
  "v_exp_f32 v72, v72\n" \
  "v_exp_f32 v73, v73\n" \
  "v_perm_b32 v122, v71, v70, %[sp]\n" \
  "v_perm_b32 v123, v73, v72, %[sp]\n" \
  "v_mfma_f32_32x32x16_bf16 v[16:31], v[166:169], v[138:141], v[16:31]\n" \
  "v_exp_f32 v74, v74\n" \
  "v_exp_f32 v75, v75\n" \
  "v_exp_f32 v76, v76\n" \
  "v_exp_f32 v77, v77\n" \
  "v_perm_b32 v124, v75, v74, %[sp]\n" \
  "v_perm_b32 v125, v77, v76, %[sp]\n" \
  "s_waitcnt lgkmcnt(0)\n" \
  "v_mfma_f32_32x32x8bf16_1k v[46:61], v[146:147], v[40:41], 0\n" \
  "ds_read_b128 v[156:159], v44 offset:21504\n" \
  "ds_read_b64 v[160:161], v45 offset:21504\n" \
  "v_exp_f32 v78, v78\n" \
  "v_exp_f32 v79, v79\n" \
  "v_exp_f32 v80, v80\n" \
  "v_exp_f32 v81, v81\n" \
  "v_perm_b32 v126, v79, v78, %[sp]\n" \
  "v_perm_b32 v127, v81, v80, %[sp]\n" \
  "v_mfma_f32_32x32x8bf16_1k v[62:77], v[146:147], v[42:43], 0\n" \
  "ds_read_b128 v[162:165], v44 offset:23040\n" \
  "ds_read_b128 v[166:169], v44 offset:24064\n" \
  "v_exp_f32 v82, v82\n" \
  "v_exp_f32 v83, v83\n" \
  "v_exp_f32 v84, v84\n" \
  "v_exp_f32 v85, v85\n" \
  "v_perm_b32 v128, v83, v82, %[sp]\n" \
  "v_perm_b32 v129, v85, v84, %[sp]\n" \
  "v_mfma_f32_32x32x16_bf16 v[46:61], v[142:145], v[32:35], v[46:61]\n" \
  "v_exp_f32 v86, v86\n" \
  "v_exp_f32 v87, v87\n" \
  "v_exp_f32 v88, v88\n" \
  "v_exp_f32 v89, v89\n" \
  "v_perm_b32 v130, v87, v86, %[sp]\n" \
  "v_perm_b32 v131, v89, v88, %[sp]\n" \
  "v_mfma_f32_32x32x16_bf16 v[62:77], v[142:145], v[36:39], v[62:77]\n" \
  "v_exp_f32 v90, v90\n" \
  "v_exp_f32 v91, v91\n" \
  "v_exp_f32 v92, v92\n" \
  "v_exp_f32 v93, v93\n" \
  "v_perm_b32 v132, v91, v90, %[sp]\n" \
  "v_perm_b32 v133, v93, v92, %[sp]\n" \
  "v_mfma_f32_32x32x16_bf16 v[0:15], v[148:151], v[110:113], v[0:15]\n" \
  "v_exp_f32 v94, v94\n" \
  "v_exp_f32 v95, v95\n" \
  "v_exp_f32 v96, v96\n" \
  "v_exp_f32 v97, v97\n" \
  "v_perm_b32 v134, v95, v94, %[sp]\n" \
  "v_perm_b32 v135, v97, v96, %[sp]\n" \
  "v_mfma_f32_32x32x16_bf16 v[16:31], v[148:151], v[118:121], v[16:31]\n" \
  "v_exp_f32 v98, v98\n" \
  "v_exp_f32 v99, v99\n" \
  "v_exp_f32 v100, v100\n" \
  "v_exp_f32 v101, v101\n" \
  "v_perm_b32 v136, v99, v98, %[sp]\n" \
  "v_perm_b32 v137, v101, v100, %[sp]\n" \
  "v_mfma_f32_32x32x16_bf16 v[0:15], v[152:155], v[114:117], v[0:15]\n" \
  "v_exp_f32 v102, v102\n" \
  "v_exp_f32 v103, v103\n" \
  "v_exp_f32 v104, v104\n" \
  "v_exp_f32 v105, v105\n" \
  "v_perm_b32 v138, v103, v102, %[sp]\n" \
  "v_perm_b32 v139, v105, v104, %[sp]\n" \
  "v_mfma_f32_32x32x16_bf16 v[16:31], v[152:155], v[122:125], v[16:31]\n" \
  "v_exp_f32 v106, v106\n" \
  "v_exp_f32 v107, v107\n" \
  "v_exp_f32 v108, v108\n" \
  "v_exp_f32 v109, v109\n" \
  "v_perm_b32 v140, v107, v106, %[sp]\n" \
  "v_perm_b32 v141, v109, v108, %[sp]\n" \
  "s_waitcnt lgkmcnt(0)\n" \
  "v_mfma_f32_32x32x8bf16_1k v[78:93], v[160:161], v[40:41], 0\n" \
  "ds_read_b128 v[142:145], v44 offset:25088\n" \
  "ds_read_b64 v[146:147], v45 offset:25088\n" \
  "v_exp_f32 v46, v46\n" \
  "v_exp_f32 v47, v47\n" \
  "v_exp_f32 v48, v48\n" \
  "v_exp_f32 v49, v49\n" \
  "v_perm_b32 v110, v47, v46, %[sp]\n" \
  "v_perm_b32 v111, v49, v48, %[sp]\n" \
  "v_mfma_f32_32x32x8bf16_1k v[94:109], v[160:161], v[42:43], 0\n" \
  "ds_read_b128 v[148:151], v44 offset:26624\n" \
  "ds_read_b128 v[152:155], v44 offset:27648\n" \
  "v_exp_f32 v50, v50\n" \
  "v_exp_f32 v51, v51\n" \
  "v_exp_f32 v52, v52\n" \
  "v_exp_f32 v53, v53\n" \
  "v_perm_b32 v112, v51, v50, %[sp]\n" \
  "v_perm_b32 v113, v53, v52, %[sp]\n" \
  "v_mfma_f32_32x32x16_bf16 v[78:93], v[156:159], v[32:35], v[78:93]\n" \
  "v_exp_f32 v54, v54\n" \
  "v_exp_f32 v55, v55\n" \
  "v_exp_f32 v56, v56\n" \
  "v_exp_f32 v57, v57\n" \
  "v_perm_b32 v114, v55, v54, %[sp]\n" \
  "v_perm_b32 v115, v57, v56, %[sp]\n" \
  "v_mfma_f32_32x32x16_bf16 v[94:109], v[156:159], v[36:39], v[94:109]\n" \
  "v_exp_f32 v58, v58\n" \
  "v_exp_f32 v59, v59\n" \
  "v_exp_f32 v60, v60\n" \
  "v_exp_f32 v61, v61\n" \
  "v_perm_b32 v116, v59, v58, %[sp]\n" \
  "v_perm_b32 v117, v61, v60, %[sp]\n" \
  "v_mfma_f32_32x32x16_bf16 v[0:15], v[162:165], v[126:129], v[0:15]\n" \
  "v_exp_f32 v62, v62\n" \
  "v_exp_f32 v63, v63\n" \
  "v_exp_f32 v64, v64\n" \
  "v_exp_f32 v65, v65\n" \
  "v_perm_b32 v118, v63, v62, %[sp]\n" \
  "v_perm_b32 v119, v65, v64, %[sp]\n" \
  "v_mfma_f32_32x32x16_bf16 v[16:31], v[162:165], v[134:137], v[16:31]\n" \
  "v_exp_f32 v66, v66\n" \
  "v_exp_f32 v67, v67\n" \
  "v_exp_f32 v68, v68\n" \
  "v_exp_f32 v69, v69\n" \
  "v_perm_b32 v120, v67, v66, %[sp]\n" \
  "v_perm_b32 v121, v69, v68, %[sp]\n" \
  "v_mfma_f32_32x32x16_bf16 v[0:15], v[166:169], v[130:133], v[0:15]\n" \
  "v_exp_f32 v70, v70\n" \
  "v_exp_f32 v71, v71\n" \
  "v_exp_f32 v72, v72\n" \
  "v_exp_f32 v73, v73\n" \
  "v_perm_b32 v122, v71, v70, %[sp]\n" \
  "v_perm_b32 v123, v73, v72, %[sp]\n" \
  "v_mfma_f32_32x32x16_bf16 v[16:31], v[166:169], v[138:141], v[16:31]\n" \
  "v_exp_f32 v74, v74\n" \
  "v_exp_f32 v75, v75\n" \
  "v_exp_f32 v76, v76\n" \
  "v_exp_f32 v77, v77\n" \
  "v_perm_b32 v124, v75, v74, %[sp]\n" \
  "v_perm_b32 v125, v77, v76, %[sp]\n" \
  "s_mov_b32 %[cnt], 7\n" \
  "2:\n" \
  "s_waitcnt lgkmcnt(0)\n" \
  "s_barrier\n" \
  "v_mfma_f32_32x32x8bf16_1k v[46:61], v[146:147], v[40:41], 0\n" \
  "v_add_u32 v44, %[d], v44\n" \
  "v_add_u32 v45, %[d], v45\n" \
  "s_sub_u32 %[d], 0, %[d]\n" \
  "ds_read_b128 v[156:159], v44 offset:0\n" \
  "ds_read_b64 v[160:161], v45 offset:0\n" \
  "v_exp_f32 v78, v78\n" \
  "v_exp_f32 v79, v79\n" \
  "v_exp_f32 v80, v80\n" \
  "v_exp_f32 v81, v81\n" \
  "v_perm_b32 v126, v79, v78, %[sp]\n" \
  "v_perm_b32 v127, v81, v80, %[sp]\n" \
  "v_mfma_f32_32x32x8bf16_1k v[62:77], v[146:147], v[42:43], 0\n" \
  "ds_read_b128 v[162:165], v44 offset:1536\n" \
  "ds_read_b128 v[166:169], v44 offset:2560\n" \
  "v_exp_f32 v82, v82\n" \
  "v_exp_f32 v83, v83\n" \
  "v_exp_f32 v84, v84\n" \
  "v_exp_f32 v85, v85\n" \
  "v_perm_b32 v128, v83, v82, %[sp]\n" \
  "v_perm_b32 v129, v85, v84, %[sp]\n" \
  "v_mfma_f32_32x32x16_bf16 v[46:61], v[142:145], v[32:35], v[46:61]\n" \
  "v_exp_f32 v86, v86\n" \
  "v_exp_f32 v87, v87\n" \
  "v_exp_f32 v88, v88\n" \
  "v_exp_f32 v89, v89\n" \
  "v_perm_b32 v130, v87, v86, %[sp]\n" \
  "v_perm_b32 v131, v89, v88, %[sp]\n" \
  "v_mfma_f32_32x32x16_bf16 v[62:77], v[142:145], v[36:39], v[62:77]\n" \
  "v_exp_f32 v90, v90\n" \
  "v_exp_f32 v91, v91\n" \
  "v_exp_f32 v92, v92\n" \
  "v_exp_f32 v93, v93\n" \
  "v_perm_b32 v132, v91, v90, %[sp]\n" \
  "v_perm_b32 v133, v93, v92, %[sp]\n" \
  "v_mfma_f32_32x32x16_bf16 v[0:15], v[148:151], v[110:113], v[0:15]\n" \
  "v_exp_f32 v94, v94\n" \
  "v_exp_f32 v95, v95\n" \
  "v_exp_f32 v96, v96\n" \
  "v_exp_f32 v97, v97\n" \
  "v_perm_b32 v134, v95, v94, %[sp]\n" \
  "v_perm_b32 v135, v97, v96, %[sp]\n" \
  "v_mfma_f32_32x32x16_bf16 v[16:31], v[148:151], v[118:121], v[16:31]\n" \
  "v_exp_f32 v98, v98\n" \
  "v_exp_f32 v99, v99\n" \
  "v_exp_f32 v100, v100\n" \
  "v_exp_f32 v101, v101\n" \
  "v_perm_b32 v136, v99, v98, %[sp]\n" \
  "v_perm_b32 v137, v101, v100, %[sp]\n" \
  "v_mfma_f32_32x32x16_bf16 v[0:15], v[152:155], v[114:117], v[0:15]\n" \
  "v_exp_f32 v102, v102\n" \
  "v_exp_f32 v103, v103\n" \
  "v_exp_f32 v104, v104\n" \
  "v_exp_f32 v105, v105\n" \
  "v_perm_b32 v138, v103, v102, %[sp]\n" \
  "v_perm_b32 v139, v105, v104, %[sp]\n" \
  "v_mfma_f32_32x32x16_bf16 v[16:31], v[152:155], v[122:125], v[16:31]\n" \
  "v_exp_f32 v106, v106\n" \
  "v_exp_f32 v107, v107\n" \
  "v_exp_f32 v108, v108\n" \
  "v_exp_f32 v109, v109\n" \
  "v_perm_b32 v140, v107, v106, %[sp]\n" \
  "v_perm_b32 v141, v109, v108, %[sp]\n" \
  "s_waitcnt lgkmcnt(0)\n" \
  "v_mfma_f32_32x32x8bf16_1k v[78:93], v[160:161], v[40:41], 0\n" \
  "ds_read_b128 v[142:145], v44 offset:3584\n" \
  "ds_read_b64 v[146:147], v45 offset:3584\n" \
  "v_exp_f32 v46, v46\n" \
  "v_exp_f32 v47, v47\n" \
  "v_exp_f32 v48, v48\n" \
  "v_exp_f32 v49, v49\n" \
  "v_perm_b32 v110, v47, v46, %[sp]\n" \
  "v_perm_b32 v111, v49, v48, %[sp]\n" \
  "v_mfma_f32_32x32x8bf16_1k v[94:109], v[160:161], v[42:43], 0\n" \
  "ds_read_b128 v[148:151], v44 offset:5120\n" \
  "ds_read_b128 v[152:155], v44 offset:6144\n" \
  "v_exp_f32 v50, v50\n" \
  "v_exp_f32 v51, v51\n" \
  "v_exp_f32 v52, v52\n" \
  "v_exp_f32 v53, v53\n" \
  "v_perm_b32 v112, v51, v50, %[sp]\n" \
  "v_perm_b32 v113, v53, v52, %[sp]\n" \
  "v_mfma_f32_32x32x16_bf16 v[78:93], v[156:159], v[32:35], v[78:93]\n" \
  "v_exp_f32 v54, v54\n" \
  "v_exp_f32 v55, v55\n" \
  "v_exp_f32 v56, v56\n" \
  "v_exp_f32 v57, v57\n" \
  "v_perm_b32 v114, v55, v54, %[sp]\n" \
  "v_perm_b32 v115, v57, v56, %[sp]\n" \
  "v_mfma_f32_32x32x16_bf16 v[94:109], v[156:159], v[36:39], v[94:109]\n" \
  "v_exp_f32 v58, v58\n" \
  "v_exp_f32 v59, v59\n" \
  "v_exp_f32 v60, v60\n" \
  "v_exp_f32 v61, v61\n" \
  "v_perm_b32 v116, v59, v58, %[sp]\n" \
  "v_perm_b32 v117, v61, v60, %[sp]\n" \
  "v_mfma_f32_32x32x16_bf16 v[0:15], v[162:165], v[126:129], v[0:15]\n" \
  "v_exp_f32 v62, v62\n" \
  "v_exp_f32 v63, v63\n" \
  "v_exp_f32 v64, v64\n" \
  "v_exp_f32 v65, v65\n" \
  "v_perm_b32 v118, v63, v62, %[sp]\n" \
  "v_perm_b32 v119, v65, v64, %[sp]\n" \
  "v_mfma_f32_32x32x16_bf16 v[16:31], v[162:165], v[134:137], v[16:31]\n" \
  "v_exp_f32 v66, v66\n" \
  "v_exp_f32 v67, v67\n" \
  "v_exp_f32 v68, v68\n" \
  "v_exp_f32 v69, v69\n" \
  "v_perm_b32 v120, v67, v66, %[sp]\n" \
  "v_perm_b32 v121, v69, v68, %[sp]\n" \
  "v_mfma_f32_32x32x16_bf16 v[0:15], v[166:169], v[130:133], v[0:15]\n" \
  "v_exp_f32 v70, v70\n" \
  "v_exp_f32 v71, v71\n" \
  "v_exp_f32 v72, v72\n" \
  "v_exp_f32 v73, v73\n" \
  "v_perm_b32 v122, v71, v70, %[sp]\n" \
  "v_perm_b32 v123, v73, v72, %[sp]\n" \
  "v_mfma_f32_32x32x16_bf16 v[16:31], v[166:169], v[138:141], v[16:31]\n" \
  "v_exp_f32 v74, v74\n" \
  "v_exp_f32 v75, v75\n" \
  "v_exp_f32 v76, v76\n" \
  "v_exp_f32 v77, v77\n" \
  "v_perm_b32 v124, v75, v74, %[sp]\n" \
  "v_perm_b32 v125, v77, v76, %[sp]\n" \
  "s_waitcnt lgkmcnt(0)\n" \
  "v_mfma_f32_32x32x8bf16_1k v[46:61], v[146:147], v[40:41], 0\n" \
  "ds_read_b128 v[156:159], v44 offset:7168\n" \
  "ds_read_b64 v[160:161], v45 offset:7168\n" \
  "v_exp_f32 v78, v78\n" \
  "v_exp_f32 v79, v79\n" \
  "v_exp_f32 v80, v80\n" \
  "v_exp_f32 v81, v81\n" \
  "v_perm_b32 v126, v79, v78, %[sp]\n" \
  "v_perm_b32 v127, v81, v80, %[sp]\n" \
  "v_mfma_f32_32x32x8bf16_1k v[62:77], v[146:147], v[42:43], 0\n" \
  "ds_read_b128 v[162:165], v44 offset:8704\n" \
  "ds_read_b128 v[166:169], v44 offset:9728\n" \
  "v_exp_f32 v82, v82\n" \
  "v_exp_f32 v83, v83\n" \
  "v_exp_f32 v84, v84\n" \
  "v_exp_f32 v85, v85\n" \
  "v_perm_b32 v128, v83, v82, %[sp]\n" \
  "v_perm_b32 v129, v85, v84, %[sp]\n" \
  "v_mfma_f32_32x32x16_bf16 v[46:61], v[142:145], v[32:35], v[46:61]\n" \
  "v_exp_f32 v86, v86\n" \
  "v_exp_f32 v87, v87\n" \
  "v_exp_f32 v88, v88\n" \
  "v_exp_f32 v89, v89\n" \
  "v_perm_b32 v130, v87, v86, %[sp]\n" \
  "v_perm_b32 v131, v89, v88, %[sp]\n" \
  "v_mfma_f32_32x32x16_bf16 v[62:77], v[142:145], v[36:39], v[62:77]\n" \
  "v_exp_f32 v90, v90\n" \
  "v_exp_f32 v91, v91\n" \
  "v_exp_f32 v92, v92\n" \
  "v_exp_f32 v93, v93\n" \
  "v_perm_b32 v132, v91, v90, %[sp]\n" \
  "v_perm_b32 v133, v93, v92, %[sp]\n" \
  "v_mfma_f32_32x32x16_bf16 v[0:15], v[148:151], v[110:113], v[0:15]\n" \
  "v_exp_f32 v94, v94\n" \
  "v_exp_f32 v95, v95\n" \
  "v_exp_f32 v96, v96\n" \
  "v_exp_f32 v97, v97\n" \
  "v_perm_b32 v134, v95, v94, %[sp]\n" \
  "v_perm_b32 v135, v97, v96, %[sp]\n" \
  "v_mfma_f32_32x32x16_bf16 v[16:31], v[148:151], v[118:121], v[16:31]\n" \
  "v_exp_f32 v98, v98\n" \
  "v_exp_f32 v99, v99\n" \
  "v_exp_f32 v100, v100\n" \
  "v_exp_f32 v101, v101\n" \
  "v_perm_b32 v136, v99, v98, %[sp]\n" \
  "v_perm_b32 v137, v101, v100, %[sp]\n" \
  "v_mfma_f32_32x32x16_bf16 v[0:15], v[152:155], v[114:117], v[0:15]\n" \
  "v_exp_f32 v102, v102\n" \
  "v_exp_f32 v103, v103\n" \
  "v_exp_f32 v104, v104\n" \
  "v_exp_f32 v105, v105\n" \
  "v_perm_b32 v138, v103, v102, %[sp]\n" \
  "v_perm_b32 v139, v105, v104, %[sp]\n" \
  "v_mfma_f32_32x32x16_bf16 v[16:31], v[152:155], v[122:125], v[16:31]\n" \
  "v_exp_f32 v106, v106\n" \
  "v_exp_f32 v107, v107\n" \
  "v_exp_f32 v108, v108\n" \
  "v_exp_f32 v109, v109\n" \
  "v_perm_b32 v140, v107, v106, %[sp]\n" \
  "v_perm_b32 v141, v109, v108, %[sp]\n" \
  "s_waitcnt lgkmcnt(0)\n" \
  "v_mfma_f32_32x32x8bf16_1k v[78:93], v[160:161], v[40:41], 0\n" \
  "ds_read_b128 v[142:145], v44 offset:10752\n" \
  "ds_read_b64 v[146:147], v45 offset:10752\n" \
  "v_exp_f32 v46, v46\n" \
  "v_exp_f32 v47, v47\n" \
  "v_exp_f32 v48, v48\n" \
  "v_exp_f32 v49, v49\n" \
  "v_perm_b32 v110, v47, v46, %[sp]\n" \
  "v_perm_b32 v111, v49, v48, %[sp]\n" \
  "v_mfma_f32_32x32x8bf16_1k v[94:109], v[160:161], v[42:43], 0\n" \
  "ds_read_b128 v[148:151], v44 offset:12288\n" \
  "ds_read_b128 v[152:155], v44 offset:13312\n" \
  "v_exp_f32 v50, v50\n" \
  "v_exp_f32 v51, v51\n" \
  "v_exp_f32 v52, v52\n" \
  "v_exp_f32 v53, v53\n" \
  "v_perm_b32 v112, v51, v50, %[sp]\n" \
  "v_perm_b32 v113, v53, v52, %[sp]\n" \
  "v_mfma_f32_32x32x16_bf16 v[78:93], v[156:159], v[32:35], v[78:93]\n" \
  "v_exp_f32 v54, v54\n" \
  "v_exp_f32 v55, v55\n" \
  "v_exp_f32 v56, v56\n" \
  "v_exp_f32 v57, v57\n" \
  "v_perm_b32 v114, v55, v54, %[sp]\n" \
  "v_perm_b32 v115, v57, v56, %[sp]\n" \
  "v_mfma_f32_32x32x16_bf16 v[94:109], v[156:159], v[36:39], v[94:109]\n" \
  "v_exp_f32 v58, v58\n" \
  "v_exp_f32 v59, v59\n" \
  "v_exp_f32 v60, v60\n" \
  "v_exp_f32 v61, v61\n" \
  "v_perm_b32 v116, v59, v58, %[sp]\n" \
  "v_perm_b32 v117, v61, v60, %[sp]\n" \
  "v_mfma_f32_32x32x16_bf16 v[0:15], v[162:165], v[126:129], v[0:15]\n" \
  "v_exp_f32 v62, v62\n" \
  "v_exp_f32 v63, v63\n" \
  "v_exp_f32 v64, v64\n" \
  "v_exp_f32 v65, v65\n" \
  "v_perm_b32 v118, v63, v62, %[sp]\n" \
  "v_perm_b32 v119, v65, v64, %[sp]\n" \
  "v_mfma_f32_32x32x16_bf16 v[16:31], v[162:165], v[134:137], v[16:31]\n" \
  "v_exp_f32 v66, v66\n" \
  "v_exp_f32 v67, v67\n" \
  "v_exp_f32 v68, v68\n" \
  "v_exp_f32 v69, v69\n" \
  "v_perm_b32 v120, v67, v66, %[sp]\n" \
  "v_perm_b32 v121, v69, v68, %[sp]\n" \
  "v_mfma_f32_32x32x16_bf16 v[0:15], v[166:169], v[130:133], v[0:15]\n" \
  "v_exp_f32 v70, v70\n" \
  "v_exp_f32 v71, v71\n" \
  "v_exp_f32 v72, v72\n" \
  "v_exp_f32 v73, v73\n" \
  "v_perm_b32 v122, v71, v70, %[sp]\n" \
  "v_perm_b32 v123, v73, v72, %[sp]\n" \
  "v_mfma_f32_32x32x16_bf16 v[16:31], v[166:169], v[138:141], v[16:31]\n" \
  "v_exp_f32 v74, v74\n" \
  "v_exp_f32 v75, v75\n" \
  "v_exp_f32 v76, v76\n" \
  "v_exp_f32 v77, v77\n" \
  "v_perm_b32 v124, v75, v74, %[sp]\n" \
  "v_perm_b32 v125, v77, v76, %[sp]\n" \
  "s_waitcnt lgkmcnt(0)\n" \
  "v_mfma_f32_32x32x8bf16_1k v[46:61], v[146:147], v[40:41], 0\n" \
  "ds_read_b128 v[156:159], v44 offset:14336\n" \
  "ds_read_b64 v[160:161], v45 offset:14336\n" \
  "v_exp_f32 v78, v78\n" \
  "v_exp_f32 v79, v79\n" \
  "v_exp_f32 v80, v80\n" \
  "v_exp_f32 v81, v81\n" \
  "v_perm_b32 v126, v79, v78, %[sp]\n" \
  "v_perm_b32 v127, v81, v80, %[sp]\n" \
  "v_mfma_f32_32x32x8bf16_1k v[62:77], v[146:147], v[42:43], 0\n" \
  "ds_read_b128 v[162:165], v44 offset:15872\n" \
  "ds_read_b128 v[166:169], v44 offset:16896\n" \
  "v_exp_f32 v82, v82\n" \
  "v_exp_f32 v83, v83\n" \
  "v_exp_f32 v84, v84\n" \
  "v_exp_f32 v85, v85\n" \
  "v_perm_b32 v128, v83, v82, %[sp]\n" \
  "v_perm_b32 v129, v85, v84, %[sp]\n" \
  "v_mfma_f32_32x32x16_bf16 v[46:61], v[142:145], v[32:35], v[46:61]\n" \
  "v_exp_f32 v86, v86\n" \
  "v_exp_f32 v87, v87\n" \
  "v_exp_f32 v88, v88\n" \
  "v_exp_f32 v89, v89\n" \
  "v_perm_b32 v130, v87, v86, %[sp]\n" \
  "v_perm_b32 v131, v89, v88, %[sp]\n" \
  "v_mfma_f32_32x32x16_bf16 v[62:77], v[142:145], v[36:39], v[62:77]\n" \
  "v_exp_f32 v90, v90\n" \
  "v_exp_f32 v91, v91\n" \
  "v_exp_f32 v92, v92\n" \
  "v_exp_f32 v93, v93\n" \
  "v_perm_b32 v132, v91, v90, %[sp]\n" \
  "v_perm_b32 v133, v93, v92, %[sp]\n" \
  "v_mfma_f32_32x32x16_bf16 v[0:15], v[148:151], v[110:113], v[0:15]\n" \
  "v_exp_f32 v94, v94\n" \
  "v_exp_f32 v95, v95\n" \
  "v_exp_f32 v96, v96\n" \
  "v_exp_f32 v97, v97\n" \
  "v_perm_b32 v134, v95, v94, %[sp]\n" \
  "v_perm_b32 v135, v97, v96, %[sp]\n" \
  "v_mfma_f32_32x32x16_bf16 v[16:31], v[148:151], v[118:121], v[16:31]\n" \
  "v_exp_f32 v98, v98\n" \
  "v_exp_f32 v99, v99\n" \
  "v_exp_f32 v100, v100\n" \
  "v_exp_f32 v101, v101\n" \
  "v_perm_b32 v136, v99, v98, %[sp]\n" \
  "v_perm_b32 v137, v101, v100, %[sp]\n" \
  "v_mfma_f32_32x32x16_bf16 v[0:15], v[152:155], v[114:117], v[0:15]\n" \
  "v_exp_f32 v102, v102\n" \
  "v_exp_f32 v103, v103\n" \
  "v_exp_f32 v104, v104\n" \
  "v_exp_f32 v105, v105\n" \
  "v_perm_b32 v138, v103, v102, %[sp]\n" \
  "v_perm_b32 v139, v105, v104, %[sp]\n" \
  "v_mfma_f32_32x32x16_bf16 v[16:31], v[152:155], v[122:125], v[16:31]\n" \
  "v_exp_f32 v106, v106\n" \
  "v_exp_f32 v107, v107\n" \
  "v_exp_f32 v108, v108\n" \
  "v_exp_f32 v109, v109\n" \
  "v_perm_b32 v140, v107, v106, %[sp]\n" \
  "v_perm_b32 v141, v109, v108, %[sp]\n" \
  "s_waitcnt lgkmcnt(0)\n" \
  "v_mfma_f32_32x32x8bf16_1k v[78:93], v[160:161], v[40:41], 0\n" \
  "ds_read_b128 v[142:145], v44 offset:17920\n" \
  "ds_read_b64 v[146:147], v45 offset:17920\n" \
  "v_exp_f32 v46, v46\n" \
  "v_exp_f32 v47, v47\n" \
  "v_exp_f32 v48, v48\n" \
  "v_exp_f32 v49, v49\n" \
  "v_perm_b32 v110, v47, v46, %[sp]\n" \
  "v_perm_b32 v111, v49, v48, %[sp]\n" \
  "v_mfma_f32_32x32x8bf16_1k v[94:109], v[160:161], v[42:43], 0\n" \
  "ds_read_b128 v[148:151], v44 offset:19456\n" \
  "ds_read_b128 v[152:155], v44 offset:20480\n" \
  "v_exp_f32 v50, v50\n" \
  "v_exp_f32 v51, v51\n" \
  "v_exp_f32 v52, v52\n" \
  "v_exp_f32 v53, v53\n" \
  "v_perm_b32 v112, v51, v50, %[sp]\n" \
  "v_perm_b32 v113, v53, v52, %[sp]\n" \
  "v_mfma_f32_32x32x16_bf16 v[78:93], v[156:159], v[32:35], v[78:93]\n" \
  "v_exp_f32 v54, v54\n" \
  "v_exp_f32 v55, v55\n" \
  "v_exp_f32 v56, v56\n" \
  "v_exp_f32 v57, v57\n" \
  "v_perm_b32 v114, v55, v54, %[sp]\n" \
  "v_perm_b32 v115, v57, v56, %[sp]\n" \
  "v_mfma_f32_32x32x16_bf16 v[94:109], v[156:159], v[36:39], v[94:109]\n" \
  "v_exp_f32 v58, v58\n" \
  "v_exp_f32 v59, v59\n" \
  "v_exp_f32 v60, v60\n" \
  "v_exp_f32 v61, v61\n" \
  "v_perm_b32 v116, v59, v58, %[sp]\n" \
  "v_perm_b32 v117, v61, v60, %[sp]\n" \
  "v_mfma_f32_32x32x16_bf16 v[0:15], v[162:165], v[126:129], v[0:15]\n" \
  "v_exp_f32 v62, v62\n" \
  "v_exp_f32 v63, v63\n" \
  "v_exp_f32 v64, v64\n" \
  "v_exp_f32 v65, v65\n" \
  "v_perm_b32 v118, v63, v62, %[sp]\n" \
  "v_perm_b32 v119, v65, v64, %[sp]\n" \
  "v_mfma_f32_32x32x16_bf16 v[16:31], v[162:165], v[134:137], v[16:31]\n" \
  "v_exp_f32 v66, v66\n" \
  "v_exp_f32 v67, v67\n" \
  "v_exp_f32 v68, v68\n" \
  "v_exp_f32 v69, v69\n" \
  "v_perm_b32 v120, v67, v66, %[sp]\n" \
  "v_perm_b32 v121, v69, v68, %[sp]\n" \
  "v_mfma_f32_32x32x16_bf16 v[0:15], v[166:169], v[130:133], v[0:15]\n" \
  "v_exp_f32 v70, v70\n" \
  "v_exp_f32 v71, v71\n" \
  "v_exp_f32 v72, v72\n" \
  "v_exp_f32 v73, v73\n" \
  "v_perm_b32 v122, v71, v70, %[sp]\n" \
  "v_perm_b32 v123, v73, v72, %[sp]\n" \
  "v_mfma_f32_32x32x16_bf16 v[16:31], v[166:169], v[138:141], v[16:31]\n" \
  "v_exp_f32 v74, v74\n" \
  "v_exp_f32 v75, v75\n" \
  "v_exp_f32 v76, v76\n" \
  "v_exp_f32 v77, v77\n" \
  "v_perm_b32 v124, v75, v74, %[sp]\n" \
  "v_perm_b32 v125, v77, v76, %[sp]\n" \
  "s_waitcnt lgkmcnt(0)\n" \
  "v_mfma_f32_32x32x8bf16_1k v[46:61], v[146:147], v[40:41], 0\n" \
  "ds_read_b128 v[156:159], v44 offset:21504\n" \
  "ds_read_b64 v[160:161], v45 offset:21504\n" \
  "v_exp_f32 v78, v78\n" \
  "v_exp_f32 v79, v79\n" \
  "v_exp_f32 v80, v80\n" \
  "v_exp_f32 v81, v81\n" \
  "v_perm_b32 v126, v79, v78, %[sp]\n" \
  "v_perm_b32 v127, v81, v80, %[sp]\n" \
  "v_mfma_f32_32x32x8bf16_1k v[62:77], v[146:147], v[42:43], 0\n" \
  "ds_read_b128 v[162:165], v44 offset:23040\n" \
  "ds_read_b128 v[166:169], v44 offset:24064\n" \
  "v_exp_f32 v82, v82\n" \
  "v_exp_f32 v83, v83\n" \
  "v_exp_f32 v84, v84\n" \
  "v_exp_f32 v85, v85\n" \
  "v_perm_b32 v128, v83, v82, %[sp]\n" \
  "v_perm_b32 v129, v85, v84, %[sp]\n" \
  "v_mfma_f32_32x32x16_bf16 v[46:61], v[142:145], v[32:35], v[46:61]\n" \
  "v_exp_f32 v86, v86\n" \
  "v_exp_f32 v87, v87\n" \
  "v_exp_f32 v88, v88\n" \
  "v_exp_f32 v89, v89\n" \
  "v_perm_b32 v130, v87, v86, %[sp]\n" \
  "v_perm_b32 v131, v89, v88, %[sp]\n" \
  "v_mfma_f32_32x32x16_bf16 v[62:77], v[142:145], v[36:39], v[62:77]\n" \
  "v_exp_f32 v90, v90\n" \
  "v_exp_f32 v91, v91\n" \
  "v_exp_f32 v92, v92\n" \
  "v_exp_f32 v93, v93\n" \
  "v_perm_b32 v132, v91, v90, %[sp]\n" \
  "v_perm_b32 v133, v93, v92, %[sp]\n" \
  "v_mfma_f32_32x32x16_bf16 v[0:15], v[148:151], v[110:113], v[0:15]\n" \
  "v_exp_f32 v94, v94\n" \
  "v_exp_f32 v95, v95\n" \
  "v_exp_f32 v96, v96\n" \
  "v_exp_f32 v97, v97\n" \
  "v_perm_b32 v134, v95, v94, %[sp]\n" \
  "v_perm_b32 v135, v97, v96, %[sp]\n" \
  "v_mfma_f32_32x32x16_bf16 v[16:31], v[148:151], v[118:121], v[16:31]\n" \
  "v_exp_f32 v98, v98\n" \
  "v_exp_f32 v99, v99\n" \
  "v_exp_f32 v100, v100\n" \
  "v_exp_f32 v101, v101\n" \
  "v_perm_b32 v136, v99, v98, %[sp]\n" \
  "v_perm_b32 v137, v101, v100, %[sp]\n" \
  "v_mfma_f32_32x32x16_bf16 v[0:15], v[152:155], v[114:117], v[0:15]\n" \
  "v_exp_f32 v102, v102\n" \
  "v_exp_f32 v103, v103\n" \
  "v_exp_f32 v104, v104\n" \
  "v_exp_f32 v105, v105\n" \
  "v_perm_b32 v138, v103, v102, %[sp]\n" \
  "v_perm_b32 v139, v105, v104, %[sp]\n" \
  "v_mfma_f32_32x32x16_bf16 v[16:31], v[152:155], v[122:125], v[16:31]\n" \
  "v_exp_f32 v106, v106\n" \
  "v_exp_f32 v107, v107\n" \
  "v_exp_f32 v108, v108\n" \
  "v_exp_f32 v109, v109\n" \
  "v_perm_b32 v140, v107, v106, %[sp]\n" \
  "v_perm_b32 v141, v109, v108, %[sp]\n" \
  "s_waitcnt lgkmcnt(0)\n" \
  "v_mfma_f32_32x32x8bf16_1k v[78:93], v[160:161], v[40:41], 0\n" \
  "ds_read_b128 v[142:145], v44 offset:25088\n" \
  "ds_read_b64 v[146:147], v45 offset:25088\n" \
  "v_exp_f32 v46, v46\n" \
  "v_exp_f32 v47, v47\n" \
  "v_exp_f32 v48, v48\n" \
  "v_exp_f32 v49, v49\n" \
  "v_perm_b32 v110, v47, v46, %[sp]\n" \
  "v_perm_b32 v111, v49, v48, %[sp]\n" \
  "v_mfma_f32_32x32x8bf16_1k v[94:109], v[160:161], v[42:43], 0\n" \
  "ds_read_b128 v[148:151], v44 offset:26624\n" \
  "ds_read_b128 v[152:155], v44 offset:27648\n" \
  "v_exp_f32 v50, v50\n" \
  "v_exp_f32 v51, v51\n" \
  "v_exp_f32 v52, v52\n" \
  "v_exp_f32 v53, v53\n" \
  "v_perm_b32 v112, v51, v50, %[sp]\n" \
  "v_perm_b32 v113, v53, v52, %[sp]\n" \
  "v_mfma_f32_32x32x16_bf16 v[78:93], v[156:159], v[32:35], v[78:93]\n" \
  "v_exp_f32 v54, v54\n" \
  "v_exp_f32 v55, v55\n" \
  "v_exp_f32 v56, v56\n" \
  "v_exp_f32 v57, v57\n" \
  "v_perm_b32 v114, v55, v54, %[sp]\n" \
  "v_perm_b32 v115, v57, v56, %[sp]\n" \
  "v_mfma_f32_32x32x16_bf16 v[94:109], v[156:159], v[36:39], v[94:109]\n" \
  "v_exp_f32 v58, v58\n" \
  "v_exp_f32 v59, v59\n" \
  "v_exp_f32 v60, v60\n" \
  "v_exp_f32 v61, v61\n" \
  "v_perm_b32 v116, v59, v58, %[sp]\n" \
  "v_perm_b32 v117, v61, v60, %[sp]\n" \
  "v_mfma_f32_32x32x16_bf16 v[0:15], v[162:165], v[126:129], v[0:15]\n" \
  "v_exp_f32 v62, v62\n" \
  "v_exp_f32 v63, v63\n" \
  "v_exp_f32 v64, v64\n" \
  "v_exp_f32 v65, v65\n" \
  "v_perm_b32 v118, v63, v62, %[sp]\n" \
  "v_perm_b32 v119, v65, v64, %[sp]\n" \
  "v_mfma_f32_32x32x16_bf16 v[16:31], v[162:165], v[134:137], v[16:31]\n" \
  "v_exp_f32 v66, v66\n" \
  "v_exp_f32 v67, v67\n" \
  "v_exp_f32 v68, v68\n" \
  "v_exp_f32 v69, v69\n" \
  "v_perm_b32 v120, v67, v66, %[sp]\n" \
  "v_perm_b32 v121, v69, v68, %[sp]\n" \
  "v_mfma_f32_32x32x16_bf16 v[0:15], v[166:169], v[130:133], v[0:15]\n" \
  "v_exp_f32 v70, v70\n" \
  "v_exp_f32 v71, v71\n" \
  "v_exp_f32 v72, v72\n" \
  "v_exp_f32 v73, v73\n" \
  "v_perm_b32 v122, v71, v70, %[sp]\n" \
  "v_perm_b32 v123, v73, v72, %[sp]\n" \
  "v_mfma_f32_32x32x16_bf16 v[16:31], v[166:169], v[138:141], v[16:31]\n" \
  "v_exp_f32 v74, v74\n" \
  "v_exp_f32 v75, v75\n" \
  "v_exp_f32 v76, v76\n" \
  "v_exp_f32 v77, v77\n" \
  "v_perm_b32 v124, v75, v74, %[sp]\n" \
  "v_perm_b32 v125, v77, v76, %[sp]\n" \
  "s_sub_u32 %[cnt], %[cnt], 1\n" \
  "s_cmp_lg_u32 %[cnt], 0\n" \
  "s_cbranch_scc1 2b\n" \
  "s_waitcnt lgkmcnt(0)\n" \
  "s_barrier\n" \
  "v_mfma_f32_32x32x8bf16_1k v[46:61], v[146:147], v[40:41], 0\n" \
  "v_add_u32 v44, %[d], v44\n" \
  "v_add_u32 v45, %[d], v45\n" \
  "s_sub_u32 %[d], 0, %[d]\n" \
  "ds_read_b128 v[156:159], v44 offset:0\n" \
  "ds_read_b64 v[160:161], v45 offset:0\n" \
  "v_exp_f32 v78, v78\n" \
  "v_exp_f32 v79, v79\n" \
  "v_exp_f32 v80, v80\n" \
  "v_exp_f32 v81, v81\n" \
  "v_perm_b32 v126, v79, v78, %[sp]\n" \
  "v_perm_b32 v127, v81, v80, %[sp]\n" \
  "v_mfma_f32_32x32x8bf16_1k v[62:77], v[146:147], v[42:43], 0\n" \
  "ds_read_b128 v[162:165], v44 offset:1536\n" \
  "ds_read_b128 v[166:169], v44 offset:2560\n" \
  "v_exp_f32 v82, v82\n" \
  "v_exp_f32 v83, v83\n" \
  "v_exp_f32 v84, v84\n" \
  "v_exp_f32 v85, v85\n" \
  "v_perm_b32 v128, v83, v82, %[sp]\n" \
  "v_perm_b32 v129, v85, v84, %[sp]\n" \
  "v_mfma_f32_32x32x16_bf16 v[46:61], v[142:145], v[32:35], v[46:61]\n" \
  "v_exp_f32 v86, v86\n" \
  "v_exp_f32 v87, v87\n" \
  "v_exp_f32 v88, v88\n" \
  "v_exp_f32 v89, v89\n" \
  "v_perm_b32 v130, v87, v86, %[sp]\n" \
  "v_perm_b32 v131, v89, v88, %[sp]\n" \
  "v_mfma_f32_32x32x16_bf16 v[62:77], v[142:145], v[36:39], v[62:77]\n" \
  "v_exp_f32 v90, v90\n" \
  "v_exp_f32 v91, v91\n" \
  "v_exp_f32 v92, v92\n" \
  "v_exp_f32 v93, v93\n" \
  "v_perm_b32 v132, v91, v90, %[sp]\n" \
  "v_perm_b32 v133, v93, v92, %[sp]\n" \
  "v_mfma_f32_32x32x16_bf16 v[0:15], v[148:151], v[110:113], v[0:15]\n" \
  "v_exp_f32 v94, v94\n" \
  "v_exp_f32 v95, v95\n" \
  "v_exp_f32 v96, v96\n" \
  "v_exp_f32 v97, v97\n" \
  "v_perm_b32 v134, v95, v94, %[sp]\n" \
  "v_perm_b32 v135, v97, v96, %[sp]\n" \
  "v_mfma_f32_32x32x16_bf16 v[16:31], v[148:151], v[118:121], v[16:31]\n" \
  "v_exp_f32 v98, v98\n" \
  "v_exp_f32 v99, v99\n" \
  "v_exp_f32 v100, v100\n" \
  "v_exp_f32 v101, v101\n" \
  "v_perm_b32 v136, v99, v98, %[sp]\n" \
  "v_perm_b32 v137, v101, v100, %[sp]\n" \
  "v_mfma_f32_32x32x16_bf16 v[0:15], v[152:155], v[114:117], v[0:15]\n" \
  "v_exp_f32 v102, v102\n" \
  "v_exp_f32 v103, v103\n" \
  "v_exp_f32 v104, v104\n" \
  "v_exp_f32 v105, v105\n" \
  "v_perm_b32 v138, v103, v102, %[sp]\n" \
  "v_perm_b32 v139, v105, v104, %[sp]\n" \
  "v_mfma_f32_32x32x16_bf16 v[16:31], v[152:155], v[122:125], v[16:31]\n" \
  "v_exp_f32 v106, v106\n" \
  "v_exp_f32 v107, v107\n" \
  "v_exp_f32 v108, v108\n" \
  "v_exp_f32 v109, v109\n" \
  "v_perm_b32 v140, v107, v106, %[sp]\n" \
  "v_perm_b32 v141, v109, v108, %[sp]\n" \
  "ds_read_b32 v32, %[ga]\n" \
  "s_waitcnt lgkmcnt(0)\n" \
  "ds_read_b128 v[142:145], v44 offset:3584\n" \
  "ds_read_b64 v[146:147], v45 offset:3584\n" \
  "v_exp_f32 v46, v46\n" \
  "v_exp_f32 v47, v47\n" \
  "v_exp_f32 v48, v48\n" \
  "v_exp_f32 v49, v49\n" \
  "v_perm_b32 v110, v47, v46, %[sp]\n" \
  "v_perm_b32 v111, v49, v48, %[sp]\n" \
  "ds_read_b128 v[148:151], v44 offset:5120\n" \
  "ds_read_b128 v[152:155], v44 offset:6144\n" \
  "v_exp_f32 v50, v50\n" \
  "v_exp_f32 v51, v51\n" \
  "v_exp_f32 v52, v52\n" \
  "v_exp_f32 v53, v53\n" \
  "v_perm_b32 v112, v51, v50, %[sp]\n" \
  "v_perm_b32 v113, v53, v52, %[sp]\n" \
  "v_exp_f32 v54, v54\n" \
  "v_exp_f32 v55, v55\n" \
  "v_exp_f32 v56, v56\n" \
  "v_exp_f32 v57, v57\n" \
  "v_perm_b32 v114, v55, v54, %[sp]\n" \
  "v_perm_b32 v115, v57, v56, %[sp]\n" \
  "v_exp_f32 v58, v58\n" \
  "v_exp_f32 v59, v59\n" \
  "v_exp_f32 v60, v60\n" \
  "v_exp_f32 v61, v61\n" \
  "v_perm_b32 v116, v59, v58, %[sp]\n" \
  "v_perm_b32 v117, v61, v60, %[sp]\n" \
  "v_mfma_f32_32x32x16_bf16 v[0:15], v[162:165], v[126:129], v[0:15]\n" \
  "v_exp_f32 v62, v62\n" \
  "v_exp_f32 v63, v63\n" \
  "v_exp_f32 v64, v64\n" \
  "v_exp_f32 v65, v65\n" \
  "v_perm_b32 v118, v63, v62, %[sp]\n" \
  "v_perm_b32 v119, v65, v64, %[sp]\n" \
  "v_mfma_f32_32x32x16_bf16 v[16:31], v[162:165], v[134:137], v[16:31]\n" \
  "v_exp_f32 v66, v66\n" \
  "v_exp_f32 v67, v67\n" \
  "v_exp_f32 v68, v68\n" \
  "v_exp_f32 v69, v69\n" \
  "v_perm_b32 v120, v67, v66, %[sp]\n" \
  "v_perm_b32 v121, v69, v68, %[sp]\n" \
  "v_mfma_f32_32x32x16_bf16 v[0:15], v[166:169], v[130:133], v[0:15]\n" \
  "v_exp_f32 v70, v70\n" \
  "v_exp_f32 v71, v71\n" \
  "v_exp_f32 v72, v72\n" \
  "v_exp_f32 v73, v73\n" \
  "v_perm_b32 v122, v71, v70, %[sp]\n" \
  "v_perm_b32 v123, v73, v72, %[sp]\n" \
  "v_mfma_f32_32x32x16_bf16 v[16:31], v[166:169], v[138:141], v[16:31]\n" \
  "v_exp_f32 v74, v74\n" \
  "v_exp_f32 v75, v75\n" \
  "v_exp_f32 v76, v76\n" \
  "v_exp_f32 v77, v77\n" \
  "v_perm_b32 v124, v75, v74, %[sp]\n" \
  "v_perm_b32 v125, v77, v76, %[sp]\n" \
  "s_waitcnt lgkmcnt(0)\n" \
  "v_mfma_f32_32x32x16_bf16 v[0:15], v[148:151], v[110:113], v[0:15]\n" \
  "v_mfma_f32_32x32x16_bf16 v[16:31], v[148:151], v[118:121], v[16:31]\n" \
  "v_mfma_f32_32x32x16_bf16 v[0:15], v[152:155], v[114:117], v[0:15]\n" \
  "v_mfma_f32_32x32x16_bf16 v[16:31], v[152:155], v[122:125], v[16:31]\n" \
  "s_barrier\n" \
  "s_nop 10\n" \
  "s_branch 25f\n" \
  "22:\n" \
  "v_mov_b32 v48, 0xff800000\n" \
  "v_mov_b32 v49, 0xff800000\n" \
  "s_mov_b32 %[sp], 0\n" \
  "24:\n" \
  "buffer_load_dwordx4 v[52:55], v180, %[rs], %[sp] offen sc1\n" \
  "buffer_load_dwordx2 v[56:57], v181, %[rs], %[sp] offen sc1\n" \
  "s_waitcnt vmcnt(0)\n" \
  "v_mfma_f32_32x32x8bf16_1k v[62:77], v[56:57], v[40:41], 0\n" \
  "v_mfma_f32_32x32x8bf16_1k v[78:93], v[56:57], v[42:43], 0\n" \
  "v_mfma_f32_32x32x16_bf16 v[62:77], v[52:55], v[32:35], v[62:77]\n" \
  "v_mfma_f32_32x32x16_bf16 v[78:93], v[52:55], v[36:39], v[78:93]\n" \
  "s_add_u32 %[sp], %[sp], 3584\n" \
  "s_cmp_lg_u32 %[sp], 229376\n" \
  "s_nop 9\n" \
  "v_max3_f32 v48, v48, v62, v63\n" \
  "v_max3_f32 v48, v48, v64, v65\n" \
  "v_max3_f32 v48, v48, v66, v67\n" \
  "v_max3_f32 v48, v48, v68, v69\n" \
  "v_max3_f32 v48, v48, v70, v71\n" \
  "v_max3_f32 v48, v48, v72, v73\n" \
  "v_max3_f32 v48, v48, v74, v75\n" \
  "v_max3_f32 v48, v48, v76, v77\n" \
  "v_max3_f32 v49, v49, v78, v79\n" \
  "v_max3_f32 v49, v49, v80, v81\n" \
  "v_max3_f32 v49, v49, v82, v83\n" \
  "v_max3_f32 v49, v49, v84, v85\n" \
  "v_max3_f32 v49, v49, v86, v87\n" \
  "v_max3_f32 v49, v49, v88, v89\n" \
  "v_max3_f32 v49, v49, v90, v91\n" \
  "v_max3_f32 v49, v49, v92, v93\n" \
  "s_cbranch_scc1 24b\n" \
  "v_mov_b32 v50, v48\n" \
  "s_nop 1\n" \
  "v_permlane32_swap_b32 v48, v50\n" \
  "s_nop 0\n" \
  "v_max_f32 v48, v48, v50\n" \
  "v_sub_f32 v48, 0, v48\n" \
  "v_cvt_pk_bf16_f32 v50, v48, 0\n" \
  "v_lshlrev_b32 v51, 16, v50\n" \
  "v_sub_f32 v48, v48, v51\n" \
  "v_cvt_pk_bf16_f32 v51, v48, 0\n" \
  "v_lshlrev_b32 v51, 16, v51\n" \
  "v_sub_f32 v48, v48, v51\n" \
  "v_or_b32 v50, v50, v51\n" \
  "v_cvt_pk_bf16_f32 v51, v48, 0\n" \
  "v_cndmask_b32 v40, v50, v40, vcc\n" \
  "v_cndmask_b32 v41, v51, v41, vcc\n" \
  "v_mov_b32 v50, v49\n" \
  "s_nop 1\n" \
  "v_permlane32_swap_b32 v49, v50\n" \
  "s_nop 0\n" \
  "v_max_f32 v49, v49, v50\n" \
  "v_sub_f32 v49, 0, v49\n" \
  "v_cvt_pk_bf16_f32 v50, v49, 0\n" \
  "v_lshlrev_b32 v51, 16, v50\n" \
  "v_sub_f32 v49, v49, v51\n" \
  "v_cvt_pk_bf16_f32 v51, v49, 0\n" \
  "v_lshlrev_b32 v51, 16, v51\n" \
  "v_sub_f32 v49, v49, v51\n" \
  "v_or_b32 v50, v50, v51\n" \
  "v_cvt_pk_bf16_f32 v51, v49, 0\n" \
  "v_cndmask_b32 v42, v50, v42, vcc\n" \
  "v_cndmask_b32 v43, v51, v43, vcc\n" \
  "s_branch 23b\n" \
  "25:\n"
#define ATTN_CLOB "v33", "v34", "v35", "v36", "v37", "v38", "v39", "v40", "v41", "v42", "v43", "v46", "v47", "v48", "v49", "v50", "v51", "v52", "v53", "v54", "v55", "v56", "v57", "v58", "v59", "v60", "v61", "v62", "v63", "v64", "v65", "v66", "v67", "v68", "v69", "v70", "v71", "v72", "v73", "v74", "v75", "v76", "v77", "v78", "v79", "v80", "v81", "v82", "v83", "v84", "v85", "v86", "v87", "v88", "v89", "v90", "v91", "v92", "v93", "v94", "v95", "v96", "v97", "v98", "v99", "v100", "v101", "v102", "v103", "v104", "v105", "v106", "v107", "v108", "v109", "v110", "v111", "v112", "v113", "v114", "v115", "v116", "v117", "v118", "v119", "v120", "v121", "v122", "v123", "v124", "v125", "v126", "v127", "v128", "v129", "v130", "v131", "v132", "v133", "v134", "v135", "v136", "v137", "v138", "v139", "v140", "v141", "v142", "v143", "v144", "v145", "v146", "v147", "v148", "v149", "v150", "v151", "v152", "v153", "v154", "v155", "v156", "v157", "v158", "v159", "v160", "v161", "v162", "v163", "v164", "v165", "v166", "v167", "v168", "v169", "v170", "v171", "v172", "v173", "v174", "v175"
#define XS1_ASM \
  "v_lshrrev_b32 v92, 7, v116\n" \
  "v_lshrrev_b32 v93, 4, v116\n" \
  "v_lshrrev_b32 v94, 8, v116\n" \
  "v_xor_b32 v95, v93, v94\n" \
  "v_mul_u32_u24 v92, 1280, v92\n" \
  "v_and_b32 v95, 7, v95\n" \
  "v_lshl_add_u32 v100, v95, 4, v92\n" \
  "v_xor_b32 v101, 64, v100\n" \
  "v_add_u32 v102, 20480, v100\n" \
  "v_add_u32 v101, 10240, v101\n" \
  "v_add_u32 v103, 20480, v101\n" \
  "s_add_u32 m0, s56, 0\n" \
  "s_nop 0\n" \
  "global_load_lds_dwordx4 v100, s[40:41]\n" \
  "s_add_u32 m0, s56, 1024\n" \
  "s_nop 0\n" \
  "global_load_lds_dwordx4 v101, s[40:41]\n" \
  "s_add_u32 m0, s56, 2048\n" \
  "s_nop 0\n" \
  "global_load_lds_dwordx4 v102, s[40:41]\n" \
  "s_add_u32 m0, s56, 3072\n" \
  "s_nop 0\n" \
  "global_load_lds_dwordx4 v103, s[40:41]\n" \
  "v_bfe_u32 v92, v93, 1, 3\n" \
  "v_xor_b32 v92, v92, v94\n" \
  "v_and_b32 v95, 16, v116\n" \
  "v_lshl_or_b32 v92, v92, 5, v95\n" \
  "v_mul_u32_u24 v95, 3072, v94\n" \
  "v_add_u32 v104, v92, v95\n" \
  "v_xor_b32 v106, 128, v104\n" \
  "v_add_u32 v105, 12288, v104\n" \
  "v_add_u32 v106, 24576, v106\n" \
  "v_add_u32 v107, 12288, v106\n" \
  "s_add_u32 m0, s56, 16384\n" \
  "s_nop 0\n" \
  "global_load_lds_dwordx4 v104, s[42:43]\n" \
  "s_add_u32 m0, s56, 32768\n" \
  "s_nop 0\n" \
  "global_load_lds_dwordx4 v104, s[44:45]\n" \
  "s_add_u32 m0, s56, 17408\n" \
  "s_nop 0\n" \
  "global_load_lds_dwordx4 v105, s[42:43]\n" \
  "s_add_u32 m0, s56, 33792\n" \
  "s_nop 0\n" \
  "global_load_lds_dwordx4 v105, s[44:45]\n" \
  "s_add_u32 m0, s56, 18432\n" \
  "s_nop 0\n" \
  "global_load_lds_dwordx4 v106, s[42:43]\n" \
  "s_add_u32 m0, s56, 34816\n" \
  "s_nop 0\n" \
  "global_load_lds_dwordx4 v106, s[44:45]\n" \
  "s_add_u32 m0, s56, 19456\n" \
  "s_nop 0\n" \
  "global_load_lds_dwordx4 v107, s[42:43]\n" \
  "s_add_u32 m0, s56, 35840\n" \
  "s_nop 0\n" \
  "global_load_lds_dwordx4 v107, s[44:45]\n" \
  "s_add_u32 s76, s76, 1\n" \
  "s_cmp_eq_u32 s76, 10\n" \
  "s_cselect_b32 s77, -9, 1\n" \
  "s_cselect_b32 s78, -1, 0\n" \
  "s_cselect_b32 s76, 0, s76\n" \
  "s_mul_i32 s79, s77, 128\n" \
  "s_add_u32 s40, s40, s79\n" \
  "s_addc_u32 s41, s41, s78\n" \
  "s_mul_i32 s79, s77, 196608\n" \
  "s_add_u32 s42, s42, s79\n" \
  "s_addc_u32 s43, s43, s78\n" \
  "s_add_u32 s44, s44, s79\n" \
  "s_addc_u32 s45, s45, s78\n" \
  "v_mov_b32 v92, 0\n" \
  "global_load_dwordx2 v[120:121], v92, s[60:61] sc1\n" \
  "s_add_u32 m0, s56, 65536\n" \
  "s_nop 0\n" \
  "global_load_lds_dwordx4 v104, s[42:43]\n" \
  "s_add_u32 m0, s56, 81920\n" \
  "s_nop 0\n" \
  "global_load_lds_dwordx4 v104, s[44:45]\n" \
  "s_add_u32 m0, s56, 66560\n" \
  "s_nop 0\n" \
  "global_load_lds_dwordx4 v105, s[42:43]\n" \
  "s_add_u32 m0, s56, 82944\n" \
  "s_nop 0\n" \
  "global_load_lds_dwordx4 v105, s[44:45]\n" \
  "s_add_u32 m0, s56, 67584\n" \
  "s_nop 0\n" \
  "global_load_lds_dwordx4 v106, s[42:43]\n" \
  "s_add_u32 m0, s56, 83968\n" \
  "s_nop 0\n" \
  "global_load_lds_dwordx4 v106, s[44:45]\n" \
  "s_add_u32 m0, s56, 68608\n" \
  "s_nop 0\n" \
  "global_load_lds_dwordx4 v107, s[42:43]\n" \
  "s_waitcnt vmcnt(7)\n" \
  "s_cmp_lg_u32 s59, 0\n" \
  "s_cbranch_scc1 2f\n" \
  "v_add_co_u32 v96, vcc, 1, v120\n" \
  "v_mov_b32 v92, 0\n" \
  "v_mov_b32 v93, 0\n" \
  "v_addc_co_u32 v97, vcc, 0, v121, vcc\n" \
  "v_mov_b32 v94, 0\n" \
  "v_mov_b32 v95, 0\n" \
  "ds_write_b64 v119, v[96:97] offset:72\n" \
  "ds_write_b128 v119, v[92:95] offset:48\n" \
  "ds_write_b128 v119, v[92:95] offset:80\n" \
  "ds_write_b64 v119, v[92:93] offset:64\n" \
  "ds_write_b128 v119, v[92:95] offset:32\n" \
  "ds_write_b128 v119, v[92:95] offset:96\n" \
  "ds_write_b128 v119, v[92:95] offset:112\n" \
  "2:\n" \
  "s_waitcnt lgkmcnt(0)\n" \
  "s_barrier\n" \
  "s_add_u32 m0, s56, 84992\n" \
  "s_nop 0\n" \
  "global_load_lds_dwordx4 v107, s[44:45]\n" \
  "s_add_u32 m0, s56, 49152\n" \
  "s_nop 0\n" \
  "global_load_lds_dwordx4 v100, s[40:41]\n" \
  "s_add_u32 m0, s56, 50176\n" \
  "s_nop 0\n" \
  "global_load_lds_dwordx4 v101, s[40:41]\n" \
  "s_add_u32 m0, s56, 51200\n" \
  "s_nop 0\n" \
  "global_load_lds_dwordx4 v102, s[40:41]\n" \
  "s_add_u32 m0, s56, 52224\n" \
  "s_nop 0\n" \
  "global_load_lds_dwordx4 v103, s[40:41]\n" \
  "s_add_u32 s76, s76, 1\n" \
  "s_cmp_eq_u32 s76, 10\n" \
  "s_cselect_b32 s77, -9, 1\n" \
  "s_cselect_b32 s78, -1, 0\n" \
  "s_cselect_b32 s76, 0, s76\n" \
  "s_mul_i32 s79, s77, 128\n" \
  "s_add_u32 s40, s40, s79\n" \
  "s_addc_u32 s41, s41, s78\n" \
  "s_mul_i32 s79, s77, 196608\n" \
  "s_add_u32 s42, s42, s79\n" \
  "s_addc_u32 s43, s43, s78\n" \
  "s_add_u32 s44, s44, s79\n" \
  "s_addc_u32 s45, s45, s78\n" \
  "s_add_u32 m0, s56, 114688\n" \
  "s_nop 0\n" \
  "global_load_lds_dwordx4 v104, s[42:43]\n" \
  "s_add_u32 m0, s56, 131072\n" \
  "s_nop 0\n" \
  "global_load_lds_dwordx4 v104, s[44:45]\n" \
  "s_add_u32 m0, s56, 115712\n" \
  "s_nop 0\n" \
  "global_load_lds_dwordx4 v105, s[42:43]\n" \
  "s_add_u32 m0, s56, 132096\n" \
  "s_nop 0\n" \
  "global_load_lds_dwordx4 v105, s[44:45]\n" \
  "s_waitcnt vmcnt(4) lgkmcnt(0)\n" \
  "s_barrier\n" \
  "s_add_u32 m0, s56, 116736\n" \
  "s_nop 0\n" \
  "global_load_lds_dwordx4 v106, s[42:43]\n" \
  "s_add_u32 m0, s56, 133120\n" \
  "s_nop 0\n" \
  "global_load_lds_dwordx4 v106, s[44:45]\n" \
  "s_add_u32 m0, s56, 117760\n" \
  "s_nop 0\n" \
  "global_load_lds_dwordx4 v107, s[42:43]\n" \
  "s_add_u32 m0, s56, 134144\n" \
  "s_nop 0\n" \
  "global_load_lds_dwordx4 v107, s[44:45]\n" \
  "s_add_u32 m0, s56, 98304\n" \
  "s_nop 0\n" \
  "global_load_lds_dwordx4 v100, s[40:41]\n" \
  "s_add_u32 m0, s56, 99328\n" \
  "s_nop 0\n" \
  "global_load_lds_dwordx4 v101, s[40:41]\n" \
  "s_add_u32 m0, s56, 100352\n" \
  "s_nop 0\n" \
  "global_load_lds_dwordx4 v102, s[40:41]\n" \
  "s_add_u32 m0, s56, 101376\n" \
  "s_nop 0\n" \
  "global_load_lds_dwordx4 v103, s[40:41]\n" \
  "s_add_u32 s76, s76, 1\n" \
  "s_cmp_eq_u32 s76, 10\n" \
  "s_cselect_b32 s77, -9, 1\n" \
  "s_cselect_b32 s78, -1, 0\n" \
  "s_cselect_b32 s76, 0, s76\n" \
  "s_mul_i32 s79, s77, 128\n" \
  "s_add_u32 s40, s40, s79\n" \
  "s_addc_u32 s41, s41, s78\n" \
  "s_mul_i32 s79, s77, 196608\n" \
  "s_add_u32 s42, s42, s79\n" \
  "s_addc_u32 s43, s43, s78\n" \
  "s_add_u32 s44, s44, s79\n" \
  "s_addc_u32 s45, s45, s78\n" \
  "s_add_u32 m0, s56, 16384\n" \
  "s_nop 0\n" \
  "global_load_lds_dwordx4 v104, s[42:43]\n" \
  "s_add_u32 m0, s56, 32768\n" \
  "s_nop 0\n" \
  "global_load_lds_dwordx4 v104, s[44:45]\n" \
  "s_add_u32 m0, s56, 17408\n" \
  "s_nop 0\n" \
  "global_load_lds_dwordx4 v105, s[42:43]\n" \
  "s_add_u32 m0, s56, 33792\n" \
  "s_nop 0\n" \
  "global_load_lds_dwordx4 v105, s[44:45]\n" \
  "s_add_u32 m0, s56, 18432\n" \
  "s_nop 0\n" \
  "global_load_lds_dwordx4 v106, s[42:43]\n" \
  "s_add_u32 m0, s56, 34816\n" \
  "s_nop 0\n" \
  "global_load_lds_dwordx4 v106, s[44:45]\n" \
  "s_waitcnt vmcnt(6) lgkmcnt(0)\n" \
  "s_barrier\n" \
  "s_add_u32 m0, s56, 19456\n" \
  "s_nop 0\n" \
  "global_load_lds_dwordx4 v107, s[42:43]\n" \
  "s_add_u32 m0, s56, 35840\n" \
  "s_nop 0\n" \
  "global_load_lds_dwordx4 v107, s[44:45]\n" \
  "s_add_u32 m0, s56, 0\n" \
  "s_nop 0\n" \
  "global_load_lds_dwordx4 v100, s[40:41]\n" \
  "s_add_u32 m0, s56, 1024\n" \
  "s_nop 0\n" \
  "global_load_lds_dwordx4 v101, s[40:41]\n" \
  "s_add_u32 m0, s56, 2048\n" \
  "s_nop 0\n" \
  "global_load_lds_dwordx4 v102, s[40:41]\n" \
  "s_add_u32 m0, s56, 3072\n" \
  "s_nop 0\n" \
  "global_load_lds_dwordx4 v103, s[40:41]\n" \
  "s_add_u32 s76, s76, 1\n" \
  "s_cmp_eq_u32 s76, 10\n" \
  "s_cselect_b32 s77, -9, 1\n" \
  "s_cselect_b32 s78, -1, 0\n" \
  "s_cselect_b32 s76, 0, s76\n" \
  "s_mul_i32 s79, s77, 128\n" \
  "s_add_u32 s40, s40, s79\n" \
  "s_addc_u32 s41, s41, s78\n" \
  "s_mul_i32 s79, s77, 196608\n" \
  "s_add_u32 s42, s42, s79\n" \
  "s_addc_u32 s43, s43, s78\n" \
  "s_add_u32 s44, s44, s79\n" \
  "s_addc_u32 s45, s45, s78\n" \
  "s_add_u32 m0, s56, 65536\n" \
  "s_nop 0\n" \
  "global_load_lds_dwordx4 v104, s[42:43]\n" \
  "s_add_u32 m0, s56, 81920\n" \
  "s_nop 0\n" \
  "global_load_lds_dwordx4 v104, s[44:45]\n" \
  "s_add_u32 m0, s56, 66560\n" \
  "s_nop 0\n" \
  "global_load_lds_dwordx4 v105, s[42:43]\n" \
  "s_add_u32 m0, s56, 82944\n" \
  "s_nop 0\n" \
  "global_load_lds_dwordx4 v105, s[44:45]\n" \
  "s_add_u32 m0, s56, 67584\n" \
  "s_nop 0\n" \
  "global_load_lds_dwordx4 v106, s[42:43]\n" \
  "s_add_u32 m0, s56, 83968\n" \
  "s_nop 0\n" \
  "global_load_lds_dwordx4 v106, s[44:45]\n" \
  "s_add_u32 m0, s56, 68608\n" \
  "s_nop 0\n" \
  "global_load_lds_dwordx4 v107, s[42:43]\n" \
  "s_add_u32 m0, s56, 84992\n" \
  "s_nop 0\n" \
  "global_load_lds_dwordx4 v107, s[44:45]\n" \
  "s_add_u32 m0, s56, 49152\n" \
  "s_nop 0\n" \
  "global_load_lds_dwordx4 v100, s[40:41]\n" \
  "s_add_u32 m0, s56, 50176\n" \
  "s_nop 0\n" \
  "global_load_lds_dwordx4 v101, s[40:41]\n" \
  "s_add_u32 m0, s56, 51200\n" \
  "s_nop 0\n" \
  "global_load_lds_dwordx4 v102, s[40:41]\n" \
  "s_add_u32 m0, s56, 52224\n" \
  "s_nop 0\n" \
  "global_load_lds_dwordx4 v103, s[40:41]\n" \
  "s_add_u32 s76, s76, 1\n" \
  "s_cmp_eq_u32 s76, 10\n" \
  "s_cselect_b32 s77, -9, 1\n" \
  "s_cselect_b32 s78, -1, 0\n" \
  "s_cselect_b32 s76, 0, s76\n" \
  "s_mul_i32 s79, s77, 128\n" \
  "s_add_u32 s40, s40, s79\n" \
  "s_addc_u32 s41, s41, s78\n" \
  "s_mul_i32 s79, s77, 196608\n" \
  "s_add_u32 s42, s42, s79\n" \
  "s_addc_u32 s43, s43, s78\n" \
  "s_add_u32 s44, s44, s79\n" \
  "s_addc_u32 s45, s45, s78\n" \
  "s_waitcnt vmcnt(12) lgkmcnt(0)\n" \
  "s_barrier\n" \
  "s_add_u32 m0, s56, 114688\n" \
  "s_nop 0\n" \
  "global_load_lds_dwordx4 v104, s[42:43]\n" \
  "s_add_u32 m0, s56, 131072\n" \
  "s_nop 0\n" \
  "global_load_lds_dwordx4 v104, s[44:45]\n" \
  "s_add_u32 m0, s56, 115712\n" \
  "s_nop 0\n" \
  "global_load_lds_dwordx4 v105, s[42:43]\n" \
  "s_add_u32 m0, s56, 132096\n" \
  "s_nop 0\n" \
  "global_load_lds_dwordx4 v105, s[44:45]\n" \
  "s_add_u32 m0, s56, 116736\n" \
  "s_nop 0\n" \
  "global_load_lds_dwordx4 v106, s[42:43]\n" \
  "s_add_u32 m0, s56, 133120\n" \
  "s_nop 0\n" \
  "global_load_lds_dwordx4 v106, s[44:45]\n" \
  "s_add_u32 m0, s56, 117760\n" \
  "s_nop 0\n" \
  "global_load_lds_dwordx4 v107, s[42:43]\n" \
  "s_add_u32 m0, s56, 134144\n" \
  "s_nop 0\n" \
  "global_load_lds_dwordx4 v107, s[44:45]\n" \
  "s_add_u32 m0, s56, 98304\n" \
  "s_nop 0\n" \
  "global_load_lds_dwordx4 v100, s[40:41]\n" \
  "s_add_u32 m0, s56, 99328\n" \
  "s_nop 0\n" \
  "global_load_lds_dwordx4 v101, s[40:41]\n" \
  "s_add_u32 m0, s56, 100352\n" \
  "s_nop 0\n" \
  "global_load_lds_dwordx4 v102, s[40:41]\n" \
  "s_add_u32 m0, s56, 101376\n" \
  "s_nop 0\n" \
  "global_load_lds_dwordx4 v103, s[40:41]\n" \
  "s_add_u32 s76, s76, 1\n" \
  "s_cmp_eq_u32 s76, 10\n" \
  "s_cselect_b32 s77, -9, 1\n" \
  "s_cselect_b32 s78, -1, 0\n" \
  "s_cselect_b32 s76, 0, s76\n" \
  "s_mul_i32 s79, s77, 128\n" \
  "s_add_u32 s40, s40, s79\n" \
  "s_addc_u32 s41, s41, s78\n" \
  "s_mul_i32 s79, s77, 196608\n" \
  "s_add_u32 s42, s42, s79\n" \
  "s_addc_u32 s43, s43, s78\n" \
  "s_add_u32 s44, s44, s79\n" \
  "s_addc_u32 s45, s45, s78\n" \
  "s_waitcnt vmcnt(12) lgkmcnt(0)\n" \
  "s_barrier\n" \
  "s_add_u32 m0, s56, 16384\n" \
  "s_nop 0\n" \
  "global_load_lds_dwordx4 v104, s[42:43]\n" \
  "s_add_u32 m0, s56, 32768\n" \
  "s_nop 0\n" \
  "global_load_lds_dwordx4 v104, s[44:45]\n" \
  "s_add_u32 m0, s56, 17408\n" \
  "s_nop 0\n" \
  "global_load_lds_dwordx4 v105, s[42:43]\n" \
  "s_add_u32 m0, s56, 33792\n" \
  "s_nop 0\n" \
  "global_load_lds_dwordx4 v105, s[44:45]\n" \
  "s_add_u32 m0, s56, 18432\n" \
  "s_nop 0\n" \
  "global_load_lds_dwordx4 v106, s[42:43]\n" \
  "s_add_u32 m0, s56, 34816\n" \
  "s_nop 0\n" \
  "global_load_lds_dwordx4 v106, s[44:45]\n" \
  "s_add_u32 m0, s56, 19456\n" \
  "s_nop 0\n" \
  "global_load_lds_dwordx4 v107, s[42:43]\n" \
  "s_add_u32 m0, s56, 35840\n" \
  "s_nop 0\n" \
  "global_load_lds_dwordx4 v107, s[44:45]\n" \
  "s_add_u32 m0, s56, 0\n" \
  "s_nop 0\n" \
  "global_load_lds_dwordx4 v100, s[40:41]\n" \
  "s_add_u32 m0, s56, 1024\n" \
  "s_nop 0\n" \
  "global_load_lds_dwordx4 v101, s[40:41]\n" \
  "s_add_u32 m0, s56, 2048\n" \
  "s_nop 0\n" \
  "global_load_lds_dwordx4 v102, s[40:41]\n" \
  "s_add_u32 m0, s56, 3072\n" \
  "s_nop 0\n" \
  "global_load_lds_dwordx4 v103, s[40:41]\n" \
  "s_add_u32 s76, s76, 1\n" \
  "s_cmp_eq_u32 s76, 10\n" \
  "s_cselect_b32 s77, -9, 1\n" \
  "s_cselect_b32 s78, -1, 0\n" \
  "s_cselect_b32 s76, 0, s76\n" \
  "s_mul_i32 s79, s77, 128\n" \
  "s_add_u32 s40, s40, s79\n" \
  "s_addc_u32 s41, s41, s78\n" \
  "s_mul_i32 s79, s77, 196608\n" \
  "s_add_u32 s42, s42, s79\n" \
  "s_addc_u32 s43, s43, s78\n" \
  "s_add_u32 s44, s44, s79\n" \
  "s_addc_u32 s45, s45, s78\n" \
  "s_waitcnt vmcnt(12) lgkmcnt(0)\n" \
  "s_barrier\n" \
  "s_add_u32 m0, s56, 65536\n" \
  "s_nop 0\n" \
  "global_load_lds_dwordx4 v104, s[42:43]\n" \
  "s_add_u32 m0, s56, 81920\n" \
  "s_nop 0\n" \
  "global_load_lds_dwordx4 v104, s[44:45]\n" \
  "s_add_u32 m0, s56, 66560\n" \
  "s_nop 0\n" \
  "global_load_lds_dwordx4 v105, s[42:43]\n" \
  "s_add_u32 m0, s56, 82944\n" \
  "s_nop 0\n" \
  "global_load_lds_dwordx4 v105, s[44:45]\n" \
  "s_add_u32 m0, s56, 67584\n" \
  "s_nop 0\n" \
  "global_load_lds_dwordx4 v106, s[42:43]\n" \
  "s_add_u32 m0, s56, 83968\n" \
  "s_nop 0\n" \
  "global_load_lds_dwordx4 v106, s[44:45]\n" \
  "s_add_u32 m0, s56, 68608\n" \
  "s_nop 0\n" \
  "global_load_lds_dwordx4 v107, s[42:43]\n" \
  "s_add_u32 m0, s56, 84992\n" \
  "s_nop 0\n" \
  "global_load_lds_dwordx4 v107, s[44:45]\n" \
  "s_add_u32 m0, s56, 49152\n" \
  "s_nop 0\n" \
  "global_load_lds_dwordx4 v100, s[40:41]\n" \
  "s_add_u32 m0, s56, 50176\n" \
  "s_nop 0\n" \
  "global_load_lds_dwordx4 v101, s[40:41]\n" \
  "s_add_u32 m0, s56, 51200\n" \
  "s_nop 0\n" \
  "global_load_lds_dwordx4 v102, s[40:41]\n" \
  "s_add_u32 m0, s56, 52224\n" \
  "s_nop 0\n" \
  "global_load_lds_dwordx4 v103, s[40:41]\n" \
  "s_add_u32 s76, s76, 1\n" \
  "s_cmp_eq_u32 s76, 10\n" \
  "s_cselect_b32 s77, -9, 1\n" \
  "s_cselect_b32 s78, -1, 0\n" \
  "s_cselect_b32 s76, 0, s76\n" \
  "s_mul_i32 s79, s77, 128\n" \
  "s_add_u32 s40, s40, s79\n" \
  "s_addc_u32 s41, s41, s78\n" \
  "s_mul_i32 s79, s77, 196608\n" \
  "s_add_u32 s42, s42, s79\n" \
  "s_addc_u32 s43, s43, s78\n" \
  "s_add_u32 s44, s44, s79\n" \
  "s_addc_u32 s45, s45, s78\n" \
  "s_waitcnt vmcnt(12) lgkmcnt(0)\n" \
  "s_barrier\n" \
  "s_add_u32 m0, s56, 114688\n" \
  "s_nop 0\n" \
  "global_load_lds_dwordx4 v104, s[42:43]\n" \
  "s_add_u32 m0, s56, 131072\n" \
  "s_nop 0\n" \
  "global_load_lds_dwordx4 v104, s[44:45]\n" \
  "s_add_u32 m0, s56, 115712\n" \
  "s_nop 0\n" \
  "global_load_lds_dwordx4 v105, s[42:43]\n" \
  "s_add_u32 m0, s56, 132096\n" \
  "s_nop 0\n" \
  "global_load_lds_dwordx4 v105, s[44:45]\n" \
  "s_add_u32 m0, s56, 116736\n" \
  "s_nop 0\n" \
  "global_load_lds_dwordx4 v106, s[42:43]\n" \
  "s_add_u32 m0, s56, 133120\n" \
  "s_nop 0\n" \
  "global_load_lds_dwordx4 v106, s[44:45]\n" \
  "s_add_u32 m0, s56, 117760\n" \
  "s_nop 0\n" \
  "global_load_lds_dwordx4 v107, s[42:43]\n" \
  "s_add_u32 m0, s56, 134144\n" \
  "s_nop 0\n" \
  "global_load_lds_dwordx4 v107, s[44:45]\n" \
  "s_add_u32 m0, s56, 98304\n" \
  "s_nop 0\n" \
  "global_load_lds_dwordx4 v100, s[40:41]\n" \
  "s_add_u32 m0, s56, 99328\n" \
  "s_nop 0\n" \
  "global_load_lds_dwordx4 v101, s[40:41]\n" \
  "s_add_u32 m0, s56, 100352\n" \
  "s_nop 0\n" \
  "global_load_lds_dwordx4 v102, s[40:41]\n" \
  "s_add_u32 m0, s56, 101376\n" \
  "s_nop 0\n" \
  "global_load_lds_dwordx4 v103, s[40:41]\n" \
  "s_add_u32 s76, s76, 1\n" \
  "s_cmp_eq_u32 s76, 10\n" \
  "s_cselect_b32 s77, -9, 1\n" \
  "s_cselect_b32 s78, -1, 0\n" \
  "s_cselect_b32 s76, 0, s76\n" \
  "s_mul_i32 s79, s77, 128\n" \
  "s_add_u32 s40, s40, s79\n" \
  "s_addc_u32 s41, s41, s78\n" \
  "s_mul_i32 s79, s77, 196608\n" \
  "s_add_u32 s42, s42, s79\n" \
  "s_addc_u32 s43, s43, s78\n" \
  "s_add_u32 s44, s44, s79\n" \
  "s_addc_u32 s45, s45, s78\n" \
  "s_waitcnt vmcnt(12) lgkmcnt(0)\n" \
  "s_barrier\n" \
  "s_add_u32 m0, s56, 16384\n" \
  "s_nop 0\n" \
  "global_load_lds_dwordx4 v104, s[42:43]\n" \
  "s_add_u32 m0, s56, 32768\n" \
  "s_nop 0\n" \
  "global_load_lds_dwordx4 v104, s[44:45]\n" \
  "s_add_u32 m0, s56, 17408\n" \
  "s_nop 0\n" \
  "global_load_lds_dwordx4 v105, s[42:43]\n" \
  "s_add_u32 m0, s56, 33792\n" \
  "s_nop 0\n" \
  "global_load_lds_dwordx4 v105, s[44:45]\n" \
  "s_add_u32 m0, s56, 18432\n" \
  "s_nop 0\n" \
  "global_load_lds_dwordx4 v106, s[42:43]\n" \
  "s_add_u32 m0, s56, 34816\n" \
  "s_nop 0\n" \
  "global_load_lds_dwordx4 v106, s[44:45]\n" \
  "s_add_u32 m0, s56, 19456\n" \
  "s_nop 0\n" \
  "global_load_lds_dwordx4 v107, s[42:43]\n" \
  "s_add_u32 m0, s56, 35840\n" \
  "s_nop 0\n" \
  "global_load_lds_dwordx4 v107, s[44:45]\n" \
  "s_add_u32 m0, s56, 0\n" \
  "s_nop 0\n" \
  "global_load_lds_dwordx4 v100, s[40:41]\n" \
  "s_add_u32 m0, s56, 1024\n" \
  "s_nop 0\n" \
  "global_load_lds_dwordx4 v101, s[40:41]\n" \
  "s_add_u32 m0, s56, 2048\n" \
  "s_nop 0\n" \
  "global_load_lds_dwordx4 v102, s[40:41]\n" \
  "s_add_u32 m0, s56, 3072\n" \
  "s_nop 0\n" \
  "global_load_lds_dwordx4 v103, s[40:41]\n" \
  "s_add_u32 s76, s76, 1\n" \
  "s_cmp_eq_u32 s76, 10\n" \
  "s_cselect_b32 s77, -9, 1\n" \
  "s_cselect_b32 s78, -1, 0\n" \
  "s_cselect_b32 s76, 0, s76\n" \
  "s_mul_i32 s79, s77, 128\n" \
  "s_add_u32 s40, s40, s79\n" \
  "s_addc_u32 s41, s41, s78\n" \
  "s_mul_i32 s79, s77, 196608\n" \
  "s_add_u32 s42, s42, s79\n" \
  "s_addc_u32 s43, s43, s78\n" \
  "s_add_u32 s44, s44, s79\n" \
  "s_addc_u32 s45, s45, s78\n" \
  "s_waitcnt vmcnt(12) lgkmcnt(0)\n" \
  "s_barrier\n" \
  "s_waitcnt vmcnt(0) lgkmcnt(0)\n" \
  "s_barrier\n"
#define XS1D_ASM \
  "v_lshrrev_b32 v92, 7, v116\n" \
  "v_lshrrev_b32 v93, 4, v116\n" \
  "v_lshrrev_b32 v94, 8, v116\n" \
  "v_xor_b32 v95, v93, v94\n" \
  "v_mul_u32_u24 v92, 1280, v92\n" \
  "v_and_b32 v95, 7, v95\n" \
  "v_lshl_add_u32 v100, v95, 4, v92\n" \
  "v_xor_b32 v101, 64, v100\n" \
  "v_add_u32 v102, 20480, v100\n" \
  "v_add_u32 v101, 10240, v101\n" \
  "v_add_u32 v103, 20480, v101\n" \
  "s_add_u32 m0, s56, 0\n" \
  "s_nop 0\n" \
  "global_load_lds_dwordx4 v100, s[40:41]\n" \
  "s_add_u32 m0, s56, 1024\n" \
  "s_nop 0\n" \
  "global_load_lds_dwordx4 v101, s[40:41]\n" \
  "s_add_u32 m0, s56, 2048\n" \
  "s_nop 0\n" \
  "global_load_lds_dwordx4 v102, s[40:41]\n" \
  "s_add_u32 m0, s56, 3072\n" \
  "s_nop 0\n" \
  "global_load_lds_dwordx4 v103, s[40:41]\n" \
  "s_mul_i32 s77, s59, 320\n" \
  "v_add_u32 v92, s77, v93\n" \
  "s_movk_i32 s78, 3277\n" \
  "s_movk_i32 s79, 960\n" \
  "v_add_u32 v94, 0, v92\n" \
  "v_mul_u32_u24 v95, s78, v94\n" \
  "v_lshrrev_b32 v95, 16, v95\n" \
  "v_lshlrev_b32 v94, 4, v94\n" \
  "v_mad_u32_u24 v104, v95, s79, v94\n" \
  "v_add_u32 v94, 64, v92\n" \
  "v_mul_u32_u24 v95, s78, v94\n" \
  "v_lshrrev_b32 v95, 16, v95\n" \
  "v_lshlrev_b32 v94, 4, v94\n" \
  "v_mad_u32_u24 v105, v95, s79, v94\n" \
  "v_add_u32 v94, 128, v92\n" \
  "v_mul_u32_u24 v95, s78, v94\n" \
  "v_lshrrev_b32 v95, 16, v95\n" \
  "v_lshlrev_b32 v94, 4, v94\n" \
  "v_mad_u32_u24 v106, v95, s79, v94\n" \
  "v_add_u32 v94, 192, v92\n" \
  "v_mul_u32_u24 v95, s78, v94\n" \
  "v_lshrrev_b32 v95, 16, v95\n" \
  "v_lshlrev_b32 v94, 4, v94\n" \
  "v_mad_u32_u24 v107, v95, s79, v94\n" \
  "v_add_u32 v94, 256, v92\n" \
  "v_mul_u32_u24 v95, s78, v94\n" \
  "v_lshrrev_b32 v95, 16, v95\n" \
  "v_lshlrev_b32 v94, 4, v94\n" \
  "v_mad_u32_u24 v108, v95, s79, v94\n" \
  "s_add_u32 m0, s57, 16384\n" \
  "s_nop 0\n" \
  "global_load_lds_dwordx4 v104, s[42:43]\n" \
  "s_add_u32 m0, s57, 17408\n" \
  "s_nop 0\n" \
  "global_load_lds_dwordx4 v105, s[42:43]\n" \
  "s_add_u32 m0, s57, 18432\n" \
  "s_nop 0\n" \
  "global_load_lds_dwordx4 v106, s[42:43]\n" \
  "s_add_u32 m0, s57, 19456\n" \
  "s_nop 0\n" \
  "global_load_lds_dwordx4 v107, s[42:43]\n" \
  "s_add_u32 m0, s57, 20480\n" \
  "s_nop 0\n" \
  "global_load_lds_dwordx4 v108, s[42:43]\n" \
  "s_cmp_eq_u32 s59, 3\n" \
  "s_cbranch_scc1 21f\n" \
  "s_add_u32 m0, s58, 36864\n" \
  "s_nop 0\n" \
  "global_load_lds_dwordx4 v109, s[44:45]\n" \
  "21:\n" \
  "s_add_u32 s76, s76, 1\n" \
  "s_cmp_eq_u32 s76, 10\n" \
  "s_cselect_b32 s77, -9, 1\n" \
  "s_cselect_b32 s78, -1, 0\n" \
  "s_cselect_b32 s76, 0, s76\n" \
  "s_mul_i32 s79, s77, 128\n" \
  "s_add_u32 s40, s40, s79\n" \
  "s_addc_u32 s41, s41, s78\n" \
  "s_mul_i32 s79, s77, 81920\n" \
  "s_add_u32 s42, s42, s79\n" \
  "s_addc_u32 s43, s43, s78\n" \
  "s_mul_i32 s79, s77, s46\n" \
  "s_add_u32 s44, s44, s79\n" \
  "s_addc_u32 s45, s45, s78\n" \
  "v_mov_b32 v92, 0\n" \
  "global_load_dwordx2 v[120:121], v92, s[60:61] sc1\n" \
  "s_add_u32 m0, s57, 65536\n" \
  "s_nop 0\n" \
  "global_load_lds_dwordx4 v104, s[42:43]\n" \
  "s_add_u32 m0, s57, 66560\n" \
  "s_nop 0\n" \
  "global_load_lds_dwordx4 v105, s[42:43]\n" \
  "s_add_u32 m0, s57, 67584\n" \
  "s_nop 0\n" \
  "global_load_lds_dwordx4 v106, s[42:43]\n" \
  "s_add_u32 m0, s57, 68608\n" \
  "s_nop 0\n" \
  "global_load_lds_dwordx4 v107, s[42:43]\n" \
  "s_add_u32 m0, s57, 69632\n" \
  "s_nop 0\n" \
  "global_load_lds_dwordx4 v108, s[42:43]\n" \
  "s_add_u32 m0, s56, 49152\n" \
  "s_nop 0\n" \
  "global_load_lds_dwordx4 v100, s[40:41]\n" \
  "s_add_u32 m0, s56, 50176\n" \
  "s_nop 0\n" \
  "global_load_lds_dwordx4 v101, s[40:41]\n" \
  "s_waitcnt vmcnt(7)\n" \
  "s_cmp_lg_u32 s59, 0\n" \
  "s_cbranch_scc1 2f\n" \
  "v_add_co_u32 v96, vcc, 1, v120\n" \
  "v_mov_b32 v92, 0\n" \
  "v_mov_b32 v93, 0\n" \
  "v_addc_co_u32 v97, vcc, 0, v121, vcc\n" \
  "v_mov_b32 v94, 0\n" \
  "v_mov_b32 v95, 0\n" \
  "ds_write_b64 v119, v[96:97] offset:72\n" \
  "ds_write_b128 v119, v[92:95] offset:48\n" \
  "ds_write_b128 v119, v[92:95] offset:80\n" \
  "ds_write_b64 v119, v[92:93] offset:64\n" \
  "ds_write_b128 v119, v[92:95] offset:32\n" \
  "ds_write_b128 v119, v[92:95] offset:96\n" \
  "ds_write_b128 v119, v[92:95] offset:112\n" \
  "2:\n" \
  "s_waitcnt lgkmcnt(0)\n" \
  "s_barrier\n" \
  "s_add_u32 m0, s56, 51200\n" \
  "s_nop 0\n" \
  "global_load_lds_dwordx4 v102, s[40:41]\n" \
  "s_add_u32 m0, s56, 52224\n" \
  "s_nop 0\n" \
  "global_load_lds_dwordx4 v103, s[40:41]\n" \
  "s_cmp_eq_u32 s59, 3\n" \
  "s_cbranch_scc1 23f\n" \
  "s_add_u32 m0, s58, 86016\n" \
  "s_nop 0\n" \
  "global_load_lds_dwordx4 v109, s[44:45]\n" \
  "23:\n" \
  "s_add_u32 s76, s76, 1\n" \
  "s_cmp_eq_u32 s76, 10\n" \
  "s_cselect_b32 s77, -9, 1\n" \
  "s_cselect_b32 s78, -1, 0\n" \
  "s_cselect_b32 s76, 0, s76\n" \
  "s_mul_i32 s79, s77, 128\n" \
  "s_add_u32 s40, s40, s79\n" \
  "s_addc_u32 s41, s41, s78\n" \
  "s_mul_i32 s79, s77, 81920\n" \
  "s_add_u32 s42, s42, s79\n" \
  "s_addc_u32 s43, s43, s78\n" \
  "s_mul_i32 s79, s77, s46\n" \
  "s_add_u32 s44, s44, s79\n" \
  "s_addc_u32 s45, s45, s78\n" \
  "s_add_u32 m0, s57, 114688\n" \
  "s_nop 0\n" \
  "global_load_lds_dwordx4 v104, s[42:43]\n" \
  "s_add_u32 m0, s57, 115712\n" \
  "s_nop 0\n" \
  "global_load_lds_dwordx4 v105, s[42:43]\n" \
  "s_add_u32 m0, s57, 116736\n" \
  "s_nop 0\n" \
  "global_load_lds_dwordx4 v106, s[42:43]\n" \
  "s_add_u32 m0, s57, 117760\n" \
  "s_nop 0\n" \
  "global_load_lds_dwordx4 v107, s[42:43]\n" \
  "s_waitcnt vmcnt(4) lgkmcnt(0)\n" \
  "s_barrier\n" \
  "s_add_u32 m0, s57, 118784\n" \
  "s_nop 0\n" \
  "global_load_lds_dwordx4 v108, s[42:43]\n" \
  "s_add_u32 m0, s56, 98304\n" \
  "s_nop 0\n" \
  "global_load_lds_dwordx4 v100, s[40:41]\n" \
  "s_add_u32 m0, s56, 99328\n" \
  "s_nop 0\n" \
  "global_load_lds_dwordx4 v101, s[40:41]\n" \
  "s_add_u32 m0, s56, 100352\n" \
  "s_nop 0\n" \
  "global_load_lds_dwordx4 v102, s[40:41]\n" \
  "s_add_u32 m0, s56, 101376\n" \
  "s_nop 0\n" \
  "global_load_lds_dwordx4 v103, s[40:41]\n" \
  "s_cmp_eq_u32 s59, 3\n" \
  "s_cbranch_scc1 25f\n" \
  "s_add_u32 m0, s58, 135168\n" \
  "s_nop 0\n" \
  "global_load_lds_dwordx4 v109, s[44:45]\n" \
  "25:\n" \
  "s_add_u32 s76, s76, 1\n" \
  "s_cmp_eq_u32 s76, 10\n" \
  "s_cselect_b32 s77, -9, 1\n" \
  "s_cselect_b32 s78, -1, 0\n" \
  "s_cselect_b32 s76, 0, s76\n" \
  "s_mul_i32 s79, s77, 128\n" \
  "s_add_u32 s40, s40, s79\n" \
  "s_addc_u32 s41, s41, s78\n" \
  "s_mul_i32 s79, s77, 81920\n" \
  "s_add_u32 s42, s42, s79\n" \
  "s_addc_u32 s43, s43, s78\n" \
  "s_mul_i32 s79, s77, s46\n" \
  "s_add_u32 s44, s44, s79\n" \
  "s_addc_u32 s45, s45, s78\n" \
  "s_add_u32 m0, s57, 16384\n" \
  "s_nop 0\n" \
  "global_load_lds_dwordx4 v104, s[42:43]\n" \
  "s_add_u32 m0, s57, 17408\n" \
  "s_nop 0\n" \
  "global_load_lds_dwordx4 v105, s[42:43]\n" \
  "s_add_u32 m0, s57, 18432\n" \
  "s_nop 0\n" \
  "global_load_lds_dwordx4 v106, s[42:43]\n" \
  "s_add_u32 m0, s57, 19456\n" \
  "s_nop 0\n" \
  "global_load_lds_dwordx4 v107, s[42:43]\n" \
  "s_add_u32 m0, s57, 20480\n" \
  "s_nop 0\n" \
  "global_load_lds_dwordx4 v108, s[42:43]\n" \
  "s_add_u32 m0, s56, 0\n" \
  "s_nop 0\n" \
  "global_load_lds_dwordx4 v100, s[40:41]\n" \
  "s_waitcnt vmcnt(6) lgkmcnt(0)\n" \
  "s_barrier\n" \
  "s_add_u32 m0, s56, 1024\n" \
  "s_nop 0\n" \
  "global_load_lds_dwordx4 v101, s[40:41]\n" \
  "s_add_u32 m0, s56, 2048\n" \
  "s_nop 0\n" \
  "global_load_lds_dwordx4 v102, s[40:41]\n" \
  "s_add_u32 m0, s56, 3072\n" \
  "s_nop 0\n" \
  "global_load_lds_dwordx4 v103, s[40:41]\n" \
  "s_cmp_eq_u32 s59, 3\n" \
  "s_cbranch_scc1 27f\n" \
  "s_add_u32 m0, s58, 36864\n" \
  "s_nop 0\n" \
  "global_load_lds_dwordx4 v109, s[44:45]\n" \
  "27:\n" \
  "s_add_u32 s76, s76, 1\n" \
  "s_cmp_eq_u32 s76, 10\n" \
  "s_cselect_b32 s77, -9, 1\n" \
  "s_cselect_b32 s78, -1, 0\n" \
  "s_cselect_b32 s76, 0, s76\n" \
  "s_mul_i32 s79, s77, 128\n" \
  "s_add_u32 s40, s40, s79\n" \
  "s_addc_u32 s41, s41, s78\n" \
  "s_mul_i32 s79, s77, 81920\n" \
  "s_add_u32 s42, s42, s79\n" \
  "s_addc_u32 s43, s43, s78\n" \
  "s_mul_i32 s79, s77, s46\n" \
  "s_add_u32 s44, s44, s79\n" \
  "s_addc_u32 s45, s45, s78\n" \
  "s_add_u32 m0, s57, 65536\n" \
  "s_nop 0\n" \
  "global_load_lds_dwordx4 v104, s[42:43]\n" \
  "s_add_u32 m0, s57, 66560\n" \
  "s_nop 0\n" \
  "global_load_lds_dwordx4 v105, s[42:43]\n" \
  "s_add_u32 m0, s57, 67584\n" \
  "s_nop 0\n" \
  "global_load_lds_dwordx4 v106, s[42:43]\n" \
  "s_add_u32 m0, s57, 68608\n" \
  "s_nop 0\n" \
  "global_load_lds_dwordx4 v107, s[42:43]\n" \
  "s_add_u32 m0, s57, 69632\n" \
  "s_nop 0\n" \
  "global_load_lds_dwordx4 v108, s[42:43]\n" \
  "s_add_u32 m0, s56, 49152\n" \
  "s_nop 0\n" \
  "global_load_lds_dwordx4 v100, s[40:41]\n" \
  "s_add_u32 m0, s56, 50176\n" \
  "s_nop 0\n" \
  "global_load_lds_dwordx4 v101, s[40:41]\n" \
  "s_add_u32 m0, s56, 51200\n" \
  "s_nop 0\n" \
  "global_load_lds_dwordx4 v102, s[40:41]\n" \
  "s_add_u32 m0, s56, 52224\n" \
  "s_nop 0\n" \
  "global_load_lds_dwordx4 v103, s[40:41]\n" \
  "s_cmp_eq_u32 s59, 3\n" \
  "s_cbranch_scc1 28f\n" \
  "s_add_u32 m0, s58, 86016\n" \
  "s_nop 0\n" \
  "global_load_lds_dwordx4 v109, s[44:45]\n" \
  "28:\n" \
  "s_add_u32 s76, s76, 1\n" \
  "s_cmp_eq_u32 s76, 10\n" \
  "s_cselect_b32 s77, -9, 1\n" \
  "s_cselect_b32 s78, -1, 0\n" \
  "s_cselect_b32 s76, 0, s76\n" \
  "s_mul_i32 s79, s77, 128\n" \
  "s_add_u32 s40, s40, s79\n" \
  "s_addc_u32 s41, s41, s78\n" \
  "s_mul_i32 s79, s77, 81920\n" \
  "s_add_u32 s42, s42, s79\n" \
  "s_addc_u32 s43, s43, s78\n" \
  "s_mul_i32 s79, s77, s46\n" \
  "s_add_u32 s44, s44, s79\n" \
  "s_addc_u32 s45, s45, s78\n" \
  "s_waitcnt vmcnt(9) lgkmcnt(0)\n" \
  "s_barrier\n" \
  "s_add_u32 m0, s57, 114688\n" \
  "s_nop 0\n" \
  "global_load_lds_dwordx4 v104, s[42:43]\n" \
  "s_add_u32 m0, s57, 115712\n" \
  "s_nop 0\n" \
  "global_load_lds_dwordx4 v105, s[42:43]\n" \
  "s_add_u32 m0, s57, 116736\n" \
  "s_nop 0\n" \
  "global_load_lds_dwordx4 v106, s[42:43]\n" \
  "s_add_u32 m0, s57, 117760\n" \
  "s_nop 0\n" \
  "global_load_lds_dwordx4 v107, s[42:43]\n" \
  "s_add_u32 m0, s57, 118784\n" \
  "s_nop 0\n" \
  "global_load_lds_dwordx4 v108, s[42:43]\n" \
  "s_add_u32 m0, s56, 98304\n" \
  "s_nop 0\n" \
  "global_load_lds_dwordx4 v100, s[40:41]\n" \
  "s_add_u32 m0, s56, 99328\n" \
  "s_nop 0\n" \
  "global_load_lds_dwordx4 v101, s[40:41]\n" \
  "s_add_u32 m0, s56, 100352\n" \
  "s_nop 0\n" \
  "global_load_lds_dwordx4 v102, s[40:41]\n" \
  "s_add_u32 m0, s56, 101376\n" \
  "s_nop 0\n" \
  "global_load_lds_dwordx4 v103, s[40:41]\n" \
  "s_cmp_eq_u32 s59, 3\n" \
  "s_cbranch_scc1 30f\n" \
  "s_add_u32 m0, s58, 135168\n" \
  "s_nop 0\n" \
  "global_load_lds_dwordx4 v109, s[44:45]\n" \
  "30:\n" \
  "s_add_u32 s76, s76, 1\n" \
  "s_cmp_eq_u32 s76, 10\n" \
  "s_cselect_b32 s77, -9, 1\n" \
  "s_cselect_b32 s78, -1, 0\n" \
  "s_cselect_b32 s76, 0, s76\n" \
  "s_mul_i32 s79, s77, 128\n" \
  "s_add_u32 s40, s40, s79\n" \
  "s_addc_u32 s41, s41, s78\n" \
  "s_mul_i32 s79, s77, 81920\n" \
  "s_add_u32 s42, s42, s79\n" \
  "s_addc_u32 s43, s43, s78\n" \
  "s_mul_i32 s79, s77, s46\n" \
  "s_add_u32 s44, s44, s79\n" \
  "s_addc_u32 s45, s45, s78\n" \
  "s_waitcnt vmcnt(9) lgkmcnt(0)\n" \
  "s_barrier\n" \
  "s_add_u32 m0, s57, 16384\n" \
  "s_nop 0\n" \
  "global_load_lds_dwordx4 v104, s[42:43]\n" \
  "s_add_u32 m0, s57, 17408\n" \
  "s_nop 0\n" \
  "global_load_lds_dwordx4 v105, s[42:43]\n" \
  "s_add_u32 m0, s57, 18432\n" \
  "s_nop 0\n" \
  "global_load_lds_dwordx4 v106, s[42:43]\n" \
  "s_add_u32 m0, s57, 19456\n" \
  "s_nop 0\n" \
  "global_load_lds_dwordx4 v107, s[42:43]\n" \
  "s_add_u32 m0, s57, 20480\n" \
  "s_nop 0\n" \
  "global_load_lds_dwordx4 v108, s[42:43]\n" \
  "s_add_u32 m0, s56, 0\n" \
  "s_nop 0\n" \
  "global_load_lds_dwordx4 v100, s[40:41]\n" \
  "s_add_u32 m0, s56, 1024\n" \
  "s_nop 0\n" \
  "global_load_lds_dwordx4 v101, s[40:41]\n" \
  "s_add_u32 m0, s56, 2048\n" \
  "s_nop 0\n" \
  "global_load_lds_dwordx4 v102, s[40:41]\n" \
  "s_add_u32 m0, s56, 3072\n" \
  "s_nop 0\n" \
  "global_load_lds_dwordx4 v103, s[40:41]\n" \
  "s_cmp_eq_u32 s59, 3\n" \
  "s_cbranch_scc1 32f\n" \
  "s_add_u32 m0, s58, 36864\n" \
  "s_nop 0\n" \
  "global_load_lds_dwordx4 v109, s[44:45]\n" \
  "32:\n" \
  "s_add_u32 s76, s76, 1\n" \
  "s_cmp_eq_u32 s76, 10\n" \
  "s_cselect_b32 s77, -9, 1\n" \
  "s_cselect_b32 s78, -1, 0\n" \
  "s_cselect_b32 s76, 0, s76\n" \
  "s_mul_i32 s79, s77, 128\n" \
  "s_add_u32 s40, s40, s79\n" \
  "s_addc_u32 s41, s41, s78\n" \
  "s_mul_i32 s79, s77, 81920\n" \
  "s_add_u32 s42, s42, s79\n" \
  "s_addc_u32 s43, s43, s78\n" \
  "s_mul_i32 s79, s77, s46\n" \
  "s_add_u32 s44, s44, s79\n" \
  "s_addc_u32 s45, s45, s78\n" \
  "s_waitcnt vmcnt(9) lgkmcnt(0)\n" \
  "s_barrier\n" \
  "s_add_u32 m0, s57, 65536\n" \
  "s_nop 0\n" \
  "global_load_lds_dwordx4 v104, s[42:43]\n" \
  "s_add_u32 m0, s57, 66560\n" \
  "s_nop 0\n" \
  "global_load_lds_dwordx4 v105, s[42:43]\n" \
  "s_add_u32 m0, s57, 67584\n" \
  "s_nop 0\n" \
  "global_load_lds_dwordx4 v106, s[42:43]\n" \
  "s_add_u32 m0, s57, 68608\n" \
  "s_nop 0\n" \
  "global_load_lds_dwordx4 v107, s[42:43]\n" \
  "s_add_u32 m0, s57, 69632\n" \
  "s_nop 0\n" \
  "global_load_lds_dwordx4 v108, s[42:43]\n" \
  "s_add_u32 m0, s56, 49152\n" \
  "s_nop 0\n" \
  "global_load_lds_dwordx4 v100, s[40:41]\n" \
  "s_add_u32 m0, s56, 50176\n" \
  "s_nop 0\n" \
  "global_load_lds_dwordx4 v101, s[40:41]\n" \
  "s_add_u32 m0, s56, 51200\n" \
  "s_nop 0\n" \
  "global_load_lds_dwordx4 v102, s[40:41]\n" \
  "s_add_u32 m0, s56, 52224\n" \
  "s_nop 0\n" \
  "global_load_lds_dwordx4 v103, s[40:41]\n" \
  "s_cmp_eq_u32 s59, 3\n" \
  "s_cbranch_scc1 34f\n" \
  "s_add_u32 m0, s58, 86016\n" \
  "s_nop 0\n" \
  "global_load_lds_dwordx4 v109, s[44:45]\n" \
  "34:\n" \
  "s_add_u32 s76, s76, 1\n" \
  "s_cmp_eq_u32 s76, 10\n" \
  "s_cselect_b32 s77, -9, 1\n" \
  "s_cselect_b32 s78, -1, 0\n" \
  "s_cselect_b32 s76, 0, s76\n" \
  "s_mul_i32 s79, s77, 128\n" \
  "s_add_u32 s40, s40, s79\n" \
  "s_addc_u32 s41, s41, s78\n" \
  "s_mul_i32 s79, s77, 81920\n" \
  "s_add_u32 s42, s42, s79\n" \
  "s_addc_u32 s43, s43, s78\n" \
  "s_mul_i32 s79, s77, s46\n" \
  "s_add_u32 s44, s44, s79\n" \
  "s_addc_u32 s45, s45, s78\n" \
  "s_waitcnt vmcnt(9) lgkmcnt(0)\n" \
  "s_barrier\n" \
  "s_add_u32 m0, s57, 114688\n" \
  "s_nop 0\n" \
  "global_load_lds_dwordx4 v104, s[42:43]\n" \
  "s_add_u32 m0, s57, 115712\n" \
  "s_nop 0\n" \
  "global_load_lds_dwordx4 v105, s[42:43]\n" \
  "s_add_u32 m0, s57, 116736\n" \
  "s_nop 0\n" \
  "global_load_lds_dwordx4 v106, s[42:43]\n" \
  "s_add_u32 m0, s57, 117760\n" \
  "s_nop 0\n" \
  "global_load_lds_dwordx4 v107, s[42:43]\n" \
  "s_add_u32 m0, s57, 118784\n" \
  "s_nop 0\n" \
  "global_load_lds_dwordx4 v108, s[42:43]\n" \
  "s_add_u32 m0, s56, 98304\n" \
  "s_nop 0\n" \
  "global_load_lds_dwordx4 v100, s[40:41]\n" \
  "s_add_u32 m0, s56, 99328\n" \
  "s_nop 0\n" \
  "global_load_lds_dwordx4 v101, s[40:41]\n" \
  "s_add_u32 m0, s56, 100352\n" \
  "s_nop 0\n" \
  "global_load_lds_dwordx4 v102, s[40:41]\n" \
  "s_add_u32 m0, s56, 101376\n" \
  "s_nop 0\n" \
  "global_load_lds_dwordx4 v103, s[40:41]\n" \
  "s_cmp_eq_u32 s59, 3\n" \
  "s_cbranch_scc1 36f\n" \
  "s_add_u32 m0, s58, 135168\n" \
  "s_nop 0\n" \
  "global_load_lds_dwordx4 v109, s[44:45]\n" \
  "36:\n" \
  "s_add_u32 s76, s76, 1\n" \
  "s_cmp_eq_u32 s76, 10\n" \
  "s_cselect_b32 s77, -9, 1\n" \
  "s_cselect_b32 s78, -1, 0\n" \
  "s_cselect_b32 s76, 0, s76\n" \
  "s_mul_i32 s79, s77, 128\n" \
  "s_add_u32 s40, s40, s79\n" \
  "s_addc_u32 s41, s41, s78\n" \
  "s_mul_i32 s79, s77, 81920\n" \
  "s_add_u32 s42, s42, s79\n" \
  "s_addc_u32 s43, s43, s78\n" \
  "s_mul_i32 s79, s77, s46\n" \
  "s_add_u32 s44, s44, s79\n" \
  "s_addc_u32 s45, s45, s78\n" \
  "s_waitcnt vmcnt(9) lgkmcnt(0)\n" \
  "s_barrier\n" \
  "s_add_u32 m0, s57, 16384\n" \
  "s_nop 0\n" \
  "global_load_lds_dwordx4 v104, s[42:43]\n" \
  "s_add_u32 m0, s57, 17408\n" \
  "s_nop 0\n" \
  "global_load_lds_dwordx4 v105, s[42:43]\n" \
  "s_add_u32 m0, s57, 18432\n" \
  "s_nop 0\n" \
  "global_load_lds_dwordx4 v106, s[42:43]\n" \
  "s_add_u32 m0, s57, 19456\n" \
  "s_nop 0\n" \
  "global_load_lds_dwordx4 v107, s[42:43]\n" \
  "s_add_u32 m0, s57, 20480\n" \
  "s_nop 0\n" \
  "global_load_lds_dwordx4 v108, s[42:43]\n" \
  "s_add_u32 m0, s56, 0\n" \
  "s_nop 0\n" \
  "global_load_lds_dwordx4 v100, s[40:41]\n" \
  "s_add_u32 m0, s56, 1024\n" \
  "s_nop 0\n" \
  "global_load_lds_dwordx4 v101, s[40:41]\n" \
  "s_add_u32 m0, s56, 2048\n" \
  "s_nop 0\n" \
  "global_load_lds_dwordx4 v102, s[40:41]\n" \
  "s_add_u32 m0, s56, 3072\n" \
  "s_nop 0\n" \
  "global_load_lds_dwordx4 v103, s[40:41]\n" \
  "s_cmp_eq_u32 s59, 3\n" \
  "s_cbranch_scc1 38f\n" \
  "s_add_u32 m0, s58, 36864\n" \
  "s_nop 0\n" \
  "global_load_lds_dwordx4 v109, s[44:45]\n" \
  "38:\n" \
  "s_add_u32 s76, s76, 1\n" \
  "s_cmp_eq_u32 s76, 10\n" \
  "s_cselect_b32 s77, -9, 1\n" \
  "s_cselect_b32 s78, -1, 0\n" \
  "s_cselect_b32 s76, 0, s76\n" \
  "s_mul_i32 s79, s77, 128\n" \
  "s_add_u32 s40, s40, s79\n" \
  "s_addc_u32 s41, s41, s78\n" \
  "s_mul_i32 s79, s77, 81920\n" \
  "s_add_u32 s42, s42, s79\n" \
  "s_addc_u32 s43, s43, s78\n" \
  "s_mul_i32 s79, s77, s46\n" \
  "s_add_u32 s44, s44, s79\n" \
  "s_addc_u32 s45, s45, s78\n" \
  "s_waitcnt vmcnt(9) lgkmcnt(0)\n" \
  "s_barrier\n" \
  "s_waitcnt vmcnt(0) lgkmcnt(0)\n" \
  "s_barrier\n"
#define XS1_CLOB "v92", "v93", "v94", "v95", "v96", "v97", "v98", "v99", "v100", "v101", "v102", "v103", "v104", "v105", "v106", "v107", "v108"
#define XS2_ASM \
  "v_and_b32 v93, 0xf0, v116\n" \
  "v_lshrrev_b32 v93, 2, v93\n" \
  "s_mul_i32 s77, s59, 7168\n" \
  "v_add_u32 v96, s77, v116\n" \
  "v_mov_b32 v94, 1\n" \
  "s_barrier\n" \
  "s_mov_b32 s61, 262144\n" \
  "s_cmp_lg_u32 s59, 0\n" \
  "s_cbranch_scc1 8f\n" \
  "6:\n" \
  "global_load_dwordx2 v[4:5], v117, s[52:53] sc1\n" \
  "s_waitcnt vmcnt(0)\n" \
  "v_cmp_eq_u64 vcc, s[48:49], v[4:5]\n" \
  "s_cmp_eq_u64 vcc, exec\n" \
  "s_cbranch_scc1 7f\n" \
  "s_sub_u32 s61, s61, 1\n" \
  "s_cbranch_scc1 7f\n" \
  "s_sleep 1\n" \
  "s_branch 6b\n" \
  "7:\n" \
  "ds_write_b32 v119, v94 offset:68\n" \
  "s_branch 9f\n" \
  "8:\n" \
  "s_mov_b32 s61, 8388608\n" \
  "10:\n" \
  "ds_read_b32 v4, v119 offset:68\n" \
  "s_waitcnt lgkmcnt(0)\n" \
  "v_cmp_ne_u32 vcc, 0, v4\n" \
  "s_sub_u32 s61, s61, 1\n" \
  "s_cbranch_scc1 9f\n" \
  "s_nop 1\n" \
  "s_cbranch_vccnz 9f\n" \
  "s_sleep 1\n" \
  "s_branch 10b\n" \
  "9:\n" \
  "s_mul_i32 s78, s59, 3072\n" \
  "s_add_u32 m0, s78, 147456\n" \
  "s_nop 0\n" \
  "global_load_lds_dwordx4 v116, s[50:51] sc1\n" \
  "s_add_u32 m0, s78, 148480\n" \
  "s_add_u32 s50, s50, 4096\n" \
  "s_addc_u32 s51, s51, 0\n" \
  "global_load_lds_dwordx4 v116, s[50:51] sc1\n" \
  "s_add_u32 m0, s78, 149504\n" \
  "s_lshl_b32 s78, s59, 7\n" \
  "global_load_lds_dwordx4 v116, s[54:55] sc1\n" \
  "s_add_u32 m0, s78, 160384\n" \
  "s_mov_b64 exec, 0xff\n" \
  "global_load_lds_dwordx4 v116, s[68:69] sc1\n" \
  "s_mov_b64 exec, -1\n" \
  "s_add_u32 m0, s77, 55296\n" \
  "s_nop 0\n" \
  "global_load_lds_dwordx4 v96, s[66:67] offset:-4096 sc1\n" \
  "global_load_lds_dwordx4 v96, s[66:67] offset:-3072 sc1\n" \
  "global_load_lds_dwordx4 v96, s[66:67] offset:-2048 sc1\n" \
  "global_load_lds_dwordx4 v96, s[66:67] offset:-1024 sc1\n" \
  "global_load_lds_dwordx4 v96, s[66:67] offset:0 sc1\n" \
  "global_load_lds_dwordx4 v96, s[66:67] offset:1024 sc1\n" \
  "global_load_lds_dwordx4 v96, s[66:67] offset:2048 sc1\n" \
  "s_add_u32 s66, s66, 28672\n" \
  "s_addc_u32 s67, s67, 0\n" \
  "s_lshl_b32 s78, s59, 2\n" \
  "v_add_u32 v95, s78, v119\n" \
  "s_waitcnt vmcnt(7)\n" \
  "ds_write_b32 v95, v94 offset:80\n"
#define XS2_CLOB "v0", "v1", "v3", "v4", "v5", "v6", "v7", "v8", "v9", "v10", "v11", "v92", "v93", "v94", "v95", "v96", "v97", "v98", "v99"
#define XPH2_ASM \
  "s_waitcnt vmcnt(0) lgkmcnt(0)\n" \
  "s_barrier\n" \
  "s_mov_b32 m0, s57\n" \
  "s_sub_u32 s57, s57, 28672\n" \
  "global_load_lds_dwordx4 v105, s[66:67] offset:-4096 sc1\n" \
  "global_load_lds_dwordx4 v105, s[66:67] offset:-3072 sc1\n" \
  "global_load_lds_dwordx4 v105, s[66:67] offset:-2048 sc1\n" \
  "global_load_lds_dwordx4 v105, s[66:67] offset:-1024 sc1\n" \
  "global_load_lds_dwordx4 v105, s[66:67] offset:0 sc1\n" \
  "global_load_lds_dwordx4 v105, s[66:67] offset:1024 sc1\n" \
  "global_load_lds_dwordx4 v105, s[66:67] offset:2048 sc1\n" \
  "s_add_u32 s66, s66, 28672\n" \
  "s_addc_u32 s67, s67, 0\n" \
  "s_cmp_lg_u32 s59, 1\n" \
  "s_cbranch_scc1 4f\n" \
  "s_mov_b32 m0, 159872\n" \
  "s_mov_b32 exec_hi, 0\n" \
  "global_load_lds_dwordx4 v116, s[78:79] sc1\n" \
  "s_mov_b64 exec, -1\n" \
  "4:\n" \
  "v_and_b32 v93, 0xf0, v116\n" \
  "v_lshrrev_b32 v93, 2, v93\n" \
  "global_load_dword v19, v93, s[64:65] sc1\n" \
  "s_cmp_eq_u32 s60, 3\n" \
  "s_cbranch_scc1 3f\n" \
  "ds_read_b128 v[20:23], v111 offset:0\n" \
  "ds_read_b128 v[24:27], v111 offset:1024\n" \
  "ds_read_b128 v[28:31], v111 offset:2048\n" \
  "ds_read_b128 v[32:35], v111 offset:3072\n" \
  "ds_read_b128 v[36:39], v111 offset:4096\n" \
  "ds_read_b128 v[40:43], v111 offset:5120\n" \
  "ds_read_b128 v[44:47], v111 offset:6144\n" \
  "ds_read_b128 v[48:51], v111 offset:7168\n" \
  "s_waitcnt lgkmcnt(7)\n" \
  "global_store_dwordx4 v116, v[20:23], s[68:69] offset:0 sc1\n" \
  "s_waitcnt lgkmcnt(6)\n" \
  "global_store_dwordx4 v116, v[24:27], s[68:69] offset:1024 sc1\n" \
  "s_waitcnt lgkmcnt(5)\n" \
  "global_store_dwordx4 v116, v[28:31], s[68:69] offset:2048 sc1\n" \
  "s_waitcnt lgkmcnt(4)\n" \
  "global_store_dwordx4 v116, v[32:35], s[68:69] offset:3072 sc1\n" \
  "s_waitcnt lgkmcnt(3)\n" \
  "global_store_dwordx4 v116, v[36:39], s[70:71] offset:0 sc1\n" \
  "s_waitcnt lgkmcnt(2)\n" \
  "global_store_dwordx4 v116, v[40:43], s[70:71] offset:1024 sc1\n" \
  "s_waitcnt lgkmcnt(1)\n" \
  "global_store_dwordx4 v116, v[44:47], s[70:71] offset:2048 sc1\n" \
  "s_waitcnt lgkmcnt(0)\n" \
  "global_store_dwordx4 v116, v[48:51], s[70:71] offset:3072 sc1\n" \
  "s_waitcnt vmcnt(8)\n" \
  "s_branch 14f\n" \
  "3:\n" \
  "s_waitcnt vmcnt(0)\n" \
  "14:\n" \
  "v_sub_f32 v99, 1.0, v19\n" \
  "s_add_u32 m0, s55, 0\n" \
  "s_nop 0\n" \
  "global_load_lds_dwordx4 v100, s[40:41]\n" \
  "s_add_u32 m0, s55, 1024\n" \
  "s_nop 0\n" \
  "global_load_lds_dwordx4 v101, s[40:41]\n" \
  "s_add_u32 m0, s55, 2048\n" \
  "s_nop 0\n" \
  "global_load_lds_dwordx4 v102, s[40:41]\n" \
  "s_add_u32 m0, s55, 3072\n" \
  "s_nop 0\n" \
  "global_load_lds_dwordx4 v103, s[40:41]\n" \
  "s_add_u32 m0, s55, 4096\n" \
  "s_nop 0\n" \
  "global_load_lds_dwordx4 v104, s[40:41]\n" \
  "s_add_u32 s40, s40, 163840\n" \
  "s_addc_u32 s41, s41, 0\n" \
  "s_waitcnt lgkmcnt(0)\n" \
  "s_barrier\n" \
  "s_mov_b32 m0, s57\n" \
  "s_add_u32 s57, s57, 28672\n" \
  "global_load_lds_dwordx4 v105, s[66:67] offset:-4096 sc1\n" \
  "global_load_lds_dwordx4 v105, s[66:67] offset:-3072 sc1\n" \
  "global_load_lds_dwordx4 v105, s[66:67] offset:-2048 sc1\n" \
  "global_load_lds_dwordx4 v105, s[66:67] offset:-1024 sc1\n" \
  "global_load_lds_dwordx4 v105, s[66:67] offset:0 sc1\n" \
  "global_load_lds_dwordx4 v105, s[66:67] offset:1024 sc1\n" \
  "global_load_lds_dwordx4 v105, s[66:67] offset:2048 sc1\n" \
  "s_add_u32 s66, s66, 28672\n" \
  "s_addc_u32 s67, s67, 0\n" \
  "s_waitcnt vmcnt(12)\n" \
  "v_mov_b32 v92, 1\n" \
  "s_mov_b64 exec, 1\n" \
  "ds_add_rtn_u32 v92, v119, v92 offset:40\n" \
  "s_waitcnt lgkmcnt(0)\n" \
  "v_cmp_eq_u32 vcc, 3, v92\n" \
  "s_nop 4\n" \
  "s_cbranch_vccz 5f\n" \
  "s_mov_b64 exec, 1\n" \
  "v_lshrrev_b32 v92, 1, v116\n" \
  "v_mov_b32 v94, s48\n" \
  "v_mov_b32 v95, s49\n" \
  "global_store_dwordx2 v92, v[94:95], s[50:51] sc1\n" \
  "5:\n" \
  "s_mov_b64 exec, -1\n" \
  "s_add_u32 m0, s56, 0\n" \
  "s_nop 0\n" \
  "global_load_lds_dwordx4 v100, s[40:41]\n" \
  "s_add_u32 m0, s56, 1024\n" \
  "s_nop 0\n" \
  "global_load_lds_dwordx4 v101, s[40:41]\n" \
  "s_add_u32 m0, s56, 2048\n" \
  "s_nop 0\n" \
  "global_load_lds_dwordx4 v102, s[40:41]\n" \
  "s_add_u32 m0, s56, 3072\n" \
  "s_nop 0\n" \
  "global_load_lds_dwordx4 v103, s[40:41]\n" \
  "s_add_u32 m0, s56, 4096\n" \
  "s_nop 0\n" \
  "global_load_lds_dwordx4 v104, s[40:41]\n" \
  "s_add_u32 s40, s40, 163840\n" \
  "s_addc_u32 s41, s41, 0\n" \
  "s_waitcnt vmcnt(0) lgkmcnt(0)\n" \
  "s_barrier\n" \
  "global_load_dwordx2 v[92:93], v107, s[46:47] sc1\n" \
  "s_mov_b32 m0, s57\n" \
  "s_sub_u32 s57, s57, 28672\n" \
  "global_load_lds_dwordx4 v105, s[66:67] offset:-4096 sc1\n" \
  "global_load_lds_dwordx4 v105, s[66:67] offset:-3072 sc1\n" \
  "global_load_lds_dwordx4 v105, s[66:67] offset:-2048 sc1\n" \
  "global_load_lds_dwordx4 v105, s[66:67] offset:-1024 sc1\n" \
  "global_load_lds_dwordx4 v105, s[66:67] offset:0 sc1\n" \
  "global_load_lds_dwordx4 v105, s[66:67] offset:1024 sc1\n" \
  "global_load_lds_dwordx4 v105, s[66:67] offset:2048 sc1\n" \
  "s_add_u32 s66, s66, 28672\n" \
  "s_addc_u32 s67, s67, 0\n" \
  "s_waitcnt vmcnt(7)\n" \
  "v_cmp_eq_u64 vcc, s[48:49], v[92:93]\n" \
  "s_cmp_eq_u64 vcc, exec\n" \
  "s_cbranch_scc1 8f\n" \
  "s_mov_b32 s61, 262144\n" \
  "7:\n" \
  "global_load_dwordx2 v[92:93], v107, s[46:47] sc1\n" \
  "s_waitcnt vmcnt(0)\n" \
  "v_mov_b32 v94, 4\n" \
  "s_nop 0\n" \
  "v_cmp_eq_u64 vcc, s[48:49], v[92:93]\n" \
  "s_cmp_eq_u64 vcc, exec\n" \
  "s_cbranch_scc1 8f\n" \
  "s_sub_u32 s61, s61, 1\n" \
  "s_cbranch_scc1 8f\n" \
  "s_sleep 2\n" \
  "s_branch 7b\n" \
  "8:\n" \
  "global_load_dwordx4 v[20:23], v116, s[44:45] sc1\n" \
  "s_add_u32 s44, s44, 4096\n" \
  "s_addc_u32 s45, s45, 0\n" \
  "global_load_dwordx4 v[24:27], v116, s[44:45] sc1\n" \
  "s_add_u32 s44, s44, 4096\n" \
  "s_addc_u32 s45, s45, 0\n" \
  "global_load_dwordx4 v[28:31], v116, s[44:45] sc1\n" \
  "s_add_u32 s44, s44, 4096\n" \
  "s_addc_u32 s45, s45, 0\n" \
  "global_load_dwordx4 v[32:35], v116, s[44:45] sc1\n" \
  "s_add_u32 s44, s44, 4096\n" \
  "s_addc_u32 s45, s45, 0\n" \
  "global_load_dwordx4 v[36:39], v116, s[44:45] sc1\n" \
  "s_add_u32 s44, s44, 4096\n" \
  "s_addc_u32 s45, s45, 0\n" \
  "global_load_dwordx4 v[40:43], v116, s[44:45] sc1\n" \
  "s_add_u32 s44, s44, 4096\n" \
  "s_addc_u32 s45, s45, 0\n" \
  "global_load_dwordx4 v[44:47], v116, s[44:45] sc1\n" \
  "s_add_u32 s44, s44, 4096\n" \
  "s_addc_u32 s45, s45, 0\n" \
  "ds_read_b64_tr_b16 v[52:53], v109 offset:10240\n" \
  "ds_read_b64_tr_b16 v[54:55], v109 offset:12800\n" \
  "ds_read_b64_tr_b16 v[56:57], v109 offset:10272\n" \
  "ds_read_b64_tr_b16 v[58:59], v109 offset:12832\n" \
  "ds_read_b64_tr_b16 v[60:61], v109 offset:10304\n" \
  "ds_read_b64_tr_b16 v[62:63], v109 offset:12864\n" \
  "ds_read_b64_tr_b16 v[64:65], v109 offset:10336\n" \
  "ds_read_b64_tr_b16 v[66:67], v109 offset:12896\n" \
  "ds_read_b64_tr_b16 v[68:69], v109 offset:10368\n" \
  "ds_read_b64_tr_b16 v[70:71], v109 offset:12928\n" \
  "s_waitcnt vmcnt(6) lgkmcnt(0)\n" \
  "v_mfma_f32_16x16x32_bf16 v[0:3], v[52:55], v[20:23], 0\n" \
  "ds_read_b64_tr_b16 v[72:73], v109 offset:15360\n" \
  "ds_read_b64_tr_b16 v[74:75], v109 offset:17920\n" \
  "global_load_dwordx4 v[48:51], v116, s[44:45] sc1\n" \
  "s_add_u32 s44, s44, 4096\n" \
  "s_addc_u32 s45, s45, 0\n" \
  "s_nop 3\n" \
  "v_mfma_f32_16x16x32_bf16 v[4:7], v[56:59], v[20:23], 0\n" \
  "ds_read_b64_tr_b16 v[76:77], v109 offset:15392\n" \
  "ds_read_b64_tr_b16 v[78:79], v109 offset:17952\n" \
  "s_add_u32 m0, s52, 0\n" \
  "s_nop 0\n" \
  "global_load_lds_dwordx4 v100, s[40:41]\n" \
  "s_nop 3\n" \
  "v_mfma_f32_16x16x32_bf16 v[8:11], v[60:63], v[20:23], 0\n" \
  "ds_read_b64_tr_b16 v[80:81], v109 offset:15424\n" \
  "ds_read_b64_tr_b16 v[82:83], v109 offset:17984\n" \
  "s_nop 6\n" \
  "v_mfma_f32_16x16x32_bf16 v[12:15], v[64:67], v[20:23], 0\n" \
  "ds_read_b64_tr_b16 v[84:85], v109 offset:15456\n" \
  "ds_read_b64_tr_b16 v[86:87], v109 offset:18016\n" \
  "s_add_u32 m0, s52, 1024\n" \
  "s_nop 0\n" \
  "global_load_lds_dwordx4 v101, s[40:41]\n" \
  "s_nop 3\n" \
  "v_mfma_f32_16x16x32_bf16 v[16:19], v[68:71], v[20:23], 0\n" \
  "ds_read_b64_tr_b16 v[88:89], v109 offset:15488\n" \
  "ds_read_b64_tr_b16 v[90:91], v109 offset:18048\n" \
  "s_nop 6\n" \
  "s_waitcnt vmcnt(8) lgkmcnt(0)\n" \
  "v_mfma_f32_16x16x32_bf16 v[0:3], v[72:75], v[24:27], v[0:3]\n" \
  "ds_read_b64_tr_b16 v[52:53], v109 offset:20480\n" \
  "ds_read_b64_tr_b16 v[54:55], v109 offset:23040\n" \
  "global_load_dwordx4 v[20:23], v116, s[44:45] sc1\n" \
  "s_add_u32 s44, s44, 4096\n" \
  "s_addc_u32 s45, s45, 0\n" \
  "s_nop 3\n" \
  "v_mfma_f32_16x16x32_bf16 v[4:7], v[76:79], v[24:27], v[4:7]\n" \
  "ds_read_b64_tr_b16 v[56:57], v109 offset:20512\n" \
  "ds_read_b64_tr_b16 v[58:59], v109 offset:23072\n" \
  "s_add_u32 m0, s52, 2048\n" \
  "s_nop 0\n" \
  "global_load_lds_dwordx4 v102, s[40:41]\n" \
  "s_nop 3\n" \
  "v_mfma_f32_16x16x32_bf16 v[8:11], v[80:83], v[24:27], v[8:11]\n" \
  "ds_read_b64_tr_b16 v[60:61], v109 offset:20544\n" \
  "ds_read_b64_tr_b16 v[62:63], v109 offset:23104\n" \
  "s_nop 6\n" \
  "v_mfma_f32_16x16x32_bf16 v[12:15], v[84:87], v[24:27], v[12:15]\n" \
  "ds_read_b64_tr_b16 v[64:65], v109 offset:20576\n" \
  "ds_read_b64_tr_b16 v[66:67], v109 offset:23136\n" \
  "s_add_u32 m0, s52, 3072\n" \
  "s_nop 0\n" \
  "global_load_lds_dwordx4 v103, s[40:41]\n" \
  "s_nop 3\n" \
  "v_mfma_f32_16x16x32_bf16 v[16:19], v[88:91], v[24:27], v[16:19]\n" \
  "ds_read_b64_tr_b16 v[68:69], v109 offset:20608\n" \
  "ds_read_b64_tr_b16 v[70:71], v109 offset:23168\n" \
  "s_nop 6\n" \
  "s_waitcnt vmcnt(10) lgkmcnt(0)\n" \
  "v_mfma_f32_16x16x32_bf16 v[0:3], v[52:55], v[28:31], v[0:3]\n" \
  "ds_read_b64_tr_b16 v[72:73], v109 offset:25600\n" \
  "ds_read_b64_tr_b16 v[74:75], v109 offset:28160\n" \
  "global_load_dwordx4 v[24:27], v116, s[44:45] sc1\n" \
  "s_add_u32 s44, s44, 4096\n" \
  "s_addc_u32 s45, s45, 0\n" \
  "s_nop 3\n" \
  "v_mfma_f32_16x16x32_bf16 v[4:7], v[56:59], v[28:31], v[4:7]\n" \
  "ds_read_b64_tr_b16 v[76:77], v109 offset:25632\n" \
  "ds_read_b64_tr_b16 v[78:79], v109 offset:28192\n" \
  "s_add_u32 m0, s52, 4096\n" \
  "s_nop 0\n" \
  "global_load_lds_dwordx4 v104, s[40:41]\n" \
  "s_add_u32 s40, s40, 163840\n" \
  "s_addc_u32 s41, s41, 0\n" \
  "s_nop 3\n" \
  "v_mfma_f32_16x16x32_bf16 v[8:11], v[60:63], v[28:31], v[8:11]\n" \
  "ds_read_b64_tr_b16 v[80:81], v109 offset:25664\n" \
  "ds_read_b64_tr_b16 v[82:83], v109 offset:28224\n" \
  "s_nop 6\n" \
  "v_mfma_f32_16x16x32_bf16 v[12:15], v[64:67], v[28:31], v[12:15]\n" \
  "ds_read_b64_tr_b16 v[84:85], v109 offset:25696\n" \
  "ds_read_b64_tr_b16 v[86:87], v109 offset:28256\n" \
  "s_add_u32 m0, s53, 0\n" \
  "s_nop 0\n" \
  "global_load_lds_dwordx4 v100, s[40:41]\n" \
  "s_nop 3\n" \
  "v_mfma_f32_16x16x32_bf16 v[16:19], v[68:71], v[28:31], v[16:19]\n" \
  "ds_read_b64_tr_b16 v[88:89], v109 offset:25728\n" \
  "ds_read_b64_tr_b16 v[90:91], v109 offset:28288\n" \
  "s_nop 6\n" \
  "s_waitcnt vmcnt(12) lgkmcnt(0)\n" \
  "v_mfma_f32_16x16x32_bf16 v[0:3], v[72:75], v[32:35], v[0:3]\n" \
  "ds_read_b64_tr_b16 v[52:53], v109 offset:30720\n" \
  "ds_read_b64_tr_b16 v[54:55], v109 offset:33280\n" \
  "global_load_dwordx4 v[28:31], v116, s[44:45] sc1\n" \
  "s_add_u32 s44, s44, 4096\n" \
  "s_addc_u32 s45, s45, 0\n" \
  "s_nop 3\n" \
  "v_mfma_f32_16x16x32_bf16 v[4:7], v[76:79], v[32:35], v[4:7]\n" \
  "ds_read_b64_tr_b16 v[56:57], v109 offset:30752\n" \
  "ds_read_b64_tr_b16 v[58:59], v109 offset:33312\n" \
  "s_add_u32 m0, s53, 1024\n" \
  "s_nop 0\n" \
  "global_load_lds_dwordx4 v101, s[40:41]\n" \
  "s_nop 3\n" \
  "v_mfma_f32_16x16x32_bf16 v[8:11], v[80:83], v[32:35], v[8:11]\n" \
  "ds_read_b64_tr_b16 v[60:61], v109 offset:30784\n" \
  "ds_read_b64_tr_b16 v[62:63], v109 offset:33344\n" \
  "s_nop 6\n" \
  "v_mfma_f32_16x16x32_bf16 v[12:15], v[84:87], v[32:35], v[12:15]\n" \
  "ds_read_b64_tr_b16 v[64:65], v109 offset:30816\n" \
  "ds_read_b64_tr_b16 v[66:67], v109 offset:33376\n" \
  "s_add_u32 m0, s53, 2048\n" \
  "s_nop 0\n" \
  "global_load_lds_dwordx4 v102, s[40:41]\n" \
  "s_nop 3\n" \
  "v_mfma_f32_16x16x32_bf16 v[16:19], v[88:91], v[32:35], v[16:19]\n" \
  "ds_read_b64_tr_b16 v[68:69], v109 offset:30848\n" \
  "ds_read_b64_tr_b16 v[70:71], v109 offset:33408\n" \
  "s_nop 6\n" \
  "s_waitcnt vmcnt(14) lgkmcnt(0)\n" \
  "v_mfma_f32_16x16x32_bf16 v[0:3], v[52:55], v[36:39], v[0:3]\n" \
  "ds_read_b64_tr_b16 v[72:73], v109 offset:35840\n" \
  "ds_read_b64_tr_b16 v[74:75], v109 offset:38400\n" \
  "global_load_dwordx4 v[32:35], v116, s[44:45] sc1\n" \
  "s_add_u32 s44, s44, 4096\n" \
  "s_addc_u32 s45, s45, 0\n" \
  "s_nop 3\n" \
  "v_mfma_f32_16x16x32_bf16 v[4:7], v[56:59], v[36:39], v[4:7]\n" \
  "ds_read_b64_tr_b16 v[76:77], v109 offset:35872\n" \
  "ds_read_b64_tr_b16 v[78:79], v109 offset:38432\n" \
  "s_add_u32 m0, s53, 3072\n" \
  "s_nop 0\n" \
  "global_load_lds_dwordx4 v103, s[40:41]\n" \
  "s_nop 3\n" \
  "v_mfma_f32_16x16x32_bf16 v[8:11], v[60:63], v[36:39], v[8:11]\n" \
  "ds_read_b64_tr_b16 v[80:81], v109 offset:35904\n" \
  "ds_read_b64_tr_b16 v[82:83], v109 offset:38464\n" \
  "s_nop 6\n" \
  "v_mfma_f32_16x16x32_bf16 v[12:15], v[64:67], v[36:39], v[12:15]\n" \
  "ds_read_b64_tr_b16 v[84:85], v109 offset:35936\n" \
  "ds_read_b64_tr_b16 v[86:87], v109 offset:38496\n" \
  "s_add_u32 m0, s53, 4096\n" \
  "s_nop 0\n" \
  "global_load_lds_dwordx4 v104, s[40:41]\n" \
  "s_add_u32 s40, s40, 163840\n" \
  "s_addc_u32 s41, s41, 0\n" \
  "s_nop 3\n" \
  "v_mfma_f32_16x16x32_bf16 v[16:19], v[68:71], v[36:39], v[16:19]\n" \
  "ds_read_b64_tr_b16 v[88:89], v109 offset:35968\n" \
  "ds_read_b64_tr_b16 v[90:91], v109 offset:38528\n" \
  "s_nop 6\n" \
  "s_waitcnt vmcnt(16) lgkmcnt(0)\n" \
  "v_mfma_f32_16x16x32_bf16 v[0:3], v[72:75], v[40:43], v[0:3]\n" \
  "global_load_dwordx4 v[36:39], v116, s[44:45] sc1\n" \
  "s_add_u32 s44, s44, 4096\n" \
  "s_addc_u32 s45, s45, 0\n" \
  "s_nop 5\n" \
  "v_mfma_f32_16x16x32_bf16 v[4:7], v[76:79], v[40:43], v[4:7]\n" \
  "s_nop 8\n" \
  "v_mfma_f32_16x16x32_bf16 v[8:11], v[80:83], v[40:43], v[8:11]\n" \
  "s_nop 8\n" \
  "v_mfma_f32_16x16x32_bf16 v[12:15], v[84:87], v[40:43], v[12:15]\n" \
  "s_nop 8\n" \
  "v_mfma_f32_16x16x32_bf16 v[16:19], v[88:91], v[40:43], v[16:19]\n" \
  "s_nop 8\n" \
  "s_waitcnt vmcnt(1) lgkmcnt(0)\n" \
  "s_barrier\n" \
  "s_mov_b32 m0, s57\n" \
  "s_add_u32 s57, s57, 28672\n" \
  "global_load_lds_dwordx4 v105, s[66:67] offset:-4096 sc1\n" \
  "global_load_lds_dwordx4 v105, s[66:67] offset:-3072 sc1\n" \
  "global_load_lds_dwordx4 v105, s[66:67] offset:-2048 sc1\n" \
  "global_load_lds_dwordx4 v105, s[66:67] offset:-1024 sc1\n" \
  "global_load_lds_dwordx4 v105, s[66:67] offset:0 sc1\n" \
  "global_load_lds_dwordx4 v105, s[66:67] offset:1024 sc1\n" \
  "global_load_lds_dwordx4 v105, s[66:67] offset:2048 sc1\n" \
  "s_add_u32 s66, s66, 28672\n" \
  "s_addc_u32 s67, s67, 0\n" \
  "ds_read_b64_tr_b16 v[52:53], v109 offset:40960\n" \
  "ds_read_b64_tr_b16 v[54:55], v109 offset:43520\n" \
  "ds_read_b64_tr_b16 v[56:57], v109 offset:40992\n" \
  "ds_read_b64_tr_b16 v[58:59], v109 offset:43552\n" \
  "ds_read_b64_tr_b16 v[60:61], v109 offset:41024\n" \
  "ds_read_b64_tr_b16 v[62:63], v109 offset:43584\n" \
  "ds_read_b64_tr_b16 v[64:65], v109 offset:41056\n" \
  "ds_read_b64_tr_b16 v[66:67], v109 offset:43616\n" \
  "ds_read_b64_tr_b16 v[68:69], v109 offset:41088\n" \
  "ds_read_b64_tr_b16 v[70:71], v109 offset:43648\n" \
  "s_waitcnt lgkmcnt(0)\n" \
  "v_mfma_f32_16x16x32_bf16 v[0:3], v[52:55], v[44:47], v[0:3]\n" \
  "ds_read_b64_tr_b16 v[72:73], v109 offset:46080\n" \
  "ds_read_b64_tr_b16 v[74:75], v109 offset:48640\n" \
  "global_load_dwordx4 v[40:43], v116, s[44:45] sc1\n" \
  "s_add_u32 s44, s44, 4096\n" \
  "s_addc_u32 s45, s45, 0\n" \
  "s_nop 3\n" \
  "v_mfma_f32_16x16x32_bf16 v[4:7], v[56:59], v[44:47], v[4:7]\n" \
  "ds_read_b64_tr_b16 v[76:77], v109 offset:46112\n" \
  "ds_read_b64_tr_b16 v[78:79], v109 offset:48672\n" \
  "s_add_u32 m0, s54, 0\n" \
  "s_nop 0\n" \
  "global_load_lds_dwordx4 v100, s[40:41]\n" \
  "s_nop 3\n" \
  "v_mfma_f32_16x16x32_bf16 v[8:11], v[60:63], v[44:47], v[8:11]\n" \
  "ds_read_b64_tr_b16 v[80:81], v109 offset:46144\n" \
  "ds_read_b64_tr_b16 v[82:83], v109 offset:48704\n" \
  "s_nop 6\n" \
  "v_mfma_f32_16x16x32_bf16 v[12:15], v[64:67], v[44:47], v[12:15]\n" \
  "ds_read_b64_tr_b16 v[84:85], v109 offset:46176\n" \
  "ds_read_b64_tr_b16 v[86:87], v109 offset:48736\n" \
  "s_add_u32 m0, s54, 1024\n" \
  "s_nop 0\n" \
  "global_load_lds_dwordx4 v101, s[40:41]\n" \
  "s_nop 3\n" \
  "v_mfma_f32_16x16x32_bf16 v[16:19], v[68:71], v[44:47], v[16:19]\n" \
  "ds_read_b64_tr_b16 v[88:89], v109 offset:46208\n" \
  "ds_read_b64_tr_b16 v[90:91], v109 offset:48768\n" \
  "s_nop 6\n" \
  "s_waitcnt lgkmcnt(0)\n" \
  "v_mfma_f32_16x16x32_bf16 v[0:3], v[72:75], v[48:51], v[0:3]\n" \
  "ds_read_b64_tr_b16 v[52:53], v108 offset:0\n" \
  "ds_read_b64_tr_b16 v[54:55], v108 offset:2560\n" \
  "global_load_dwordx4 v[44:47], v116, s[44:45] sc1\n" \
  "s_add_u32 s44, s44, 4096\n" \
  "s_addc_u32 s45, s45, 0\n" \
  "s_nop 3\n" \
  "v_mfma_f32_16x16x32_bf16 v[4:7], v[76:79], v[48:51], v[4:7]\n" \
  "ds_read_b64_tr_b16 v[56:57], v108 offset:32\n" \
  "ds_read_b64_tr_b16 v[58:59], v108 offset:2592\n" \
  "s_add_u32 m0, s54, 2048\n" \
  "s_nop 0\n" \
  "global_load_lds_dwordx4 v102, s[40:41]\n" \
  "s_nop 3\n" \
  "v_mfma_f32_16x16x32_bf16 v[8:11], v[80:83], v[48:51], v[8:11]\n" \
  "ds_read_b64_tr_b16 v[60:61], v108 offset:64\n" \
  "ds_read_b64_tr_b16 v[62:63], v108 offset:2624\n" \
  "s_nop 6\n" \
  "v_mfma_f32_16x16x32_bf16 v[12:15], v[84:87], v[48:51], v[12:15]\n" \
  "ds_read_b64_tr_b16 v[64:65], v108 offset:96\n" \
  "ds_read_b64_tr_b16 v[66:67], v108 offset:2656\n" \
  "s_add_u32 m0, s54, 3072\n" \
  "s_nop 0\n" \
  "global_load_lds_dwordx4 v103, s[40:41]\n" \
  "s_nop 3\n" \
  "v_mfma_f32_16x16x32_bf16 v[16:19], v[88:91], v[48:51], v[16:19]\n" \
  "ds_read_b64_tr_b16 v[68:69], v108 offset:128\n" \
  "ds_read_b64_tr_b16 v[70:71], v108 offset:2688\n" \
  "s_nop 6\n" \
  "s_waitcnt lgkmcnt(0)\n" \
  "v_mfma_f32_16x16x32_bf16 v[0:3], v[52:55], v[20:23], v[0:3]\n" \
  "ds_read_b64_tr_b16 v[72:73], v108 offset:5120\n" \
  "ds_read_b64_tr_b16 v[74:75], v108 offset:7680\n" \
  "global_load_dwordx4 v[48:51], v116, s[44:45] sc1\n" \
  "s_add_u32 s44, s44, 4096\n" \
  "s_addc_u32 s45, s45, 0\n" \
  "s_nop 3\n" \
  "v_mfma_f32_16x16x32_bf16 v[4:7], v[56:59], v[20:23], v[4:7]\n" \
  "ds_read_b64_tr_b16 v[76:77], v108 offset:5152\n" \
  "ds_read_b64_tr_b16 v[78:79], v108 offset:7712\n" \
  "s_add_u32 m0, s54, 4096\n" \
  "s_nop 0\n" \
  "global_load_lds_dwordx4 v104, s[40:41]\n" \
  "s_add_u32 s40, s40, 163840\n" \
  "s_addc_u32 s41, s41, 0\n" \
  "s_nop 3\n" \
  "v_mfma_f32_16x16x32_bf16 v[8:11], v[60:63], v[20:23], v[8:11]\n" \
  "ds_read_b64_tr_b16 v[80:81], v108 offset:5184\n" \
  "ds_read_b64_tr_b16 v[82:83], v108 offset:7744\n" \
  "s_nop 6\n" \
  "v_mfma_f32_16x16x32_bf16 v[12:15], v[64:67], v[20:23], v[12:15]\n" \
  "ds_read_b64_tr_b16 v[84:85], v108 offset:5216\n" \
  "ds_read_b64_tr_b16 v[86:87], v108 offset:7776\n" \
  "s_add_u32 m0, s55, 0\n" \
  "s_nop 0\n" \
  "global_load_lds_dwordx4 v100, s[40:41]\n" \
  "s_nop 3\n" \
  "v_mfma_f32_16x16x32_bf16 v[16:19], v[68:71], v[20:23], v[16:19]\n" \
  "ds_read_b64_tr_b16 v[88:89], v108 offset:5248\n" \
  "ds_read_b64_tr_b16 v[90:91], v108 offset:7808\n" \
  "s_nop 6\n" \
  "s_waitcnt lgkmcnt(0)\n" \
  "v_mfma_f32_16x16x32_bf16 v[0:3], v[72:75], v[24:27], v[0:3]\n" \
  "ds_read_b64_tr_b16 v[52:53], v108 offset:10240\n" \
  "ds_read_b64_tr_b16 v[54:55], v108 offset:12800\n" \
  "global_load_dwordx4 v[20:23], v116, s[44:45] sc1\n" \
  "s_add_u32 s44, s44, 4096\n" \
  "s_addc_u32 s45, s45, 0\n" \
  "s_nop 3\n" \
  "v_mfma_f32_16x16x32_bf16 v[4:7], v[76:79], v[24:27], v[4:7]\n" \
  "ds_read_b64_tr_b16 v[56:57], v108 offset:10272\n" \
  "ds_read_b64_tr_b16 v[58:59], v108 offset:12832\n" \
  "s_add_u32 m0, s55, 1024\n" \
  "s_nop 0\n" \
  "global_load_lds_dwordx4 v101, s[40:41]\n" \
  "s_nop 3\n" \
  "v_mfma_f32_16x16x32_bf16 v[8:11], v[80:83], v[24:27], v[8:11]\n" \
  "ds_read_b64_tr_b16 v[60:61], v108 offset:10304\n" \
  "ds_read_b64_tr_b16 v[62:63], v108 offset:12864\n" \
  "s_nop 6\n" \
  "v_mfma_f32_16x16x32_bf16 v[12:15], v[84:87], v[24:27], v[12:15]\n" \
  "ds_read_b64_tr_b16 v[64:65], v108 offset:10336\n" \
  "ds_read_b64_tr_b16 v[66:67], v108 offset:12896\n" \
  "s_add_u32 m0, s55, 2048\n" \
  "s_nop 0\n" \
  "global_load_lds_dwordx4 v102, s[40:41]\n" \
  "s_nop 3\n" \
  "v_mfma_f32_16x16x32_bf16 v[16:19], v[88:91], v[24:27], v[16:19]\n" \
  "ds_read_b64_tr_b16 v[68:69], v108 offset:10368\n" \
  "ds_read_b64_tr_b16 v[70:71], v108 offset:12928\n" \
  "s_nop 6\n" \
  "s_waitcnt lgkmcnt(0)\n" \
  "v_mfma_f32_16x16x32_bf16 v[0:3], v[52:55], v[28:31], v[0:3]\n" \
  "ds_read_b64_tr_b16 v[72:73], v108 offset:15360\n" \
  "ds_read_b64_tr_b16 v[74:75], v108 offset:17920\n" \
  "global_load_dwordx4 v[24:27], v116, s[44:45] sc1\n" \
  "s_add_u32 s44, s44, 4096\n" \
  "s_addc_u32 s45, s45, 0\n" \
  "s_nop 3\n" \
  "v_mfma_f32_16x16x32_bf16 v[4:7], v[56:59], v[28:31], v[4:7]\n" \
  "ds_read_b64_tr_b16 v[76:77], v108 offset:15392\n" \
  "ds_read_b64_tr_b16 v[78:79], v108 offset:17952\n" \
  "s_add_u32 m0, s55, 3072\n" \
  "s_nop 0\n" \
  "global_load_lds_dwordx4 v103, s[40:41]\n" \
  "s_nop 3\n" \
  "v_mfma_f32_16x16x32_bf16 v[8:11], v[60:63], v[28:31], v[8:11]\n" \
  "ds_read_b64_tr_b16 v[80:81], v108 offset:15424\n" \
  "ds_read_b64_tr_b16 v[82:83], v108 offset:17984\n" \
  "s_nop 6\n" \
  "v_mfma_f32_16x16x32_bf16 v[12:15], v[64:67], v[28:31], v[12:15]\n" \
  "ds_read_b64_tr_b16 v[84:85], v108 offset:15456\n" \
  "ds_read_b64_tr_b16 v[86:87], v108 offset:18016\n" \
  "s_add_u32 m0, s55, 4096\n" \
  "s_nop 0\n" \
  "global_load_lds_dwordx4 v104, s[40:41]\n" \
  "s_add_u32 s40, s40, 163840\n" \
  "s_addc_u32 s41, s41, 0\n" \
  "s_nop 3\n" \
  "v_mfma_f32_16x16x32_bf16 v[16:19], v[68:71], v[28:31], v[16:19]\n" \
  "ds_read_b64_tr_b16 v[88:89], v108 offset:15488\n" \
  "ds_read_b64_tr_b16 v[90:91], v108 offset:18048\n" \
  "s_nop 6\n" \
  "s_waitcnt lgkmcnt(0)\n" \
  "v_mfma_f32_16x16x32_bf16 v[0:3], v[72:75], v[32:35], v[0:3]\n" \
  "ds_read_b64_tr_b16 v[52:53], v108 offset:20480\n" \
  "ds_read_b64_tr_b16 v[54:55], v108 offset:23040\n" \
  "global_load_dwordx4 v[28:31], v116, s[44:45] sc1\n" \
  "s_add_u32 s44, s44, 4096\n" \
  "s_addc_u32 s45, s45, 0\n" \
  "s_nop 3\n" \
  "v_mfma_f32_16x16x32_bf16 v[4:7], v[76:79], v[32:35], v[4:7]\n" \
  "ds_read_b64_tr_b16 v[56:57], v108 offset:20512\n" \
  "ds_read_b64_tr_b16 v[58:59], v108 offset:23072\n" \
  "s_nop 6\n" \
  "v_mfma_f32_16x16x32_bf16 v[8:11], v[80:83], v[32:35], v[8:11]\n" \
  "ds_read_b64_tr_b16 v[60:61], v108 offset:20544\n" \
  "ds_read_b64_tr_b16 v[62:63], v108 offset:23104\n" \
  "s_nop 6\n" \
  "v_mfma_f32_16x16x32_bf16 v[12:15], v[84:87], v[32:35], v[12:15]\n" \
  "ds_read_b64_tr_b16 v[64:65], v108 offset:20576\n" \
  "ds_read_b64_tr_b16 v[66:67], v108 offset:23136\n" \
  "s_nop 6\n" \
  "v_mfma_f32_16x16x32_bf16 v[16:19], v[88:91], v[32:35], v[16:19]\n" \
  "ds_read_b64_tr_b16 v[68:69], v108 offset:20608\n" \
  "ds_read_b64_tr_b16 v[70:71], v108 offset:23168\n" \
  "s_nop 6\n" \
  "s_waitcnt vmcnt(23) lgkmcnt(0)\n" \
  "v_mfma_f32_16x16x32_bf16 v[0:3], v[52:55], v[36:39], v[0:3]\n" \
  "ds_read_b64_tr_b16 v[72:73], v108 offset:25600\n" \
  "ds_read_b64_tr_b16 v[74:75], v108 offset:28160\n" \
  "global_load_dwordx4 v[32:35], v116, s[44:45] sc1\n" \
  "s_add_u32 s44, s44, 4096\n" \
  "s_addc_u32 s45, s45, 0\n" \
  "s_nop 3\n" \
  "v_mfma_f32_16x16x32_bf16 v[4:7], v[56:59], v[36:39], v[4:7]\n" \
  "ds_read_b64_tr_b16 v[76:77], v108 offset:25632\n" \
  "ds_read_b64_tr_b16 v[78:79], v108 offset:28192\n" \
  "s_nop 6\n" \
  "v_mfma_f32_16x16x32_bf16 v[8:11], v[60:63], v[36:39], v[8:11]\n" \
  "ds_read_b64_tr_b16 v[80:81], v108 offset:25664\n" \
  "ds_read_b64_tr_b16 v[82:83], v108 offset:28224\n" \
  "s_nop 6\n" \
  "v_mfma_f32_16x16x32_bf16 v[12:15], v[64:67], v[36:39], v[12:15]\n" \
  "ds_read_b64_tr_b16 v[84:85], v108 offset:25696\n" \
  "ds_read_b64_tr_b16 v[86:87], v108 offset:28256\n" \
  "s_nop 6\n" \
  "v_mfma_f32_16x16x32_bf16 v[16:19], v[68:71], v[36:39], v[16:19]\n" \
  "ds_read_b64_tr_b16 v[88:89], v108 offset:25728\n" \
  "ds_read_b64_tr_b16 v[90:91], v108 offset:28288\n" \
  "s_nop 6\n" \
  "s_waitcnt vmcnt(16) lgkmcnt(0)\n" \
  "v_mfma_f32_16x16x32_bf16 v[0:3], v[72:75], v[40:43], v[0:3]\n" \
  "global_load_dwordx4 v[36:39], v116, s[44:45] sc1\n" \
  "s_add_u32 s44, s44, 4096\n" \
  "s_addc_u32 s45, s45, 0\n" \
  "s_nop 5\n" \
  "v_mfma_f32_16x16x32_bf16 v[4:7], v[76:79], v[40:43], v[4:7]\n" \
  "s_nop 8\n" \
  "v_mfma_f32_16x16x32_bf16 v[8:11], v[80:83], v[40:43], v[8:11]\n" \
  "s_nop 8\n" \
  "v_mfma_f32_16x16x32_bf16 v[12:15], v[84:87], v[40:43], v[12:15]\n" \
  "s_nop 8\n" \
  "v_mfma_f32_16x16x32_bf16 v[16:19], v[88:91], v[40:43], v[16:19]\n" \
  "s_nop 8\n" \
  "s_waitcnt vmcnt(3) lgkmcnt(0)\n" \
  "s_barrier\n" \
  "s_mov_b32 m0, s57\n" \
  "s_sub_u32 s57, s57, 28672\n" \
  "global_load_lds_dwordx4 v105, s[66:67] offset:-4096 sc1\n" \
  "global_load_lds_dwordx4 v105, s[66:67] offset:-3072 sc1\n" \
  "global_load_lds_dwordx4 v105, s[66:67] offset:-2048 sc1\n" \
  "global_load_lds_dwordx4 v105, s[66:67] offset:-1024 sc1\n" \
  "global_load_lds_dwordx4 v105, s[66:67] offset:0 sc1\n" \
  "global_load_lds_dwordx4 v105, s[66:67] offset:1024 sc1\n" \
  "global_load_lds_dwordx4 v105, s[66:67] offset:2048 sc1\n" \
  "s_add_u32 s66, s66, 28672\n" \
  "s_addc_u32 s67, s67, 0\n" \
  "ds_read_b64_tr_b16 v[52:53], v108 offset:30720\n" \
  "ds_read_b64_tr_b16 v[54:55], v108 offset:33280\n" \
  "ds_read_b64_tr_b16 v[56:57], v108 offset:30752\n" \
  "ds_read_b64_tr_b16 v[58:59], v108 offset:33312\n" \
  "ds_read_b64_tr_b16 v[60:61], v108 offset:30784\n" \
  "ds_read_b64_tr_b16 v[62:63], v108 offset:33344\n" \
  "ds_read_b64_tr_b16 v[64:65], v108 offset:30816\n" \
  "ds_read_b64_tr_b16 v[66:67], v108 offset:33376\n" \
  "ds_read_b64_tr_b16 v[68:69], v108 offset:30848\n" \
  "ds_read_b64_tr_b16 v[70:71], v108 offset:33408\n" \
  "s_waitcnt lgkmcnt(0)\n" \
  "v_mfma_f32_16x16x32_bf16 v[0:3], v[52:55], v[44:47], v[0:3]\n" \
  "ds_read_b64_tr_b16 v[72:73], v108 offset:35840\n" \
  "ds_read_b64_tr_b16 v[74:75], v108 offset:38400\n" \
  "global_load_dwordx4 v[40:43], v116, s[44:45] sc1\n" \
  "s_add_u32 s44, s44, 4096\n" \
  "s_addc_u32 s45, s45, 0\n" \
  "s_nop 3\n" \
  "v_mfma_f32_16x16x32_bf16 v[4:7], v[56:59], v[44:47], v[4:7]\n" \
  "ds_read_b64_tr_b16 v[76:77], v108 offset:35872\n" \
  "ds_read_b64_tr_b16 v[78:79], v108 offset:38432\n" \
  "s_add_u32 m0, s56, 0\n" \
  "s_nop 0\n" \
  "global_load_lds_dwordx4 v100, s[40:41]\n" \
  "s_nop 3\n" \
  "v_mfma_f32_16x16x32_bf16 v[8:11], v[60:63], v[44:47], v[8:11]\n" \
  "ds_read_b64_tr_b16 v[80:81], v108 offset:35904\n" \
  "ds_read_b64_tr_b16 v[82:83], v108 offset:38464\n" \
  "s_nop 6\n" \
  "v_mfma_f32_16x16x32_bf16 v[12:15], v[64:67], v[44:47], v[12:15]\n" \
  "ds_read_b64_tr_b16 v[84:85], v108 offset:35936\n" \
  "ds_read_b64_tr_b16 v[86:87], v108 offset:38496\n" \
  "s_add_u32 m0, s56, 1024\n" \
  "s_nop 0\n" \
  "global_load_lds_dwordx4 v101, s[40:41]\n" \
  "s_nop 3\n" \
  "v_mfma_f32_16x16x32_bf16 v[16:19], v[68:71], v[44:47], v[16:19]\n" \
  "ds_read_b64_tr_b16 v[88:89], v108 offset:35968\n" \
  "ds_read_b64_tr_b16 v[90:91], v108 offset:38528\n" \
  "s_nop 6\n" \
  "s_waitcnt lgkmcnt(0)\n" \
  "v_mfma_f32_16x16x32_bf16 v[0:3], v[72:75], v[48:51], v[0:3]\n" \
  "ds_read_b64_tr_b16 v[52:53], v108 offset:40960\n" \
  "ds_read_b64_tr_b16 v[54:55], v108 offset:43520\n" \
  "global_load_dwordx4 v[44:47], v116, s[44:45] sc1\n" \
  "s_add_u32 s44, s44, 4096\n" \
  "s_addc_u32 s45, s45, 0\n" \
  "s_nop 3\n" \
  "v_mfma_f32_16x16x32_bf16 v[4:7], v[76:79], v[48:51], v[4:7]\n" \
  "ds_read_b64_tr_b16 v[56:57], v108 offset:40992\n" \
  "ds_read_b64_tr_b16 v[58:59], v108 offset:43552\n" \
  "s_add_u32 m0, s56, 2048\n" \
  "s_nop 0\n" \
  "global_load_lds_dwordx4 v102, s[40:41]\n" \
  "s_nop 3\n" \
  "v_mfma_f32_16x16x32_bf16 v[8:11], v[80:83], v[48:51], v[8:11]\n" \
  "ds_read_b64_tr_b16 v[60:61], v108 offset:41024\n" \
  "ds_read_b64_tr_b16 v[62:63], v108 offset:43584\n" \
  "s_nop 6\n" \
  "v_mfma_f32_16x16x32_bf16 v[12:15], v[84:87], v[48:51], v[12:15]\n" \
  "ds_read_b64_tr_b16 v[64:65], v108 offset:41056\n" \
  "ds_read_b64_tr_b16 v[66:67], v108 offset:43616\n" \
  "s_add_u32 m0, s56, 3072\n" \
  "s_nop 0\n" \
  "global_load_lds_dwordx4 v103, s[40:41]\n" \
  "s_nop 3\n" \
  "v_mfma_f32_16x16x32_bf16 v[16:19], v[88:91], v[48:51], v[16:19]\n" \
  "ds_read_b64_tr_b16 v[68:69], v108 offset:41088\n" \
  "ds_read_b64_tr_b16 v[70:71], v108 offset:43648\n" \
  "s_nop 6\n" \
  "s_waitcnt lgkmcnt(0)\n" \
  "v_mfma_f32_16x16x32_bf16 v[0:3], v[52:55], v[20:23], v[0:3]\n" \
  "ds_read_b64_tr_b16 v[72:73], v108 offset:46080\n" \
  "ds_read_b64_tr_b16 v[74:75], v108 offset:48640\n" \
  "global_load_dwordx4 v[48:51], v116, s[44:45] sc1\n" \
  "s_add_u32 s44, s44, 4096\n" \
  "s_addc_u32 s45, s45, 0\n" \
  "s_nop 3\n" \
  "v_mfma_f32_16x16x32_bf16 v[4:7], v[56:59], v[20:23], v[4:7]\n" \
  "ds_read_b64_tr_b16 v[76:77], v108 offset:46112\n" \
  "ds_read_b64_tr_b16 v[78:79], v108 offset:48672\n" \
  "s_add_u32 m0, s56, 4096\n" \
  "s_nop 0\n" \
  "global_load_lds_dwordx4 v104, s[40:41]\n" \
  "s_add_u32 s40, s40, 163840\n" \
  "s_addc_u32 s41, s41, 0\n" \
  "s_nop 3\n" \
  "v_mfma_f32_16x16x32_bf16 v[8:11], v[60:63], v[20:23], v[8:11]\n" \
  "ds_read_b64_tr_b16 v[80:81], v108 offset:46144\n" \
  "ds_read_b64_tr_b16 v[82:83], v108 offset:48704\n" \
  "s_nop 6\n" \
  "v_mfma_f32_16x16x32_bf16 v[12:15], v[64:67], v[20:23], v[12:15]\n" \
  "ds_read_b64_tr_b16 v[84:85], v108 offset:46176\n" \
  "ds_read_b64_tr_b16 v[86:87], v108 offset:48736\n" \
  "s_add_u32 m0, s52, 0\n" \
  "s_nop 0\n" \
  "global_load_lds_dwordx4 v100, s[40:41]\n" \
  "s_nop 3\n" \
  "v_mfma_f32_16x16x32_bf16 v[16:19], v[68:71], v[20:23], v[16:19]\n" \
  "ds_read_b64_tr_b16 v[88:89], v108 offset:46208\n" \
  "ds_read_b64_tr_b16 v[90:91], v108 offset:48768\n" \
  "s_nop 6\n" \
  "s_waitcnt lgkmcnt(0)\n" \
  "v_mfma_f32_16x16x32_bf16 v[0:3], v[72:75], v[24:27], v[0:3]\n" \
  "ds_read_b64_tr_b16 v[52:53], v109 offset:0\n" \
  "ds_read_b64_tr_b16 v[54:55], v109 offset:2560\n" \
  "global_load_dwordx4 v[20:23], v116, s[44:45] sc1\n" \
  "s_add_u32 s44, s44, 4096\n" \
  "s_addc_u32 s45, s45, 0\n" \
  "s_nop 3\n" \
  "v_mfma_f32_16x16x32_bf16 v[4:7], v[76:79], v[24:27], v[4:7]\n" \
  "ds_read_b64_tr_b16 v[56:57], v109 offset:32\n" \
  "ds_read_b64_tr_b16 v[58:59], v109 offset:2592\n" \
  "s_add_u32 m0, s52, 1024\n" \
  "s_nop 0\n" \
  "global_load_lds_dwordx4 v101, s[40:41]\n" \
  "s_nop 3\n" \
  "v_mfma_f32_16x16x32_bf16 v[8:11], v[80:83], v[24:27], v[8:11]\n" \
  "ds_read_b64_tr_b16 v[60:61], v109 offset:64\n" \
  "ds_read_b64_tr_b16 v[62:63], v109 offset:2624\n" \
  "s_nop 6\n" \
  "v_mfma_f32_16x16x32_bf16 v[12:15], v[84:87], v[24:27], v[12:15]\n" \
  "ds_read_b64_tr_b16 v[64:65], v109 offset:96\n" \
  "ds_read_b64_tr_b16 v[66:67], v109 offset:2656\n" \
  "s_add_u32 m0, s52, 2048\n" \
  "s_nop 0\n" \
  "global_load_lds_dwordx4 v102, s[40:41]\n" \
  "s_nop 3\n" \
  "v_mfma_f32_16x16x32_bf16 v[16:19], v[88:91], v[24:27], v[16:19]\n" \
  "ds_read_b64_tr_b16 v[68:69], v109 offset:128\n" \
  "ds_read_b64_tr_b16 v[70:71], v109 offset:2688\n" \
  "s_nop 6\n" \
  "s_waitcnt vmcnt(21) lgkmcnt(0)\n" \
  "v_mfma_f32_16x16x32_bf16 v[0:3], v[52:55], v[28:31], v[0:3]\n" \
  "ds_read_b64_tr_b16 v[72:73], v109 offset:5120\n" \
  "ds_read_b64_tr_b16 v[74:75], v109 offset:7680\n" \
  "global_load_dwordx4 v[24:27], v116, s[44:45] sc1\n" \
  "s_add_u32 s44, s44, 4096\n" \
  "s_addc_u32 s45, s45, 0\n" \
  "s_nop 3\n" \
  "v_mfma_f32_16x16x32_bf16 v[4:7], v[56:59], v[28:31], v[4:7]\n" \
  "ds_read_b64_tr_b16 v[76:77], v109 offset:5152\n" \
  "ds_read_b64_tr_b16 v[78:79], v109 offset:7712\n" \
  "s_add_u32 m0, s52, 3072\n" \
  "s_nop 0\n" \
  "global_load_lds_dwordx4 v103, s[40:41]\n" \
  "s_nop 3\n" \
  "v_mfma_f32_16x16x32_bf16 v[8:11], v[60:63], v[28:31], v[8:11]\n" \
  "ds_read_b64_tr_b16 v[80:81], v109 offset:5184\n" \
  "ds_read_b64_tr_b16 v[82:83], v109 offset:7744\n" \
  "s_nop 6\n" \
  "v_mfma_f32_16x16x32_bf16 v[12:15], v[64:67], v[28:31], v[12:15]\n" \
  "ds_read_b64_tr_b16 v[84:85], v109 offset:5216\n" \
  "ds_read_b64_tr_b16 v[86:87], v109 offset:7776\n" \
  "s_add_u32 m0, s52, 4096\n" \
  "s_nop 0\n" \
  "global_load_lds_dwordx4 v104, s[40:41]\n" \
  "s_add_u32 s40, s40, 163840\n" \
  "s_addc_u32 s41, s41, 0\n" \
  "s_nop 3\n" \
  "v_mfma_f32_16x16x32_bf16 v[16:19], v[68:71], v[28:31], v[16:19]\n" \
  "ds_read_b64_tr_b16 v[88:89], v109 offset:5248\n" \
  "ds_read_b64_tr_b16 v[90:91], v109 offset:7808\n" \
  "s_nop 6\n" \
  "s_waitcnt vmcnt(23) lgkmcnt(0)\n" \
  "v_mfma_f32_16x16x32_bf16 v[0:3], v[72:75], v[32:35], v[0:3]\n" \
  "ds_read_b64_tr_b16 v[52:53], v109 offset:10240\n" \
  "ds_read_b64_tr_b16 v[54:55], v109 offset:12800\n" \
  "global_load_dwordx4 v[28:31], v116, s[44:45] sc1\n" \
  "s_add_u32 s44, s44, 4096\n" \
  "s_addc_u32 s45, s45, 0\n" \
  "s_nop 3\n" \
  "v_mfma_f32_16x16x32_bf16 v[4:7], v[76:79], v[32:35], v[4:7]\n" \
  "ds_read_b64_tr_b16 v[56:57], v109 offset:10272\n" \
  "ds_read_b64_tr_b16 v[58:59], v109 offset:12832\n" \
  "s_nop 6\n" \
  "v_mfma_f32_16x16x32_bf16 v[8:11], v[80:83], v[32:35], v[8:11]\n" \
  "ds_read_b64_tr_b16 v[60:61], v109 offset:10304\n" \
  "ds_read_b64_tr_b16 v[62:63], v109 offset:12864\n" \
  "s_nop 6\n" \
  "v_mfma_f32_16x16x32_bf16 v[12:15], v[84:87], v[32:35], v[12:15]\n" \
  "ds_read_b64_tr_b16 v[64:65], v109 offset:10336\n" \
  "ds_read_b64_tr_b16 v[66:67], v109 offset:12896\n" \
  "s_nop 6\n" \
  "v_mfma_f32_16x16x32_bf16 v[16:19], v[88:91], v[32:35], v[16:19]\n" \
  "ds_read_b64_tr_b16 v[68:69], v109 offset:10368\n" \
  "ds_read_b64_tr_b16 v[70:71], v109 offset:12928\n" \
  "s_nop 6\n" \
  "s_waitcnt vmcnt(23) lgkmcnt(0)\n" \
  "v_mfma_f32_16x16x32_bf16 v[0:3], v[52:55], v[36:39], v[0:3]\n" \
  "ds_read_b64_tr_b16 v[72:73], v109 offset:15360\n" \
  "ds_read_b64_tr_b16 v[74:75], v109 offset:17920\n" \
  "global_load_dwordx4 v[32:35], v116, s[44:45] sc1\n" \
  "s_add_u32 s44, s44, 4096\n" \
  "s_addc_u32 s45, s45, 0\n" \
  "s_nop 3\n" \
  "v_mfma_f32_16x16x32_bf16 v[4:7], v[56:59], v[36:39], v[4:7]\n" \
  "ds_read_b64_tr_b16 v[76:77], v109 offset:15392\n" \
  "ds_read_b64_tr_b16 v[78:79], v109 offset:17952\n" \
  "s_nop 6\n" \
  "v_mfma_f32_16x16x32_bf16 v[8:11], v[60:63], v[36:39], v[8:11]\n" \
  "ds_read_b64_tr_b16 v[80:81], v109 offset:15424\n" \
  "ds_read_b64_tr_b16 v[82:83], v109 offset:17984\n" \
  "s_nop 6\n" \
  "v_mfma_f32_16x16x32_bf16 v[12:15], v[64:67], v[36:39], v[12:15]\n" \
  "ds_read_b64_tr_b16 v[84:85], v109 offset:15456\n" \
  "ds_read_b64_tr_b16 v[86:87], v109 offset:18016\n" \
  "s_nop 6\n" \
  "v_mfma_f32_16x16x32_bf16 v[16:19], v[68:71], v[36:39], v[16:19]\n" \
  "ds_read_b64_tr_b16 v[88:89], v109 offset:15488\n" \
  "ds_read_b64_tr_b16 v[90:91], v109 offset:18048\n" \
  "s_nop 6\n" \
  "s_waitcnt vmcnt(16) lgkmcnt(0)\n" \
  "v_mfma_f32_16x16x32_bf16 v[0:3], v[72:75], v[40:43], v[0:3]\n" \
  "global_load_dwordx4 v[36:39], v116, s[44:45] sc1\n" \
  "s_add_u32 s44, s44, 4096\n" \
  "s_addc_u32 s45, s45, 0\n" \
  "s_nop 5\n" \
  "v_mfma_f32_16x16x32_bf16 v[4:7], v[76:79], v[40:43], v[4:7]\n" \
  "s_nop 8\n" \
  "v_mfma_f32_16x16x32_bf16 v[8:11], v[80:83], v[40:43], v[8:11]\n" \
  "s_nop 8\n" \
  "v_mfma_f32_16x16x32_bf16 v[12:15], v[84:87], v[40:43], v[12:15]\n" \
  "s_nop 8\n" \
  "v_mfma_f32_16x16x32_bf16 v[16:19], v[88:91], v[40:43], v[16:19]\n" \
  "s_nop 8\n" \
  "s_waitcnt vmcnt(3) lgkmcnt(0)\n" \
  "s_barrier\n" \
  "s_mov_b32 m0, s57\n" \
  "s_add_u32 s57, s57, 28672\n" \
  "global_load_lds_dwordx4 v105, s[66:67] offset:-4096 sc1\n" \
  "global_load_lds_dwordx4 v105, s[66:67] offset:-3072 sc1\n" \
  "global_load_lds_dwordx4 v105, s[66:67] offset:-2048 sc1\n" \
  "global_load_lds_dwordx4 v105, s[66:67] offset:-1024 sc1\n" \
  "global_load_lds_dwordx4 v105, s[66:67] offset:0 sc1\n" \
  "global_load_lds_dwordx4 v105, s[66:67] offset:1024 sc1\n" \
  "global_load_lds_dwordx4 v105, s[66:67] offset:2048 sc1\n" \
  "s_add_u32 s66, s66, 28672\n" \
  "s_addc_u32 s67, s67, 0\n" \
  "ds_read_b64_tr_b16 v[52:53], v109 offset:20480\n" \
  "ds_read_b64_tr_b16 v[54:55], v109 offset:23040\n" \
  "ds_read_b64_tr_b16 v[56:57], v109 offset:20512\n" \
  "ds_read_b64_tr_b16 v[58:59], v109 offset:23072\n" \
  "ds_read_b64_tr_b16 v[60:61], v109 offset:20544\n" \
  "ds_read_b64_tr_b16 v[62:63], v109 offset:23104\n" \
  "ds_read_b64_tr_b16 v[64:65], v109 offset:20576\n" \
  "ds_read_b64_tr_b16 v[66:67], v109 offset:23136\n" \
  "ds_read_b64_tr_b16 v[68:69], v109 offset:20608\n" \
  "ds_read_b64_tr_b16 v[70:71], v109 offset:23168\n" \
  "s_waitcnt lgkmcnt(0)\n" \
  "v_mfma_f32_16x16x32_bf16 v[0:3], v[52:55], v[44:47], v[0:3]\n" \
  "ds_read_b64_tr_b16 v[72:73], v109 offset:25600\n" \
  "ds_read_b64_tr_b16 v[74:75], v109 offset:28160\n" \
  "global_load_dwordx4 v[40:43], v116, s[44:45] sc1\n" \
  "s_add_u32 s44, s44, 4096\n" \
  "s_addc_u32 s45, s45, 0\n" \
  "s_nop 3\n" \
  "v_mfma_f32_16x16x32_bf16 v[4:7], v[56:59], v[44:47], v[4:7]\n" \
  "ds_read_b64_tr_b16 v[76:77], v109 offset:25632\n" \
  "ds_read_b64_tr_b16 v[78:79], v109 offset:28192\n" \
  "s_add_u32 m0, s53, 0\n" \
  "s_nop 0\n" \
  "global_load_lds_dwordx4 v100, s[40:41]\n" \
  "s_nop 3\n" \
  "v_mfma_f32_16x16x32_bf16 v[8:11], v[60:63], v[44:47], v[8:11]\n" \
  "ds_read_b64_tr_b16 v[80:81], v109 offset:25664\n" \
  "ds_read_b64_tr_b16 v[82:83], v109 offset:28224\n" \
  "s_nop 6\n" \
  "v_mfma_f32_16x16x32_bf16 v[12:15], v[64:67], v[44:47], v[12:15]\n" \
  "ds_read_b64_tr_b16 v[84:85], v109 offset:25696\n" \
  "ds_read_b64_tr_b16 v[86:87], v109 offset:28256\n" \
  "s_add_u32 m0, s53, 1024\n" \
  "s_nop 0\n" \
  "global_load_lds_dwordx4 v101, s[40:41]\n" \
  "s_nop 3\n" \
  "v_mfma_f32_16x16x32_bf16 v[16:19], v[68:71], v[44:47], v[16:19]\n" \
  "ds_read_b64_tr_b16 v[88:89], v109 offset:25728\n" \
  "ds_read_b64_tr_b16 v[90:91], v109 offset:28288\n" \
  "s_nop 6\n" \
  "s_waitcnt lgkmcnt(0)\n" \
  "v_mfma_f32_16x16x32_bf16 v[0:3], v[72:75], v[48:51], v[0:3]\n" \
  "ds_read_b64_tr_b16 v[52:53], v109 offset:30720\n" \
  "ds_read_b64_tr_b16 v[54:55], v109 offset:33280\n" \
  "global_load_dwordx4 v[44:47], v116, s[44:45] sc1\n" \
  "s_add_u32 s44, s44, 4096\n" \
  "s_addc_u32 s45, s45, 0\n" \
  "s_nop 3\n" \
  "v_mfma_f32_16x16x32_bf16 v[4:7], v[76:79], v[48:51], v[4:7]\n" \
  "ds_read_b64_tr_b16 v[56:57], v109 offset:30752\n" \
  "ds_read_b64_tr_b16 v[58:59], v109 offset:33312\n" \
  "s_add_u32 m0, s53, 2048\n" \
  "s_nop 0\n" \
  "global_load_lds_dwordx4 v102, s[40:41]\n" \
  "s_nop 3\n" \
  "v_mfma_f32_16x16x32_bf16 v[8:11], v[80:83], v[48:51], v[8:11]\n" \
  "ds_read_b64_tr_b16 v[60:61], v109 offset:30784\n" \
  "ds_read_b64_tr_b16 v[62:63], v109 offset:33344\n" \
  "s_nop 6\n" \
  "v_mfma_f32_16x16x32_bf16 v[12:15], v[84:87], v[48:51], v[12:15]\n" \
  "ds_read_b64_tr_b16 v[64:65], v109 offset:30816\n" \
  "ds_read_b64_tr_b16 v[66:67], v109 offset:33376\n" \
  "s_add_u32 m0, s53, 3072\n" \
  "s_nop 0\n" \
  "global_load_lds_dwordx4 v103, s[40:41]\n" \
  "s_nop 3\n" \
  "v_mfma_f32_16x16x32_bf16 v[16:19], v[88:91], v[48:51], v[16:19]\n" \
  "ds_read_b64_tr_b16 v[68:69], v109 offset:30848\n" \
  "ds_read_b64_tr_b16 v[70:71], v109 offset:33408\n" \
  "s_nop 6\n" \
  "s_waitcnt lgkmcnt(0)\n" \
  "v_mfma_f32_16x16x32_bf16 v[0:3], v[52:55], v[20:23], v[0:3]\n" \
  "ds_read_b64_tr_b16 v[72:73], v109 offset:35840\n" \
  "ds_read_b64_tr_b16 v[74:75], v109 offset:38400\n" \
  "global_load_dwordx4 v[48:51], v116, s[44:45] sc1\n" \
  "s_add_u32 s44, s44, 4096\n" \
  "s_addc_u32 s45, s45, 0\n" \
  "s_nop 3\n" \
  "v_mfma_f32_16x16x32_bf16 v[4:7], v[56:59], v[20:23], v[4:7]\n" \
  "ds_read_b64_tr_b16 v[76:77], v109 offset:35872\n" \
  "ds_read_b64_tr_b16 v[78:79], v109 offset:38432\n" \
  "s_add_u32 m0, s53, 4096\n" \
  "s_nop 0\n" \
  "global_load_lds_dwordx4 v104, s[40:41]\n" \
  "s_add_u32 s40, s40, 163840\n" \
  "s_addc_u32 s41, s41, 0\n" \
  "s_nop 3\n" \
  "v_mfma_f32_16x16x32_bf16 v[8:11], v[60:63], v[20:23], v[8:11]\n" \
  "ds_read_b64_tr_b16 v[80:81], v109 offset:35904\n" \
  "ds_read_b64_tr_b16 v[82:83], v109 offset:38464\n" \
  "s_nop 6\n" \
  "v_mfma_f32_16x16x32_bf16 v[12:15], v[64:67], v[20:23], v[12:15]\n" \
  "ds_read_b64_tr_b16 v[84:85], v109 offset:35936\n" \
  "ds_read_b64_tr_b16 v[86:87], v109 offset:38496\n" \
  "s_add_u32 m0, s54, 0\n" \
  "s_nop 0\n" \
  "global_load_lds_dwordx4 v100, s[40:41]\n" \
  "s_nop 3\n" \
  "v_mfma_f32_16x16x32_bf16 v[16:19], v[68:71], v[20:23], v[16:19]\n" \
  "ds_read_b64_tr_b16 v[88:89], v109 offset:35968\n" \
  "ds_read_b64_tr_b16 v[90:91], v109 offset:38528\n" \
  "s_nop 6\n" \
  "s_waitcnt lgkmcnt(0)\n" \
  "v_mfma_f32_16x16x32_bf16 v[0:3], v[72:75], v[24:27], v[0:3]\n" \
  "ds_read_b64_tr_b16 v[52:53], v109 offset:40960\n" \
  "ds_read_b64_tr_b16 v[54:55], v109 offset:43520\n" \
  "global_load_dwordx4 v[20:23], v116, s[44:45] sc1\n" \
  "s_add_u32 s44, s44, 4096\n" \
  "s_addc_u32 s45, s45, 0\n" \
  "s_nop 3\n" \
  "v_mfma_f32_16x16x32_bf16 v[4:7], v[76:79], v[24:27], v[4:7]\n" \
  "ds_read_b64_tr_b16 v[56:57], v109 offset:40992\n" \
  "ds_read_b64_tr_b16 v[58:59], v109 offset:43552\n" \
  "s_add_u32 m0, s54, 1024\n" \
  "s_nop 0\n" \
  "global_load_lds_dwordx4 v101, s[40:41]\n" \
  "s_nop 3\n" \
  "v_mfma_f32_16x16x32_bf16 v[8:11], v[80:83], v[24:27], v[8:11]\n" \
  "ds_read_b64_tr_b16 v[60:61], v109 offset:41024\n" \
  "ds_read_b64_tr_b16 v[62:63], v109 offset:43584\n" \
  "s_nop 6\n" \
  "v_mfma_f32_16x16x32_bf16 v[12:15], v[84:87], v[24:27], v[12:15]\n" \
  "ds_read_b64_tr_b16 v[64:65], v109 offset:41056\n" \
  "ds_read_b64_tr_b16 v[66:67], v109 offset:43616\n" \
  "s_add_u32 m0, s54, 2048\n" \
  "s_nop 0\n" \
  "global_load_lds_dwordx4 v102, s[40:41]\n" \
  "s_nop 3\n" \
  "v_mfma_f32_16x16x32_bf16 v[16:19], v[88:91], v[24:27], v[16:19]\n" \
  "ds_read_b64_tr_b16 v[68:69], v109 offset:41088\n" \
  "ds_read_b64_tr_b16 v[70:71], v109 offset:43648\n" \
  "s_nop 6\n" \
  "s_waitcnt vmcnt(21) lgkmcnt(0)\n" \
  "v_mfma_f32_16x16x32_bf16 v[0:3], v[52:55], v[28:31], v[0:3]\n" \
  "ds_read_b64_tr_b16 v[72:73], v109 offset:46080\n" \
  "ds_read_b64_tr_b16 v[74:75], v109 offset:48640\n" \
  "global_load_dwordx4 v[24:27], v116, s[44:45] sc1\n" \
  "s_add_u32 s44, s44, 4096\n" \
  "s_addc_u32 s45, s45, 0\n" \
  "s_nop 3\n" \
  "v_mfma_f32_16x16x32_bf16 v[4:7], v[56:59], v[28:31], v[4:7]\n" \
  "ds_read_b64_tr_b16 v[76:77], v109 offset:46112\n" \
  "ds_read_b64_tr_b16 v[78:79], v109 offset:48672\n" \
  "s_add_u32 m0, s54, 3072\n" \
  "s_nop 0\n" \
  "global_load_lds_dwordx4 v103, s[40:41]\n" \
  "s_nop 3\n" \
  "v_mfma_f32_16x16x32_bf16 v[8:11], v[60:63], v[28:31], v[8:11]\n" \
  "ds_read_b64_tr_b16 v[80:81], v109 offset:46144\n" \
  "ds_read_b64_tr_b16 v[82:83], v109 offset:48704\n" \
  "s_nop 6\n" \
  "v_mfma_f32_16x16x32_bf16 v[12:15], v[64:67], v[28:31], v[12:15]\n" \
  "ds_read_b64_tr_b16 v[84:85], v109 offset:46176\n" \
  "ds_read_b64_tr_b16 v[86:87], v109 offset:48736\n" \
  "s_add_u32 m0, s54, 4096\n" \
  "s_nop 0\n" \
  "global_load_lds_dwordx4 v104, s[40:41]\n" \
  "s_add_u32 s40, s40, 163840\n" \
  "s_addc_u32 s41, s41, 0\n" \
  "s_nop 3\n" \
  "v_mfma_f32_16x16x32_bf16 v[16:19], v[68:71], v[28:31], v[16:19]\n" \
  "ds_read_b64_tr_b16 v[88:89], v109 offset:46208\n" \
  "ds_read_b64_tr_b16 v[90:91], v109 offset:48768\n" \
  "s_nop 6\n" \
  "s_waitcnt vmcnt(23) lgkmcnt(0)\n" \
  "v_mfma_f32_16x16x32_bf16 v[0:3], v[72:75], v[32:35], v[0:3]\n" \
  "ds_read_b64_tr_b16 v[52:53], v108 offset:0\n" \
  "ds_read_b64_tr_b16 v[54:55], v108 offset:2560\n" \
  "global_load_dwordx4 v[28:31], v116, s[44:45] sc1\n" \
  "s_add_u32 s44, s44, 4096\n" \
  "s_addc_u32 s45, s45, 0\n" \
  "s_nop 3\n" \
  "v_mfma_f32_16x16x32_bf16 v[4:7], v[76:79], v[32:35], v[4:7]\n" \
  "ds_read_b64_tr_b16 v[56:57], v108 offset:32\n" \
  "ds_read_b64_tr_b16 v[58:59], v108 offset:2592\n" \
  "s_nop 6\n" \
  "v_mfma_f32_16x16x32_bf16 v[8:11], v[80:83], v[32:35], v[8:11]\n" \
  "ds_read_b64_tr_b16 v[60:61], v108 offset:64\n" \
  "ds_read_b64_tr_b16 v[62:63], v108 offset:2624\n" \
  "s_nop 6\n" \
  "v_mfma_f32_16x16x32_bf16 v[12:15], v[84:87], v[32:35], v[12:15]\n" \
  "ds_read_b64_tr_b16 v[64:65], v108 offset:96\n" \
  "ds_read_b64_tr_b16 v[66:67], v108 offset:2656\n" \
  "s_nop 6\n" \
  "v_mfma_f32_16x16x32_bf16 v[16:19], v[88:91], v[32:35], v[16:19]\n" \
  "ds_read_b64_tr_b16 v[68:69], v108 offset:128\n" \
  "ds_read_b64_tr_b16 v[70:71], v108 offset:2688\n" \
  "s_nop 6\n" \
  "s_waitcnt vmcnt(23) lgkmcnt(0)\n" \
  "v_mfma_f32_16x16x32_bf16 v[0:3], v[52:55], v[36:39], v[0:3]\n" \
  "ds_read_b64_tr_b16 v[72:73], v108 offset:5120\n" \
  "ds_read_b64_tr_b16 v[74:75], v108 offset:7680\n" \
  "global_load_dwordx4 v[32:35], v116, s[44:45] sc1\n" \
  "s_add_u32 s44, s44, 4096\n" \
  "s_addc_u32 s45, s45, 0\n" \
  "s_nop 3\n" \
  "v_mfma_f32_16x16x32_bf16 v[4:7], v[56:59], v[36:39], v[4:7]\n" \
  "ds_read_b64_tr_b16 v[76:77], v108 offset:5152\n" \
  "ds_read_b64_tr_b16 v[78:79], v108 offset:7712\n" \
  "s_nop 6\n" \
  "v_mfma_f32_16x16x32_bf16 v[8:11], v[60:63], v[36:39], v[8:11]\n" \
  "ds_read_b64_tr_b16 v[80:81], v108 offset:5184\n" \
  "ds_read_b64_tr_b16 v[82:83], v108 offset:7744\n" \
  "s_nop 6\n" \
  "v_mfma_f32_16x16x32_bf16 v[12:15], v[64:67], v[36:39], v[12:15]\n" \
  "ds_read_b64_tr_b16 v[84:85], v108 offset:5216\n" \
  "ds_read_b64_tr_b16 v[86:87], v108 offset:7776\n" \
  "s_nop 6\n" \
  "v_mfma_f32_16x16x32_bf16 v[16:19], v[68:71], v[36:39], v[16:19]\n" \
  "ds_read_b64_tr_b16 v[88:89], v108 offset:5248\n" \
  "ds_read_b64_tr_b16 v[90:91], v108 offset:7808\n" \
  "s_nop 6\n" \
  "s_waitcnt vmcnt(16) lgkmcnt(0)\n" \
  "v_mfma_f32_16x16x32_bf16 v[0:3], v[72:75], v[40:43], v[0:3]\n" \
  "global_load_dwordx4 v[36:39], v116, s[44:45] sc1\n" \
  "s_add_u32 s44, s44, 4096\n" \
  "s_addc_u32 s45, s45, 0\n" \
  "s_nop 5\n" \
  "v_mfma_f32_16x16x32_bf16 v[4:7], v[76:79], v[40:43], v[4:7]\n" \
  "s_nop 8\n" \
  "v_mfma_f32_16x16x32_bf16 v[8:11], v[80:83], v[40:43], v[8:11]\n" \
  "s_nop 8\n" \
  "v_mfma_f32_16x16x32_bf16 v[12:15], v[84:87], v[40:43], v[12:15]\n" \
  "s_nop 8\n" \
  "v_mfma_f32_16x16x32_bf16 v[16:19], v[88:91], v[40:43], v[16:19]\n" \
  "s_nop 8\n" \
  "s_waitcnt vmcnt(3) lgkmcnt(0)\n" \
  "s_barrier\n" \
  "s_mov_b32 m0, s57\n" \
  "s_sub_u32 s57, s57, 28672\n" \
  "global_load_lds_dwordx4 v105, s[66:67] offset:-4096 sc1\n" \
  "global_load_lds_dwordx4 v105, s[66:67] offset:-3072 sc1\n" \
  "global_load_lds_dwordx4 v105, s[66:67] offset:-2048 sc1\n" \
  "global_load_lds_dwordx4 v105, s[66:67] offset:-1024 sc1\n" \
  "global_load_lds_dwordx4 v105, s[66:67] offset:0 sc1\n" \
  "global_load_lds_dwordx4 v105, s[66:67] offset:1024 sc1\n" \
  "global_load_lds_dwordx4 v105, s[66:67] offset:2048 sc1\n" \
  "s_add_u32 s66, s66, 28672\n" \
  "s_addc_u32 s67, s67, 0\n" \
  "s_cmp_lg_u32 s72, 0\n" \
  "s_cbranch_scc1 12f\n" \
  "v_lshlrev_b32 v96, 3, v116\n" \
  "s_mov_b32 s61, 262144\n" \
  "10:\n" \
  "global_load_dwordx2 v[92:93], v96, s[74:75] sc1\n" \
  "s_waitcnt vmcnt(0)\n" \
  "v_cmp_eq_u64 vcc, s[48:49], v[92:93]\n" \
  "s_cmp_eq_u64 vcc, exec\n" \
  "s_cbranch_scc0 13f\n" \
  "v_add_u32 v96, 8192, v96\n" \
  "global_load_dwordx2 v[92:93], v96, s[74:75] sc1\n" \
  "s_waitcnt vmcnt(0)\n" \
  "v_cmp_eq_u64 vcc, s[48:49], v[92:93]\n" \
  "s_cmp_eq_u64 vcc, exec\n" \
  "s_cbranch_scc0 13f\n" \
  "v_add_u32 v96, 8192, v96\n" \
  "global_load_dwordx2 v[92:93], v96, s[74:75] sc1\n" \
  "s_waitcnt vmcnt(0)\n" \
  "v_cmp_eq_u64 vcc, s[48:49], v[92:93]\n" \
  "s_cmp_eq_u64 vcc, exec\n" \
  "s_cbranch_scc0 13f\n" \
  "v_add_u32 v96, 8192, v96\n" \
  "global_load_dwordx2 v[92:93], v96, s[74:75] sc1\n" \
  "s_waitcnt vmcnt(0)\n" \
  "v_cmp_eq_u64 vcc, s[48:49], v[92:93]\n" \
  "s_cmp_eq_u64 vcc, exec\n" \
  "s_cbranch_scc0 13f\n" \
  "v_add_u32 v96, 8192, v96\n" \
  "s_branch 11f\n" \
  "13:\n" \
  "v_lshlrev_b32 v96, 3, v116\n" \
  "s_sub_u32 s61, s61, 1\n" \
  "s_cbranch_scc0 10b\n" \
  "11:\n" \
  "v_mov_b32 v92, 0\n" \
  "v_mov_b32 v94, s48\n" \
  "v_mov_b32 v95, s49\n" \
  "s_mov_b64 exec, 1\n" \
  "global_store_dwordx2 v92, v[94:95], s[76:77] sc1\n" \
  "s_mov_b64 exec, -1\n" \
  "12:\n" \
  "ds_read_b64_tr_b16 v[52:53], v108 offset:10240\n" \
  "ds_read_b64_tr_b16 v[54:55], v108 offset:12800\n" \
  "ds_read_b64_tr_b16 v[56:57], v108 offset:10272\n" \
  "ds_read_b64_tr_b16 v[58:59], v108 offset:12832\n" \
  "ds_read_b64_tr_b16 v[60:61], v108 offset:10304\n" \
  "ds_read_b64_tr_b16 v[62:63], v108 offset:12864\n" \
  "ds_read_b64_tr_b16 v[64:65], v108 offset:10336\n" \
  "ds_read_b64_tr_b16 v[66:67], v108 offset:12896\n" \
  "ds_read_b64_tr_b16 v[68:69], v108 offset:10368\n" \
  "ds_read_b64_tr_b16 v[70:71], v108 offset:12928\n" \
  "s_waitcnt lgkmcnt(0)\n" \
  "v_mfma_f32_16x16x32_bf16 v[0:3], v[52:55], v[44:47], v[0:3]\n" \
  "ds_read_b64_tr_b16 v[72:73], v108 offset:15360\n" \
  "ds_read_b64_tr_b16 v[74:75], v108 offset:17920\n" \
  "global_load_dwordx4 v[40:43], v116, s[44:45] sc1\n" \
  "s_add_u32 s44, s44, 4096\n" \
  "s_addc_u32 s45, s45, 0\n" \
  "s_nop 3\n" \
  "v_mfma_f32_16x16x32_bf16 v[4:7], v[56:59], v[44:47], v[4:7]\n" \
  "ds_read_b64_tr_b16 v[76:77], v108 offset:15392\n" \
  "ds_read_b64_tr_b16 v[78:79], v108 offset:17952\n" \
  "s_add_u32 m0, s55, 0\n" \
  "s_nop 0\n" \
  "global_load_lds_dwordx4 v100, s[40:41]\n" \
  "s_nop 3\n" \
  "v_mfma_f32_16x16x32_bf16 v[8:11], v[60:63], v[44:47], v[8:11]\n" \
  "ds_read_b64_tr_b16 v[80:81], v108 offset:15424\n" \
  "ds_read_b64_tr_b16 v[82:83], v108 offset:17984\n" \
  "s_nop 6\n" \
  "v_mfma_f32_16x16x32_bf16 v[12:15], v[64:67], v[44:47], v[12:15]\n" \
  "ds_read_b64_tr_b16 v[84:85], v108 offset:15456\n" \
  "ds_read_b64_tr_b16 v[86:87], v108 offset:18016\n" \
  "s_add_u32 m0, s55, 1024\n" \
  "s_nop 0\n" \
  "global_load_lds_dwordx4 v101, s[40:41]\n" \
  "s_nop 3\n" \
  "v_mfma_f32_16x16x32_bf16 v[16:19], v[68:71], v[44:47], v[16:19]\n" \
  "ds_read_b64_tr_b16 v[88:89], v108 offset:15488\n" \
  "ds_read_b64_tr_b16 v[90:91], v108 offset:18048\n" \
  "s_nop 6\n" \
  "s_waitcnt lgkmcnt(0)\n" \
  "v_mfma_f32_16x16x32_bf16 v[0:3], v[72:75], v[48:51], v[0:3]\n" \
  "ds_read_b64_tr_b16 v[52:53], v108 offset:20480\n" \
  "ds_read_b64_tr_b16 v[54:55], v108 offset:23040\n" \
  "global_load_dwordx4 v[44:47], v116, s[44:45] sc1\n" \
  "s_add_u32 s44, s44, 4096\n" \
  "s_addc_u32 s45, s45, 0\n" \
  "s_nop 3\n" \
  "v_mfma_f32_16x16x32_bf16 v[4:7], v[76:79], v[48:51], v[4:7]\n" \
  "ds_read_b64_tr_b16 v[56:57], v108 offset:20512\n" \
  "ds_read_b64_tr_b16 v[58:59], v108 offset:23072\n" \
  "s_add_u32 m0, s55, 2048\n" \
  "s_nop 0\n" \
  "global_load_lds_dwordx4 v102, s[40:41]\n" \
  "s_nop 3\n" \
  "v_mfma_f32_16x16x32_bf16 v[8:11], v[80:83], v[48:51], v[8:11]\n" \
  "ds_read_b64_tr_b16 v[60:61], v108 offset:20544\n" \
  "ds_read_b64_tr_b16 v[62:63], v108 offset:23104\n" \
  "s_nop 6\n" \
  "v_mfma_f32_16x16x32_bf16 v[12:15], v[84:87], v[48:51], v[12:15]\n" \
  "ds_read_b64_tr_b16 v[64:65], v108 offset:20576\n" \
  "ds_read_b64_tr_b16 v[66:67], v108 offset:23136\n" \
  "s_add_u32 m0, s55, 3072\n" \
  "s_nop 0\n" \
  "global_load_lds_dwordx4 v103, s[40:41]\n" \
  "s_nop 3\n" \
  "v_mfma_f32_16x16x32_bf16 v[16:19], v[88:91], v[48:51], v[16:19]\n" \
  "ds_read_b64_tr_b16 v[68:69], v108 offset:20608\n" \
  "ds_read_b64_tr_b16 v[70:71], v108 offset:23168\n" \
  "s_nop 6\n" \
  "s_waitcnt lgkmcnt(0)\n" \
  "v_mfma_f32_16x16x32_bf16 v[0:3], v[52:55], v[20:23], v[0:3]\n" \
  "ds_read_b64_tr_b16 v[72:73], v108 offset:25600\n" \
  "ds_read_b64_tr_b16 v[74:75], v108 offset:28160\n" \
  "global_load_dwordx4 v[48:51], v116, s[44:45] sc1\n" \
  "s_add_u32 s44, s44, 4096\n" \
  "s_addc_u32 s45, s45, 0\n" \
  "s_nop 3\n" \
  "v_mfma_f32_16x16x32_bf16 v[4:7], v[56:59], v[20:23], v[4:7]\n" \
  "ds_read_b64_tr_b16 v[76:77], v108 offset:25632\n" \
  "ds_read_b64_tr_b16 v[78:79], v108 offset:28192\n" \
  "s_add_u32 m0, s55, 4096\n" \
  "s_nop 0\n" \
  "global_load_lds_dwordx4 v104, s[40:41]\n" \
  "s_add_u32 s40, s40, 163840\n" \
  "s_addc_u32 s41, s41, 0\n" \
  "s_nop 3\n" \
  "v_mfma_f32_16x16x32_bf16 v[8:11], v[60:63], v[20:23], v[8:11]\n" \
  "ds_read_b64_tr_b16 v[80:81], v108 offset:25664\n" \
  "ds_read_b64_tr_b16 v[82:83], v108 offset:28224\n" \
  "s_nop 6\n" \
  "v_mfma_f32_16x16x32_bf16 v[12:15], v[64:67], v[20:23], v[12:15]\n" \
  "ds_read_b64_tr_b16 v[84:85], v108 offset:25696\n" \
  "ds_read_b64_tr_b16 v[86:87], v108 offset:28256\n" \
  "s_add_u32 m0, s56, 0\n" \
  "s_nop 0\n" \
  "global_load_lds_dwordx4 v100, s[40:41]\n" \
  "s_nop 3\n" \
  "v_mfma_f32_16x16x32_bf16 v[16:19], v[68:71], v[20:23], v[16:19]\n" \
  "ds_read_b64_tr_b16 v[88:89], v108 offset:25728\n" \
  "ds_read_b64_tr_b16 v[90:91], v108 offset:28288\n" \
  "s_nop 6\n" \
  "s_waitcnt lgkmcnt(0)\n" \
  "v_mfma_f32_16x16x32_bf16 v[0:3], v[72:75], v[24:27], v[0:3]\n" \
  "ds_read_b64_tr_b16 v[52:53], v108 offset:30720\n" \
  "ds_read_b64_tr_b16 v[54:55], v108 offset:33280\n" \
  "global_load_dwordx4 v[20:23], v116, s[44:45] sc1\n" \
  "s_add_u32 s44, s44, 4096\n" \
  "s_addc_u32 s45, s45, 0\n" \
  "s_nop 3\n" \
  "v_mfma_f32_16x16x32_bf16 v[4:7], v[76:79], v[24:27], v[4:7]\n" \
  "ds_read_b64_tr_b16 v[56:57], v108 offset:30752\n" \
  "ds_read_b64_tr_b16 v[58:59], v108 offset:33312\n" \
  "s_add_u32 m0, s56, 1024\n" \
  "s_nop 0\n" \
  "global_load_lds_dwordx4 v101, s[40:41]\n" \
  "s_nop 3\n" \
  "v_mfma_f32_16x16x32_bf16 v[8:11], v[80:83], v[24:27], v[8:11]\n" \
  "ds_read_b64_tr_b16 v[60:61], v108 offset:30784\n" \
  "ds_read_b64_tr_b16 v[62:63], v108 offset:33344\n" \
  "s_nop 6\n" \
  "v_mfma_f32_16x16x32_bf16 v[12:15], v[84:87], v[24:27], v[12:15]\n" \
  "ds_read_b64_tr_b16 v[64:65], v108 offset:30816\n" \
  "ds_read_b64_tr_b16 v[66:67], v108 offset:33376\n" \
  "s_add_u32 m0, s56, 2048\n" \
  "s_nop 0\n" \
  "global_load_lds_dwordx4 v102, s[40:41]\n" \
  "s_nop 3\n" \
  "v_mfma_f32_16x16x32_bf16 v[16:19], v[88:91], v[24:27], v[16:19]\n" \
  "ds_read_b64_tr_b16 v[68:69], v108 offset:30848\n" \
  "ds_read_b64_tr_b16 v[70:71], v108 offset:33408\n" \
  "s_nop 6\n" \
  "s_waitcnt vmcnt(21) lgkmcnt(0)\n" \
  "v_mfma_f32_16x16x32_bf16 v[0:3], v[52:55], v[28:31], v[0:3]\n" \
  "ds_read_b64_tr_b16 v[72:73], v108 offset:35840\n" \
  "ds_read_b64_tr_b16 v[74:75], v108 offset:38400\n" \
  "global_load_dwordx4 v[24:27], v116, s[44:45] sc1\n" \
  "s_add_u32 s44, s44, 4096\n" \
  "s_addc_u32 s45, s45, 0\n" \
  "s_nop 3\n" \
  "v_mfma_f32_16x16x32_bf16 v[4:7], v[56:59], v[28:31], v[4:7]\n" \
  "ds_read_b64_tr_b16 v[76:77], v108 offset:35872\n" \
  "ds_read_b64_tr_b16 v[78:79], v108 offset:38432\n" \
  "s_add_u32 m0, s56, 3072\n" \
  "s_nop 0\n" \
  "global_load_lds_dwordx4 v103, s[40:41]\n" \
  "s_nop 3\n" \
  "v_mfma_f32_16x16x32_bf16 v[8:11], v[60:63], v[28:31], v[8:11]\n" \
  "ds_read_b64_tr_b16 v[80:81], v108 offset:35904\n" \
  "ds_read_b64_tr_b16 v[82:83], v108 offset:38464\n" \
  "s_nop 6\n" \
  "v_mfma_f32_16x16x32_bf16 v[12:15], v[64:67], v[28:31], v[12:15]\n" \
  "ds_read_b64_tr_b16 v[84:85], v108 offset:35936\n" \
  "ds_read_b64_tr_b16 v[86:87], v108 offset:38496\n" \
  "s_add_u32 m0, s56, 4096\n" \
  "s_nop 0\n" \
  "global_load_lds_dwordx4 v104, s[40:41]\n" \
  "s_mov_b64 s[40:41], s[42:43]\n" \
  "s_nop 3\n" \
  "v_mfma_f32_16x16x32_bf16 v[16:19], v[68:71], v[28:31], v[16:19]\n" \
  "ds_read_b64_tr_b16 v[88:89], v108 offset:35968\n" \
  "ds_read_b64_tr_b16 v[90:91], v108 offset:38528\n" \
  "s_nop 6\n" \
  "s_waitcnt vmcnt(23) lgkmcnt(0)\n" \
  "v_mfma_f32_16x16x32_bf16 v[0:3], v[72:75], v[32:35], v[0:3]\n" \
  "ds_read_b64_tr_b16 v[52:53], v108 offset:40960\n" \
  "ds_read_b64_tr_b16 v[54:55], v108 offset:43520\n" \
  "global_load_dwordx4 v[28:31], v116, s[44:45] sc1\n" \
  "s_add_u32 s44, s44, 4096\n" \
  "s_addc_u32 s45, s45, 0\n" \
  "s_nop 3\n" \
  "v_mfma_f32_16x16x32_bf16 v[4:7], v[76:79], v[32:35], v[4:7]\n" \
  "ds_read_b64_tr_b16 v[56:57], v108 offset:40992\n" \
  "ds_read_b64_tr_b16 v[58:59], v108 offset:43552\n" \
  "s_nop 6\n" \
  "v_mfma_f32_16x16x32_bf16 v[8:11], v[80:83], v[32:35], v[8:11]\n" \
  "ds_read_b64_tr_b16 v[60:61], v108 offset:41024\n" \
  "ds_read_b64_tr_b16 v[62:63], v108 offset:43584\n" \
  "s_nop 6\n" \
  "v_mfma_f32_16x16x32_bf16 v[12:15], v[84:87], v[32:35], v[12:15]\n" \
  "ds_read_b64_tr_b16 v[64:65], v108 offset:41056\n" \
  "ds_read_b64_tr_b16 v[66:67], v108 offset:43616\n" \
  "s_nop 6\n" \
  "v_mfma_f32_16x16x32_bf16 v[16:19], v[88:91], v[32:35], v[16:19]\n" \
  "ds_read_b64_tr_b16 v[68:69], v108 offset:41088\n" \
  "ds_read_b64_tr_b16 v[70:71], v108 offset:43648\n" \
  "s_nop 6\n" \
  "s_waitcnt vmcnt(23) lgkmcnt(0)\n" \
  "v_mfma_f32_16x16x32_bf16 v[0:3], v[52:55], v[36:39], v[0:3]\n" \
  "ds_read_b64_tr_b16 v[72:73], v108 offset:46080\n" \
  "ds_read_b64_tr_b16 v[74:75], v108 offset:48640\n" \
  "global_load_dwordx4 v[32:35], v116, s[44:45] sc1\n" \
  "s_add_u32 s44, s44, 4096\n" \
  "s_addc_u32 s45, s45, 0\n" \
  "s_nop 3\n" \
  "v_mfma_f32_16x16x32_bf16 v[4:7], v[56:59], v[36:39], v[4:7]\n" \
  "ds_read_b64_tr_b16 v[76:77], v108 offset:46112\n" \
  "ds_read_b64_tr_b16 v[78:79], v108 offset:48672\n" \
  "s_nop 6\n" \
  "v_mfma_f32_16x16x32_bf16 v[8:11], v[60:63], v[36:39], v[8:11]\n" \
  "ds_read_b64_tr_b16 v[80:81], v108 offset:46144\n" \
  "ds_read_b64_tr_b16 v[82:83], v108 offset:48704\n" \
  "s_nop 6\n" \
  "v_mfma_f32_16x16x32_bf16 v[12:15], v[64:67], v[36:39], v[12:15]\n" \
  "ds_read_b64_tr_b16 v[84:85], v108 offset:46176\n" \
  "ds_read_b64_tr_b16 v[86:87], v108 offset:48736\n" \
  "s_nop 6\n" \
  "v_mfma_f32_16x16x32_bf16 v[16:19], v[68:71], v[36:39], v[16:19]\n" \
  "ds_read_b64_tr_b16 v[88:89], v108 offset:46208\n" \
  "ds_read_b64_tr_b16 v[90:91], v108 offset:48768\n" \
  "s_nop 6\n" \
  "s_waitcnt vmcnt(16) lgkmcnt(0)\n" \
  "v_mfma_f32_16x16x32_bf16 v[0:3], v[72:75], v[40:43], v[0:3]\n" \
  "global_load_dwordx4 v[36:39], v116, s[44:45] sc1\n" \
  "s_add_u32 s44, s44, 4096\n" \
  "s_addc_u32 s45, s45, 0\n" \
  "s_nop 5\n" \
  "v_mfma_f32_16x16x32_bf16 v[4:7], v[76:79], v[40:43], v[4:7]\n" \
  "s_nop 8\n" \
  "v_mfma_f32_16x16x32_bf16 v[8:11], v[80:83], v[40:43], v[8:11]\n" \
  "s_nop 8\n" \
  "v_mfma_f32_16x16x32_bf16 v[12:15], v[84:87], v[40:43], v[12:15]\n" \
  "s_nop 8\n" \
  "v_mfma_f32_16x16x32_bf16 v[16:19], v[88:91], v[40:43], v[16:19]\n" \
  "s_nop 8\n" \
  "s_waitcnt vmcnt(3) lgkmcnt(0)\n" \
  "s_barrier\n" \
  "s_mov_b32 m0, s58\n" \
  "s_nop 0\n" \
  "global_load_lds_dwordx4 v106, s[66:67] offset:-4096 sc1\n" \
  "global_load_lds_dwordx4 v106, s[66:67] offset:-3072 sc1\n" \
  "ds_read_b64_tr_b16 v[52:53], v109 offset:0\n" \
  "ds_read_b64_tr_b16 v[54:55], v109 offset:2560\n" \
  "ds_read_b64_tr_b16 v[56:57], v109 offset:32\n" \
  "ds_read_b64_tr_b16 v[58:59], v109 offset:2592\n" \
  "ds_read_b64_tr_b16 v[60:61], v109 offset:64\n" \
  "ds_read_b64_tr_b16 v[62:63], v109 offset:2624\n" \
  "ds_read_b64_tr_b16 v[64:65], v109 offset:96\n" \
  "ds_read_b64_tr_b16 v[66:67], v109 offset:2656\n" \
  "ds_read_b64_tr_b16 v[68:69], v109 offset:128\n" \
  "ds_read_b64_tr_b16 v[70:71], v109 offset:2688\n" \
  "s_waitcnt lgkmcnt(0)\n" \
  "v_mfma_f32_16x16x32_bf16 v[0:3], v[52:55], v[44:47], v[0:3]\n" \
  "ds_read_b64_tr_b16 v[72:73], v109 offset:5120\n" \
  "ds_read_b64_tr_b16 v[74:75], v109 offset:7680\n" \
  "global_load_dwordx4 v[40:43], v116, s[44:45] sc1\n" \
  "s_add_u32 s44, s44, 4096\n" \
  "s_addc_u32 s45, s45, 0\n" \
  "s_nop 3\n" \
  "v_mfma_f32_16x16x32_bf16 v[4:7], v[56:59], v[44:47], v[4:7]\n" \
  "ds_read_b64_tr_b16 v[76:77], v109 offset:5152\n" \
  "ds_read_b64_tr_b16 v[78:79], v109 offset:7712\n" \
  "s_add_u32 m0, s52, 0\n" \
  "s_nop 0\n" \
  "global_load_lds_dwordx4 v100, s[40:41]\n" \
  "s_nop 3\n" \
  "v_mfma_f32_16x16x32_bf16 v[8:11], v[60:63], v[44:47], v[8:11]\n" \
  "ds_read_b64_tr_b16 v[80:81], v109 offset:5184\n" \
  "ds_read_b64_tr_b16 v[82:83], v109 offset:7744\n" \
  "s_nop 6\n" \
  "v_mfma_f32_16x16x32_bf16 v[12:15], v[64:67], v[44:47], v[12:15]\n" \
  "ds_read_b64_tr_b16 v[84:85], v109 offset:5216\n" \
  "ds_read_b64_tr_b16 v[86:87], v109 offset:7776\n" \
  "s_add_u32 m0, s52, 1024\n" \
  "s_nop 0\n" \
  "global_load_lds_dwordx4 v101, s[40:41]\n" \
  "s_nop 3\n" \
  "v_mfma_f32_16x16x32_bf16 v[16:19], v[68:71], v[44:47], v[16:19]\n" \
  "ds_read_b64_tr_b16 v[88:89], v109 offset:5248\n" \
  "ds_read_b64_tr_b16 v[90:91], v109 offset:7808\n" \
  "s_nop 6\n" \
  "s_waitcnt lgkmcnt(0)\n" \
  "v_mfma_f32_16x16x32_bf16 v[0:3], v[72:75], v[48:51], v[0:3]\n" \
  "global_load_dwordx4 v[44:47], v116, s[44:45] sc1\n" \
  "s_add_u32 s44, s44, 4096\n" \
  "s_addc_u32 s45, s45, 0\n" \
  "s_nop 5\n" \
  "v_mfma_f32_16x16x32_bf16 v[4:7], v[76:79], v[48:51], v[4:7]\n" \
  "s_add_u32 m0, s52, 2048\n" \
  "s_nop 0\n" \
  "global_load_lds_dwordx4 v102, s[40:41]\n" \
  "s_nop 5\n" \
  "v_mfma_f32_16x16x32_bf16 v[8:11], v[80:83], v[48:51], v[8:11]\n" \
  "s_nop 8\n" \
  "v_mfma_f32_16x16x32_bf16 v[12:15], v[84:87], v[48:51], v[12:15]\n" \
  "s_add_u32 m0, s52, 3072\n" \
  "s_nop 0\n" \
  "global_load_lds_dwordx4 v103, s[40:41]\n" \
  "s_nop 5\n" \
  "v_mfma_f32_16x16x32_bf16 v[16:19], v[88:91], v[48:51], v[16:19]\n" \
  "s_nop 8\n" \
  "s_add_u32 m0, s52, 4096\n" \
  "s_nop 0\n" \
  "global_load_lds_dwordx4 v104, s[40:41]\n" \
  "s_add_u32 s40, s40, 163840\n" \
  "s_addc_u32 s41, s41, 0\n" \
  "s_add_u32 m0, s53, 0\n" \
  "s_nop 0\n" \
  "global_load_lds_dwordx4 v100, s[40:41]\n" \
  "s_add_u32 m0, s53, 1024\n" \
  "s_nop 0\n" \
  "global_load_lds_dwordx4 v101, s[40:41]\n" \
  "s_add_u32 m0, s53, 2048\n" \
  "s_nop 0\n" \
  "global_load_lds_dwordx4 v102, s[40:41]\n" \
  "s_add_u32 m0, s53, 3072\n" \
  "s_nop 0\n" \
  "global_load_lds_dwordx4 v103, s[40:41]\n" \
  "s_add_u32 m0, s53, 4096\n" \
  "s_nop 0\n" \
  "global_load_lds_dwordx4 v104, s[40:41]\n" \
  "s_add_u32 s40, s40, 163840\n" \
  "s_addc_u32 s41, s41, 0\n" \
  "s_add_u32 m0, s80, 0\n" \
  "s_nop 0\n" \
  "global_load_lds_dwordx4 v100, s[40:41]\n" \
  "s_add_u32 m0, s80, 1024\n" \
  "s_nop 0\n" \
  "global_load_lds_dwordx4 v101, s[40:41]\n" \
  "s_add_u32 m0, s80, 2048\n" \
  "s_nop 0\n" \
  "global_load_lds_dwordx4 v102, s[40:41]\n" \
  "s_add_u32 m0, s80, 3072\n" \
  "s_nop 0\n" \
  "global_load_lds_dwordx4 v103, s[40:41]\n" \
  "s_add_u32 m0, s80, 4096\n" \
  "s_nop 0\n" \
  "global_load_lds_dwordx4 v104, s[40:41]\n" \
  "s_add_u32 s40, s40, 163840\n" \
  "s_addc_u32 s41, s41, 0\n" \
  "global_load_dwordx4 v[48:51], v116, s[44:45] sc1\n" \
  "s_add_u32 s44, s44, 4096\n" \
  "s_addc_u32 s45, s45, 0\n" \
  "s_waitcnt vmcnt(18) lgkmcnt(0)\n" \
  "s_barrier\n" \
  "s_add_u32 m0, s81, 0\n" \
  "s_nop 0\n" \
  "global_load_lds_dwordx4 v100, s[40:41]\n" \
  "s_add_u32 m0, s81, 1024\n" \
  "s_nop 0\n" \
  "global_load_lds_dwordx4 v101, s[40:41]\n" \
  "s_add_u32 m0, s81, 2048\n" \
  "s_nop 0\n" \
  "global_load_lds_dwordx4 v102, s[40:41]\n" \
  "s_add_u32 m0, s81, 3072\n" \
  "s_nop 0\n" \
  "global_load_lds_dwordx4 v103, s[40:41]\n" \
  "s_add_u32 m0, s81, 4096\n" \
  "s_nop 0\n" \
  "global_load_lds_dwordx4 v104, s[40:41]\n" \
  "s_add_u32 s40, s40, 163840\n" \
  "s_addc_u32 s41, s41, 0\n" \
  "s_add_u32 m0, s82, 0\n" \
  "s_nop 0\n" \
  "global_load_lds_dwordx4 v100, s[40:41]\n" \
  "s_add_u32 m0, s82, 1024\n" \
  "s_nop 0\n" \
  "global_load_lds_dwordx4 v101, s[40:41]\n" \
  "s_add_u32 m0, s82, 2048\n" \
  "s_nop 0\n" \
  "global_load_lds_dwordx4 v102, s[40:41]\n" \
  "s_add_u32 m0, s82, 3072\n" \
  "s_nop 0\n" \
  "global_load_lds_dwordx4 v103, s[40:41]\n" \
  "s_add_u32 m0, s82, 4096\n" \
  "s_nop 0\n" \
  "global_load_lds_dwordx4 v104, s[40:41]\n" \
  "s_add_u32 s40, s40, 163840\n" \
  "s_addc_u32 s41, s41, 0\n" \
  "ds_read_b64_tr_b16 v[52:53], v109 offset:10240\n" \
  "ds_read_b64_tr_b16 v[54:55], v109 offset:12800\n" \
  "ds_read_b64_tr_b16 v[56:57], v109 offset:10272\n" \
  "ds_read_b64_tr_b16 v[58:59], v109 offset:12832\n" \
  "ds_read_b64_tr_b16 v[60:61], v109 offset:10304\n" \
  "ds_read_b64_tr_b16 v[62:63], v109 offset:12864\n" \
  "ds_read_b64_tr_b16 v[64:65], v109 offset:10336\n" \
  "ds_read_b64_tr_b16 v[66:67], v109 offset:12896\n" \
  "ds_read_b64_tr_b16 v[68:69], v109 offset:10368\n" \
  "ds_read_b64_tr_b16 v[70:71], v109 offset:12928\n" \
  "s_waitcnt lgkmcnt(4)\n" \
  "ds_read_b64_tr_b16 v[72:73], v109 offset:15360\n" \
  "ds_read_b64_tr_b16 v[74:75], v109 offset:17920\n" \
  "ds_read_b64_tr_b16 v[76:77], v109 offset:15392\n" \
  "ds_read_b64_tr_b16 v[78:79], v109 offset:17952\n" \
  "ds_read_b64_tr_b16 v[80:81], v109 offset:15424\n" \
  "ds_read_b64_tr_b16 v[82:83], v109 offset:17984\n" \
  "ds_read_b64_tr_b16 v[84:85], v109 offset:15456\n" \
  "ds_read_b64_tr_b16 v[86:87], v109 offset:18016\n" \
  "ds_read_b64_tr_b16 v[88:89], v109 offset:15488\n" \
  "ds_read_b64_tr_b16 v[90:91], v109 offset:18048\n" \
  "s_waitcnt lgkmcnt(4)\n" \
  "ds_read_b64_tr_b16 v[126:127], v109 offset:20480\n" \
  "ds_read_b64_tr_b16 v[128:129], v109 offset:23040\n" \
  "ds_read_b64_tr_b16 v[130:131], v109 offset:20512\n" \
  "ds_read_b64_tr_b16 v[132:133], v109 offset:23072\n" \
  "ds_read_b64_tr_b16 v[134:135], v109 offset:20544\n" \
  "ds_read_b64_tr_b16 v[136:137], v109 offset:23104\n" \
  "ds_read_b64_tr_b16 v[138:139], v109 offset:20576\n" \
  "ds_read_b64_tr_b16 v[140:141], v109 offset:23136\n" \
  "ds_read_b64_tr_b16 v[142:143], v109 offset:20608\n" \
  "ds_read_b64_tr_b16 v[144:145], v109 offset:23168\n" \
  "s_waitcnt lgkmcnt(4)\n" \
  "ds_read_b64_tr_b16 v[146:147], v109 offset:25600\n" \
  "ds_read_b64_tr_b16 v[148:149], v109 offset:28160\n" \
  "ds_read_b64_tr_b16 v[150:151], v109 offset:25632\n" \
  "ds_read_b64_tr_b16 v[152:153], v109 offset:28192\n" \
  "ds_read_b64_tr_b16 v[154:155], v109 offset:25664\n" \
  "ds_read_b64_tr_b16 v[156:157], v109 offset:28224\n" \
  "ds_read_b64_tr_b16 v[158:159], v109 offset:25696\n" \
  "ds_read_b64_tr_b16 v[160:161], v109 offset:28256\n" \
  "ds_read_b64_tr_b16 v[162:163], v109 offset:25728\n" \
  "ds_read_b64_tr_b16 v[164:165], v109 offset:28288\n" \
  "s_waitcnt lgkmcnt(4)\n" \
  "ds_read_b64_tr_b16 v[166:167], v109 offset:30720\n" \
  "ds_read_b64_tr_b16 v[168:169], v109 offset:33280\n" \
  "ds_read_b64_tr_b16 v[170:171], v109 offset:30752\n" \
  "ds_read_b64_tr_b16 v[172:173], v109 offset:33312\n" \
  "ds_read_b64_tr_b16 v[174:175], v109 offset:30784\n" \
  "ds_read_b64_tr_b16 v[176:177], v109 offset:33344\n" \
  "ds_read_b64_tr_b16 v[178:179], v109 offset:30816\n" \
  "ds_read_b64_tr_b16 v[180:181], v109 offset:33376\n" \
  "ds_read_b64_tr_b16 v[182:183], v109 offset:30848\n" \
  "ds_read_b64_tr_b16 v[184:185], v109 offset:33408\n" \
  "s_waitcnt lgkmcnt(4)\n" \
  "s_setprio 0\n" \
  "s_waitcnt lgkmcnt(0)\n" \
  "s_barrier\n" \
  "v_mfma_f32_16x16x32_bf16 v[0:3], v[52:55], v[20:23], v[0:3]\n" \
  "v_mfma_f32_16x16x32_bf16 v[4:7], v[56:59], v[20:23], v[4:7]\n" \
  "ds_read_b64_tr_b16 v[52:53], v109 offset:35840\n" \
  "ds_read_b64_tr_b16 v[54:55], v109 offset:38400\n" \
  "v_mfma_f32_16x16x32_bf16 v[8:11], v[60:63], v[20:23], v[8:11]\n" \
  "ds_read_b64_tr_b16 v[56:57], v109 offset:35872\n" \
  "ds_read_b64_tr_b16 v[58:59], v109 offset:38432\n" \
  "v_mfma_f32_16x16x32_bf16 v[12:15], v[64:67], v[20:23], v[12:15]\n" \
  "ds_read_b64_tr_b16 v[60:61], v109 offset:35904\n" \
  "ds_read_b64_tr_b16 v[62:63], v109 offset:38464\n" \
  "v_mfma_f32_16x16x32_bf16 v[16:19], v[68:71], v[20:23], v[16:19]\n" \
  "ds_read_b64_tr_b16 v[64:65], v109 offset:35936\n" \
  "ds_read_b64_tr_b16 v[66:67], v109 offset:38496\n" \
  "s_nop 0\n" \
  "ds_read_b64_tr_b16 v[68:69], v109 offset:35968\n" \
  "ds_read_b64_tr_b16 v[70:71], v109 offset:38528\n" \
  "v_mfma_f32_16x16x32_bf16 v[0:3], v[72:75], v[24:27], v[0:3]\n" \
  "v_mfma_f32_16x16x32_bf16 v[4:7], v[76:79], v[24:27], v[4:7]\n" \
  "ds_read_b64_tr_b16 v[72:73], v109 offset:40960\n" \
  "ds_read_b64_tr_b16 v[74:75], v109 offset:43520\n" \
  "v_mfma_f32_16x16x32_bf16 v[8:11], v[80:83], v[24:27], v[8:11]\n" \
  "ds_read_b64_tr_b16 v[76:77], v109 offset:40992\n" \
  "ds_read_b64_tr_b16 v[78:79], v109 offset:43552\n" \
  "v_mfma_f32_16x16x32_bf16 v[12:15], v[84:87], v[24:27], v[12:15]\n" \
  "ds_read_b64_tr_b16 v[80:81], v109 offset:41024\n" \
  "ds_read_b64_tr_b16 v[82:83], v109 offset:43584\n" \
  "v_mfma_f32_16x16x32_bf16 v[16:19], v[88:91], v[24:27], v[16:19]\n" \
  "ds_read_b64_tr_b16 v[84:85], v109 offset:41056\n" \
  "ds_read_b64_tr_b16 v[86:87], v109 offset:43616\n" \
  "s_nop 0\n" \
  "ds_read_b64_tr_b16 v[88:89], v109 offset:41088\n" \
  "ds_read_b64_tr_b16 v[90:91], v109 offset:43648\n" \
  "v_mfma_f32_16x16x32_bf16 v[0:3], v[126:129], v[28:31], v[0:3]\n" \
  "v_mfma_f32_16x16x32_bf16 v[4:7], v[130:133], v[28:31], v[4:7]\n" \
  "ds_read_b64_tr_b16 v[126:127], v109 offset:46080\n" \
  "ds_read_b64_tr_b16 v[128:129], v109 offset:48640\n" \
  "v_mfma_f32_16x16x32_bf16 v[8:11], v[134:137], v[28:31], v[8:11]\n" \
  "ds_read_b64_tr_b16 v[130:131], v109 offset:46112\n" \
  "ds_read_b64_tr_b16 v[132:133], v109 offset:48672\n" \
  "v_mfma_f32_16x16x32_bf16 v[12:15], v[138:141], v[28:31], v[12:15]\n" \
  "ds_read_b64_tr_b16 v[134:135], v109 offset:46144\n" \
  "ds_read_b64_tr_b16 v[136:137], v109 offset:48704\n" \
  "v_mfma_f32_16x16x32_bf16 v[16:19], v[142:145], v[28:31], v[16:19]\n" \
  "ds_read_b64_tr_b16 v[138:139], v109 offset:46176\n" \
  "ds_read_b64_tr_b16 v[140:141], v109 offset:48736\n" \
  "s_nop 0\n" \
  "ds_read_b64_tr_b16 v[142:143], v109 offset:46208\n" \
  "ds_read_b64_tr_b16 v[144:145], v109 offset:48768\n" \
  "v_mfma_f32_16x16x32_bf16 v[0:3], v[146:149], v[32:35], v[0:3]\n" \
  "v_mfma_f32_16x16x32_bf16 v[4:7], v[150:153], v[32:35], v[4:7]\n" \
  "v_mfma_f32_16x16x32_bf16 v[8:11], v[154:157], v[32:35], v[8:11]\n" \
  "v_mfma_f32_16x16x32_bf16 v[12:15], v[158:161], v[32:35], v[12:15]\n" \
  "v_mfma_f32_16x16x32_bf16 v[16:19], v[162:165], v[32:35], v[16:19]\n" \
  "v_mfma_f32_16x16x32_bf16 v[0:3], v[166:169], v[36:39], v[0:3]\n" \
  "v_mfma_f32_16x16x32_bf16 v[4:7], v[170:173], v[36:39], v[4:7]\n" \
  "v_mfma_f32_16x16x32_bf16 v[8:11], v[174:177], v[36:39], v[8:11]\n" \
  "v_mfma_f32_16x16x32_bf16 v[12:15], v[178:181], v[36:39], v[12:15]\n" \
  "v_mfma_f32_16x16x32_bf16 v[16:19], v[182:185], v[36:39], v[16:19]\n" \
  "s_waitcnt vmcnt(27) lgkmcnt(15)\n" \
  "v_mfma_f32_16x16x32_bf16 v[0:3], v[52:55], v[40:43], v[0:3]\n" \
  "v_mfma_f32_16x16x32_bf16 v[4:7], v[56:59], v[40:43], v[4:7]\n" \
  "v_mfma_f32_16x16x32_bf16 v[8:11], v[60:63], v[40:43], v[8:11]\n" \
  "v_mfma_f32_16x16x32_bf16 v[12:15], v[64:67], v[40:43], v[12:15]\n" \
  "v_mfma_f32_16x16x32_bf16 v[16:19], v[68:71], v[40:43], v[16:19]\n" \
  "s_waitcnt vmcnt(24) lgkmcnt(10)\n" \
  "v_mfma_f32_16x16x32_bf16 v[0:3], v[72:75], v[44:47], v[0:3]\n" \
  "v_mfma_f32_16x16x32_bf16 v[4:7], v[76:79], v[44:47], v[4:7]\n" \
  "v_mfma_f32_16x16x32_bf16 v[8:11], v[80:83], v[44:47], v[8:11]\n" \
  "v_mfma_f32_16x16x32_bf16 v[12:15], v[84:87], v[44:47], v[12:15]\n" \
  "v_mfma_f32_16x16x32_bf16 v[16:19], v[88:91], v[44:47], v[16:19]\n" \
  "s_waitcnt vmcnt(10) lgkmcnt(0)\n" \
  "v_mfma_f32_16x16x32_bf16 v[0:3], v[126:129], v[48:51], v[0:3]\n" \
  "v_mfma_f32_16x16x32_bf16 v[4:7], v[130:133], v[48:51], v[4:7]\n" \
  "v_mfma_f32_16x16x32_bf16 v[8:11], v[134:137], v[48:51], v[8:11]\n" \
  "v_mfma_f32_16x16x32_bf16 v[12:15], v[138:141], v[48:51], v[12:15]\n" \
  "v_mfma_f32_16x16x32_bf16 v[16:19], v[142:145], v[48:51], v[16:19]\n" \
  "s_nop 7\n" \
  "v_mul_f32 v0, v0, v99\n" \
  "v_mul_f32 v1, v1, v99\n" \
  "v_mul_f32 v2, v2, v99\n" \
  "v_mul_f32 v3, v3, v99\n" \
  "v_mul_f32 v4, v4, v99\n" \
  "v_mul_f32 v5, v5, v99\n" \
  "v_mul_f32 v6, v6, v99\n" \
  "v_mul_f32 v7, v7, v99\n" \
  "v_mul_f32 v8, v8, v99\n" \
  "v_mul_f32 v9, v9, v99\n" \
  "v_mul_f32 v10, v10, v99\n" \
  "v_mul_f32 v11, v11, v99\n" \
  "v_mul_f32 v12, v12, v99\n" \
  "v_mul_f32 v13, v13, v99\n" \
  "v_mul_f32 v14, v14, v99\n" \
  "v_mul_f32 v15, v15, v99\n" \
  "v_mul_f32 v16, v16, v99\n" \
  "v_mul_f32 v17, v17, v99\n" \
  "v_mul_f32 v18, v18, v99\n" \
  "v_mul_f32 v19, v19, v99\n" \
  "s_waitcnt vmcnt(0) lgkmcnt(0)\n" \
  "v_mov_b32 v92, 9\n" \
  "ds_write_b32 v110, v92 offset:48\n" \
  "s_mov_b32 s61, 1048576\n" \
  "20:\n" \
  "ds_read_b128 v[92:95], v119 offset:48\n" \
  "ds_read_b128 v[122:125], v119 offset:112\n" \
  "s_waitcnt lgkmcnt(0)\n" \
  "v_min3_u32 v92, v92, v93, v94\n" \
  "v_min3_u32 v122, v122, v123, v124\n" \
  "v_min_u32 v92, v92, v95\n" \
  "v_min_u32 v122, v122, v125\n" \
  "v_cmp_gt_u32 vcc, 9, v92\n" \
  "v_cmp_gt_u32 s[62:63], 0, v122\n" \
  "s_or_b64 vcc, vcc, s[62:63]\n" \
  "s_sub_u32 s61, s61, 1\n" \
  "s_cbranch_scc1 21f\n" \
  "s_cbranch_vccz 21f\n" \
  "s_sleep 0\n" \
  "s_branch 20b\n" \
  "21:\n"
#define XPH2_CLOB "v20", "v21", "v22", "v23", "v24", "v25", "v26", "v27", "v28", "v29", "v30", "v31", "v32", "v33", "v34", "v35", "v36", "v37", "v38", "v39", "v40", "v41", "v42", "v43", "v44", "v45", "v46", "v47", "v48", "v49", "v50", "v51", "v52", "v53", "v54", "v55", "v56", "v57", "v58", "v59", "v60", "v61", "v62", "v63", "v64", "v65", "v66", "v67", "v68", "v69", "v70", "v71", "v72", "v73", "v74", "v75", "v76", "v77", "v78", "v79", "v80", "v81", "v82", "v83", "v84", "v85", "v86", "v87", "v88", "v89", "v90", "v91", "v92", "v93", "v94", "v95", "v96", "v97", "v98", "v99", "v122", "v123", "v124", "v125", "v126", "v127", "v128", "v129", "v130", "v131", "v132", "v133", "v134", "v135", "v136", "v137", "v138", "v139", "v140", "v141", "v142", "v143", "v144", "v145", "v146", "v147", "v148", "v149", "v150", "v151", "v152", "v153", "v154", "v155", "v156", "v157", "v158", "v159", "v160", "v161", "v162", "v163", "v164", "v165", "v166", "v167", "v168", "v169", "v170", "v171", "v172", "v173", "v174", "v175", "v176", "v177", "v178", "v179", "v180", "v181", "v182", "v183", "v184", "v185"
#define TAIL_ASM \
  "s_mov_b32 s83, 262144\n" \
  "s_waitcnt lgkmcnt(0)\n" \
  "ds_read_b64_tr_b16 v[100:101], v200 offset:0\n" \
  "ds_read_b64_tr_b16 v[102:103], v201 offset:0\n" \
  "ds_read_b64_tr_b16 v[104:105], v200 offset:32\n" \
  "ds_read_b64_tr_b16 v[106:107], v201 offset:32\n" \
  "ds_read_b64_tr_b16 v[108:109], v200 offset:64\n" \
  "ds_read_b64_tr_b16 v[110:111], v201 offset:64\n" \
  "ds_read_b64_tr_b16 v[112:113], v200 offset:96\n" \
  "ds_read_b64_tr_b16 v[114:115], v201 offset:96\n" \
  "ds_read_b64_tr_b16 v[116:117], v200 offset:128\n" \
  "ds_read_b64_tr_b16 v[118:119], v201 offset:128\n" \
  "s_waitcnt lgkmcnt(4)\n" \
  "ds_read_b64_tr_b16 v[120:121], v202 offset:0\n" \
  "ds_read_b64_tr_b16 v[122:123], v203 offset:0\n" \
  "ds_read_b64_tr_b16 v[124:125], v202 offset:32\n" \
  "ds_read_b64_tr_b16 v[126:127], v203 offset:32\n" \
  "ds_read_b64_tr_b16 v[128:129], v202 offset:64\n" \
  "ds_read_b64_tr_b16 v[130:131], v203 offset:64\n" \
  "ds_read_b64_tr_b16 v[132:133], v202 offset:96\n" \
  "ds_read_b64_tr_b16 v[134:135], v203 offset:96\n" \
  "ds_read_b64_tr_b16 v[136:137], v202 offset:128\n" \
  "ds_read_b64_tr_b16 v[138:139], v203 offset:128\n" \
  "s_waitcnt lgkmcnt(4)\n" \
  "ds_read_b64_tr_b16 v[140:141], v204 offset:0\n" \
  "ds_read_b64_tr_b16 v[142:143], v205 offset:0\n" \
  "ds_read_b64_tr_b16 v[144:145], v204 offset:32\n" \
  "ds_read_b64_tr_b16 v[146:147], v205 offset:32\n" \
  "ds_read_b64_tr_b16 v[148:149], v204 offset:64\n" \
  "ds_read_b64_tr_b16 v[150:151], v205 offset:64\n" \
  "ds_read_b64_tr_b16 v[152:153], v204 offset:96\n" \
  "ds_read_b64_tr_b16 v[154:155], v205 offset:96\n" \
  "ds_read_b64_tr_b16 v[156:157], v204 offset:128\n" \
  "ds_read_b64_tr_b16 v[158:159], v205 offset:128\n" \
  "s_waitcnt lgkmcnt(4)\n" \
  "ds_read_b64_tr_b16 v[160:161], v206 offset:0\n" \
  "ds_read_b64_tr_b16 v[162:163], v207 offset:0\n" \
  "ds_read_b64_tr_b16 v[164:165], v206 offset:32\n" \
  "ds_read_b64_tr_b16 v[166:167], v207 offset:32\n" \
  "ds_read_b64_tr_b16 v[168:169], v206 offset:64\n" \
  "ds_read_b64_tr_b16 v[170:171], v207 offset:64\n" \
  "ds_read_b64_tr_b16 v[172:173], v206 offset:96\n" \
  "ds_read_b64_tr_b16 v[174:175], v207 offset:96\n" \
  "ds_read_b64_tr_b16 v[176:177], v206 offset:128\n" \
  "ds_read_b64_tr_b16 v[178:179], v207 offset:128\n" \
  "s_waitcnt lgkmcnt(4)\n" \
  "ds_read_b64_tr_b16 v[180:181], v208 offset:0\n" \
  "ds_read_b64_tr_b16 v[182:183], v209 offset:0\n" \
  "ds_read_b64_tr_b16 v[184:185], v208 offset:32\n" \
  "ds_read_b64_tr_b16 v[186:187], v209 offset:32\n" \
  "ds_read_b64_tr_b16 v[188:189], v208 offset:64\n" \
  "ds_read_b64_tr_b16 v[190:191], v209 offset:64\n" \
  "ds_read_b64_tr_b16 v[192:193], v208 offset:96\n" \
  "ds_read_b64_tr_b16 v[194:195], v209 offset:96\n" \
  "ds_read_b64_tr_b16 v[196:197], v208 offset:128\n" \
  "ds_read_b64_tr_b16 v[198:199], v209 offset:128\n" \
  "s_waitcnt lgkmcnt(4)\n" \
  "v_mov_b32 v22, 159744\n" \
  "v_mov_b32 v23, 1\n" \
  "s_cmp_lg_u32 %[wv], 0\n" \
  "s_cbranch_scc1 S_%=\n" \
  "s_sleep 16\n" \
  "global_load_dwordx2 v[20:21], %[pa], %[pb] sc1\n" \
  "s_sleep 6\n" \
  "P_%=:\n" \
  "global_load_dwordx2 v[24:25], %[pa], %[pb] sc1\n" \
  "s_waitcnt vmcnt(1)\n" \
  "v_cmp_eq_u64 vcc, %[tag], v[20:21]\n" \
  "s_cmp_eq_u64 vcc, exec\n" \
  "s_cbranch_scc1 T_%=\n" \
  "global_load_dwordx2 v[20:21], %[pa], %[pb] sc1\n" \
  "s_waitcnt vmcnt(1)\n" \
  "v_cmp_eq_u64 vcc, %[tag], v[24:25]\n" \
  "s_cmp_eq_u64 vcc, exec\n" \
  "s_cbranch_scc1 T_%=\n" \
  "s_sub_u32 s83, s83, 1\n" \
  "s_cbranch_scc0 P_%=\n" \
  "T_%=:\n" \
  "ds_write_b32 v22, v23 offset:64\n" \
  "s_branch Q_%=\n" \
  "S_%=:\n" \
  "s_mov_b32 s83, 8388608\n" \
  "U_%=:\n" \
  "ds_read_b32 v20, v22 offset:64\n" \
  "s_waitcnt lgkmcnt(0)\n" \
  "v_cmp_ne_u32 vcc, 0, v20\n" \
  "s_sub_u32 s83, s83, 1\n" \
  "s_cbranch_scc1 Q_%=\n" \
  "s_nop 1\n" \
  "s_cbranch_vccz U_%=\n" \
  "Q_%=:\n" \
  "global_load_dwordx4 v[20:23], %[oo], %[ob0] offset:-1024 sc1\n" \
  "global_load_dwordx4 v[24:27], %[oo], %[ob0] offset:0 sc1\n" \
  "global_load_dwordx4 v[28:31], %[oo], %[ob0] offset:1024 sc1\n" \
  "global_load_dwordx4 v[32:35], %[oo], %[ob0] offset:2048 sc1\n" \
  "global_load_dwordx4 v[36:39], %[oo], %[ob0] offset:3072 sc1\n" \
  "global_load_dwordx4 v[40:43], %[oo], %[ob1] offset:-1024 sc1\n" \
  "global_load_dwordx4 v[44:47], %[oo], %[ob1] offset:0 sc1\n" \
  "global_load_dwordx4 v[48:51], %[oo], %[ob1] offset:1024 sc1\n" \
  "global_load_dwordx4 v[52:55], %[oo], %[ob1] offset:2048 sc1\n" \
  "global_load_dwordx4 v[56:59], %[oo], %[ob1] offset:3072 sc1\n" \
  "global_load_dwordx4 v[60:63], %[oo], %[ob2] offset:-1024 sc1\n" \
  "global_load_dwordx4 v[64:67], %[oo], %[ob2] offset:0 sc1\n" \
  "global_load_dwordx4 v[68:71], %[oo], %[ob2] offset:1024 sc1\n" \
  "global_load_dwordx4 v[72:75], %[oo], %[ob2] offset:2048 sc1\n" \
  "global_load_dwordx4 v[76:79], %[oo], %[ob2] offset:3072 sc1\n" \
  "global_load_dwordx4 v[80:83], %[oo], %[ob3] offset:-1024 sc1\n" \
  "global_load_dwordx4 v[84:87], %[oo], %[ob3] offset:0 sc1\n" \
  "global_load_dwordx4 v[88:91], %[oo], %[ob3] offset:1024 sc1\n" \
  "global_load_dwordx4 v[92:95], %[oo], %[ob3] offset:2048 sc1\n" \
  "global_load_dwordx4 v[96:99], %[oo], %[ob3] offset:3072 sc1\n" \
  "s_waitcnt vmcnt(19) lgkmcnt(0)\n" \
  "v_mfma_f32_16x16x32_bf16 v[0:3], v[100:103], v[20:23], v[0:3]\n" \
  "v_add_u32 v200, %[d1], v200\n" \
  "v_add_u32 v201, %[d1], v201\n" \
  "v_mfma_f32_16x16x32_bf16 v[4:7], v[104:107], v[20:23], v[4:7]\n" \
  "ds_read_b64_tr_b16 v[100:101], v200 offset:0\n" \
  "ds_read_b64_tr_b16 v[102:103], v201 offset:0\n" \
  "v_mfma_f32_16x16x32_bf16 v[8:11], v[108:111], v[20:23], v[8:11]\n" \
  "ds_read_b64_tr_b16 v[104:105], v200 offset:32\n" \
  "ds_read_b64_tr_b16 v[106:107], v201 offset:32\n" \
  "v_mfma_f32_16x16x32_bf16 v[12:15], v[112:115], v[20:23], v[12:15]\n" \
  "ds_read_b64_tr_b16 v[108:109], v200 offset:64\n" \
  "ds_read_b64_tr_b16 v[110:111], v201 offset:64\n" \
  "v_mfma_f32_16x16x32_bf16 v[16:19], v[116:119], v[20:23], v[16:19]\n" \
  "ds_read_b64_tr_b16 v[112:113], v200 offset:96\n" \
  "ds_read_b64_tr_b16 v[114:115], v201 offset:96\n" \
  "s_nop 0\n" \
  "ds_read_b64_tr_b16 v[116:117], v200 offset:128\n" \
  "ds_read_b64_tr_b16 v[118:119], v201 offset:128\n" \
  "s_waitcnt vmcnt(18)\n" \
  "v_mfma_f32_16x16x32_bf16 v[0:3], v[120:123], v[24:27], v[0:3]\n" \
  "v_add_u32 v202, %[d1], v202\n" \
  "v_add_u32 v203, %[d1], v203\n" \
  "v_mfma_f32_16x16x32_bf16 v[4:7], v[124:127], v[24:27], v[4:7]\n" \
  "ds_read_b64_tr_b16 v[120:121], v202 offset:0\n" \
  "ds_read_b64_tr_b16 v[122:123], v203 offset:0\n" \
  "v_mfma_f32_16x16x32_bf16 v[8:11], v[128:131], v[24:27], v[8:11]\n" \
  "ds_read_b64_tr_b16 v[124:125], v202 offset:32\n" \
  "ds_read_b64_tr_b16 v[126:127], v203 offset:32\n" \
  "v_mfma_f32_16x16x32_bf16 v[12:15], v[132:135], v[24:27], v[12:15]\n" \
  "ds_read_b64_tr_b16 v[128:129], v202 offset:64\n" \
  "ds_read_b64_tr_b16 v[130:131], v203 offset:64\n" \
  "v_mfma_f32_16x16x32_bf16 v[16:19], v[136:139], v[24:27], v[16:19]\n" \
  "ds_read_b64_tr_b16 v[132:133], v202 offset:96\n" \
  "ds_read_b64_tr_b16 v[134:135], v203 offset:96\n" \
  "s_nop 0\n" \
  "ds_read_b64_tr_b16 v[136:137], v202 offset:128\n" \
  "ds_read_b64_tr_b16 v[138:139], v203 offset:128\n" \
  "s_waitcnt vmcnt(17)\n" \
  "v_mfma_f32_16x16x32_bf16 v[0:3], v[140:143], v[28:31], v[0:3]\n" \
  "v_add_u32 v204, %[d1], v204\n" \
  "v_add_u32 v205, %[d1], v205\n" \
  "v_mfma_f32_16x16x32_bf16 v[4:7], v[144:147], v[28:31], v[4:7]\n" \
  "ds_read_b64_tr_b16 v[140:141], v204 offset:0\n" \
  "ds_read_b64_tr_b16 v[142:143], v205 offset:0\n" \
  "v_mfma_f32_16x16x32_bf16 v[8:11], v[148:151], v[28:31], v[8:11]\n" \
  "ds_read_b64_tr_b16 v[144:145], v204 offset:32\n" \
  "ds_read_b64_tr_b16 v[146:147], v205 offset:32\n" \
  "v_mfma_f32_16x16x32_bf16 v[12:15], v[152:155], v[28:31], v[12:15]\n" \
  "ds_read_b64_tr_b16 v[148:149], v204 offset:64\n" \
  "ds_read_b64_tr_b16 v[150:151], v205 offset:64\n" \
  "v_mfma_f32_16x16x32_bf16 v[16:19], v[156:159], v[28:31], v[16:19]\n" \
  "ds_read_b64_tr_b16 v[152:153], v204 offset:96\n" \
  "ds_read_b64_tr_b16 v[154:155], v205 offset:96\n" \
  "s_nop 0\n" \
  "ds_read_b64_tr_b16 v[156:157], v204 offset:128\n" \
  "ds_read_b64_tr_b16 v[158:159], v205 offset:128\n" \
  "s_waitcnt vmcnt(16)\n" \
  "v_mfma_f32_16x16x32_bf16 v[0:3], v[160:163], v[32:35], v[0:3]\n" \
  "v_add_u32 v206, %[d1], v206\n" \
  "v_add_u32 v207, %[d1], v207\n" \
  "v_mfma_f32_16x16x32_bf16 v[4:7], v[164:167], v[32:35], v[4:7]\n" \
  "ds_read_b64_tr_b16 v[160:161], v206 offset:0\n" \
  "ds_read_b64_tr_b16 v[162:163], v207 offset:0\n" \
  "v_mfma_f32_16x16x32_bf16 v[8:11], v[168:171], v[32:35], v[8:11]\n" \
  "ds_read_b64_tr_b16 v[164:165], v206 offset:32\n" \
  "ds_read_b64_tr_b16 v[166:167], v207 offset:32\n" \
  "v_mfma_f32_16x16x32_bf16 v[12:15], v[172:175], v[32:35], v[12:15]\n" \
  "ds_read_b64_tr_b16 v[168:169], v206 offset:64\n" \
  "ds_read_b64_tr_b16 v[170:171], v207 offset:64\n" \
  "v_mfma_f32_16x16x32_bf16 v[16:19], v[176:179], v[32:35], v[16:19]\n" \
  "ds_read_b64_tr_b16 v[172:173], v206 offset:96\n" \
  "ds_read_b64_tr_b16 v[174:175], v207 offset:96\n" \
  "s_nop 0\n" \
  "ds_read_b64_tr_b16 v[176:177], v206 offset:128\n" \
  "ds_read_b64_tr_b16 v[178:179], v207 offset:128\n" \
  "s_waitcnt vmcnt(15)\n" \
  "v_mfma_f32_16x16x32_bf16 v[0:3], v[180:183], v[36:39], v[0:3]\n" \
  "v_add_u32 v208, %[d1], v208\n" \
  "v_add_u32 v209, %[d1], v209\n" \
  "v_mfma_f32_16x16x32_bf16 v[4:7], v[184:187], v[36:39], v[4:7]\n" \
  "ds_read_b64_tr_b16 v[180:181], v208 offset:0\n" \
  "ds_read_b64_tr_b16 v[182:183], v209 offset:0\n" \
  "v_mfma_f32_16x16x32_bf16 v[8:11], v[188:191], v[36:39], v[8:11]\n" \
  "ds_read_b64_tr_b16 v[184:185], v208 offset:32\n" \
  "ds_read_b64_tr_b16 v[186:187], v209 offset:32\n" \
  "v_mfma_f32_16x16x32_bf16 v[12:15], v[192:195], v[36:39], v[12:15]\n" \
  "ds_read_b64_tr_b16 v[188:189], v208 offset:64\n" \
  "ds_read_b64_tr_b16 v[190:191], v209 offset:64\n" \
  "v_mfma_f32_16x16x32_bf16 v[16:19], v[196:199], v[36:39], v[16:19]\n" \
  "ds_read_b64_tr_b16 v[192:193], v208 offset:96\n" \
  "ds_read_b64_tr_b16 v[194:195], v209 offset:96\n" \
  "s_nop 0\n" \
  "ds_read_b64_tr_b16 v[196:197], v208 offset:128\n" \
  "ds_read_b64_tr_b16 v[198:199], v209 offset:128\n" \
  "s_waitcnt vmcnt(14) lgkmcnt(10)\n" \
  "v_mfma_f32_16x16x32_bf16 v[0:3], v[100:103], v[40:43], v[0:3]\n" \
  "v_add_u32 v200, %[d2], v200\n" \
  "v_add_u32 v201, %[d2], v201\n" \
  "v_mfma_f32_16x16x32_bf16 v[4:7], v[104:107], v[40:43], v[4:7]\n" \
  "ds_read_b64_tr_b16 v[100:101], v200 offset:0\n" \
  "ds_read_b64_tr_b16 v[102:103], v201 offset:0\n" \
  "v_mfma_f32_16x16x32_bf16 v[8:11], v[108:111], v[40:43], v[8:11]\n" \
  "ds_read_b64_tr_b16 v[104:105], v200 offset:32\n" \
  "ds_read_b64_tr_b16 v[106:107], v201 offset:32\n" \
  "v_mfma_f32_16x16x32_bf16 v[12:15], v[112:115], v[40:43], v[12:15]\n" \
  "ds_read_b64_tr_b16 v[108:109], v200 offset:64\n" \
  "ds_read_b64_tr_b16 v[110:111], v201 offset:64\n" \
  "v_mfma_f32_16x16x32_bf16 v[16:19], v[116:119], v[40:43], v[16:19]\n" \
  "ds_read_b64_tr_b16 v[112:113], v200 offset:96\n" \
  "ds_read_b64_tr_b16 v[114:115], v201 offset:96\n" \
  "s_nop 0\n" \
  "ds_read_b64_tr_b16 v[116:117], v200 offset:128\n" \
  "ds_read_b64_tr_b16 v[118:119], v201 offset:128\n" \
  "s_waitcnt vmcnt(13) lgkmcnt(10)\n" \
  "v_mfma_f32_16x16x32_bf16 v[0:3], v[120:123], v[44:47], v[0:3]\n" \
  "v_add_u32 v202, %[d2], v202\n" \
  "v_add_u32 v203, %[d2], v203\n" \
  "v_mfma_f32_16x16x32_bf16 v[4:7], v[124:127], v[44:47], v[4:7]\n" \
  "ds_read_b64_tr_b16 v[120:121], v202 offset:0\n" \
  "ds_read_b64_tr_b16 v[122:123], v203 offset:0\n" \
  "v_mfma_f32_16x16x32_bf16 v[8:11], v[128:131], v[44:47], v[8:11]\n" \
  "ds_read_b64_tr_b16 v[124:125], v202 offset:32\n" \
  "ds_read_b64_tr_b16 v[126:127], v203 offset:32\n" \
  "v_mfma_f32_16x16x32_bf16 v[12:15], v[132:135], v[44:47], v[12:15]\n" \
  "ds_read_b64_tr_b16 v[128:129], v202 offset:64\n" \
  "ds_read_b64_tr_b16 v[130:131], v203 offset:64\n" \
  "v_mfma_f32_16x16x32_bf16 v[16:19], v[136:139], v[44:47], v[16:19]\n" \
  "ds_read_b64_tr_b16 v[132:133], v202 offset:96\n" \
  "ds_read_b64_tr_b16 v[134:135], v203 offset:96\n" \
  "s_nop 0\n" \
  "ds_read_b64_tr_b16 v[136:137], v202 offset:128\n" \
  "ds_read_b64_tr_b16 v[138:139], v203 offset:128\n" \
  "s_waitcnt vmcnt(12) lgkmcnt(10)\n" \
  "v_mfma_f32_16x16x32_bf16 v[0:3], v[140:143], v[48:51], v[0:3]\n" \
  "v_add_u32 v204, %[d2], v204\n" \
  "v_add_u32 v205, %[d2], v205\n" \
  "v_mfma_f32_16x16x32_bf16 v[4:7], v[144:147], v[48:51], v[4:7]\n" \
  "ds_read_b64_tr_b16 v[140:141], v204 offset:0\n" \
  "ds_read_b64_tr_b16 v[142:143], v205 offset:0\n" \
  "v_mfma_f32_16x16x32_bf16 v[8:11], v[148:151], v[48:51], v[8:11]\n" \
  "ds_read_b64_tr_b16 v[144:145], v204 offset:32\n" \
  "ds_read_b64_tr_b16 v[146:147], v205 offset:32\n" \
  "v_mfma_f32_16x16x32_bf16 v[12:15], v[152:155], v[48:51], v[12:15]\n" \
  "ds_read_b64_tr_b16 v[148:149], v204 offset:64\n" \
  "ds_read_b64_tr_b16 v[150:151], v205 offset:64\n" \
  "v_mfma_f32_16x16x32_bf16 v[16:19], v[156:159], v[48:51], v[16:19]\n" \
  "ds_read_b64_tr_b16 v[152:153], v204 offset:96\n" \
  "ds_read_b64_tr_b16 v[154:155], v205 offset:96\n" \
  "s_nop 0\n" \
  "ds_read_b64_tr_b16 v[156:157], v204 offset:128\n" \
  "ds_read_b64_tr_b16 v[158:159], v205 offset:128\n" \
  "s_waitcnt vmcnt(11) lgkmcnt(10)\n" \
  "v_mfma_f32_16x16x32_bf16 v[0:3], v[160:163], v[52:55], v[0:3]\n" \
  "v_add_u32 v206, %[d2], v206\n" \
  "v_add_u32 v207, %[d2], v207\n" \
  "v_mfma_f32_16x16x32_bf16 v[4:7], v[164:167], v[52:55], v[4:7]\n" \
  "ds_read_b64_tr_b16 v[160:161], v206 offset:0\n" \
  "ds_read_b64_tr_b16 v[162:163], v207 offset:0\n" \
  "v_mfma_f32_16x16x32_bf16 v[8:11], v[168:171], v[52:55], v[8:11]\n" \
  "ds_read_b64_tr_b16 v[164:165], v206 offset:32\n" \
  "ds_read_b64_tr_b16 v[166:167], v207 offset:32\n" \
  "v_mfma_f32_16x16x32_bf16 v[12:15], v[172:175], v[52:55], v[12:15]\n" \
  "ds_read_b64_tr_b16 v[168:169], v206 offset:64\n" \
  "ds_read_b64_tr_b16 v[170:171], v207 offset:64\n" \
  "v_mfma_f32_16x16x32_bf16 v[16:19], v[176:179], v[52:55], v[16:19]\n" \
  "ds_read_b64_tr_b16 v[172:173], v206 offset:96\n" \
  "ds_read_b64_tr_b16 v[174:175], v207 offset:96\n" \
  "s_nop 0\n" \
  "ds_read_b64_tr_b16 v[176:177], v206 offset:128\n" \
  "ds_read_b64_tr_b16 v[178:179], v207 offset:128\n" \
  "s_waitcnt vmcnt(10) lgkmcnt(10)\n" \
  "v_mfma_f32_16x16x32_bf16 v[0:3], v[180:183], v[56:59], v[0:3]\n" \
  "v_add_u32 v208, %[d2], v208\n" \
  "v_add_u32 v209, %[d2], v209\n" \
  "v_mfma_f32_16x16x32_bf16 v[4:7], v[184:187], v[56:59], v[4:7]\n" \
  "ds_read_b64_tr_b16 v[180:181], v208 offset:0\n" \
  "ds_read_b64_tr_b16 v[182:183], v209 offset:0\n" \
  "v_mfma_f32_16x16x32_bf16 v[8:11], v[188:191], v[56:59], v[8:11]\n" \
  "ds_read_b64_tr_b16 v[184:185], v208 offset:32\n" \
  "ds_read_b64_tr_b16 v[186:187], v209 offset:32\n" \
  "v_mfma_f32_16x16x32_bf16 v[12:15], v[192:195], v[56:59], v[12:15]\n" \
  "ds_read_b64_tr_b16 v[188:189], v208 offset:64\n" \
  "ds_read_b64_tr_b16 v[190:191], v209 offset:64\n" \
  "v_mfma_f32_16x16x32_bf16 v[16:19], v[196:199], v[56:59], v[16:19]\n" \
  "ds_read_b64_tr_b16 v[192:193], v208 offset:96\n" \
  "ds_read_b64_tr_b16 v[194:195], v209 offset:96\n" \
  "s_nop 0\n" \
  "ds_read_b64_tr_b16 v[196:197], v208 offset:128\n" \
  "ds_read_b64_tr_b16 v[198:199], v209 offset:128\n" \
  "s_waitcnt vmcnt(9) lgkmcnt(10)\n" \
  "v_mfma_f32_16x16x32_bf16 v[0:3], v[100:103], v[60:63], v[0:3]\n" \
  "v_add_u32 v200, %[d3], v200\n" \
  "v_add_u32 v201, %[d3], v201\n" \
  "v_mfma_f32_16x16x32_bf16 v[4:7], v[104:107], v[60:63], v[4:7]\n" \
  "ds_read_b64_tr_b16 v[100:101], v200 offset:0\n" \
  "ds_read_b64_tr_b16 v[102:103], v201 offset:0\n" \
  "v_mfma_f32_16x16x32_bf16 v[8:11], v[108:111], v[60:63], v[8:11]\n" \
  "ds_read_b64_tr_b16 v[104:105], v200 offset:32\n" \
  "ds_read_b64_tr_b16 v[106:107], v201 offset:32\n" \
  "v_mfma_f32_16x16x32_bf16 v[12:15], v[112:115], v[60:63], v[12:15]\n" \
  "ds_read_b64_tr_b16 v[108:109], v200 offset:64\n" \
  "ds_read_b64_tr_b16 v[110:111], v201 offset:64\n" \
  "v_mfma_f32_16x16x32_bf16 v[16:19], v[116:119], v[60:63], v[16:19]\n" \
  "ds_read_b64_tr_b16 v[112:113], v200 offset:96\n" \
  "ds_read_b64_tr_b16 v[114:115], v201 offset:96\n" \
  "s_nop 0\n" \
  "ds_read_b64_tr_b16 v[116:117], v200 offset:128\n" \
  "ds_read_b64_tr_b16 v[118:119], v201 offset:128\n" \
  "s_waitcnt vmcnt(8) lgkmcnt(10)\n" \
  "v_mfma_f32_16x16x32_bf16 v[0:3], v[120:123], v[64:67], v[0:3]\n" \
  "v_add_u32 v202, %[d3], v202\n" \
  "v_add_u32 v203, %[d3], v203\n" \
  "v_mfma_f32_16x16x32_bf16 v[4:7], v[124:127], v[64:67], v[4:7]\n" \
  "ds_read_b64_tr_b16 v[120:121], v202 offset:0\n" \
  "ds_read_b64_tr_b16 v[122:123], v203 offset:0\n" \
  "v_mfma_f32_16x16x32_bf16 v[8:11], v[128:131], v[64:67], v[8:11]\n" \
  "ds_read_b64_tr_b16 v[124:125], v202 offset:32\n" \
  "ds_read_b64_tr_b16 v[126:127], v203 offset:32\n" \
  "v_mfma_f32_16x16x32_bf16 v[12:15], v[132:135], v[64:67], v[12:15]\n" \
  "ds_read_b64_tr_b16 v[128:129], v202 offset:64\n" \
  "ds_read_b64_tr_b16 v[130:131], v203 offset:64\n" \
  "v_mfma_f32_16x16x32_bf16 v[16:19], v[136:139], v[64:67], v[16:19]\n" \
  "ds_read_b64_tr_b16 v[132:133], v202 offset:96\n" \
  "ds_read_b64_tr_b16 v[134:135], v203 offset:96\n" \
  "s_nop 0\n" \
  "ds_read_b64_tr_b16 v[136:137], v202 offset:128\n" \
  "ds_read_b64_tr_b16 v[138:139], v203 offset:128\n" \
  "s_waitcnt vmcnt(7) lgkmcnt(10)\n" \
  "v_mfma_f32_16x16x32_bf16 v[0:3], v[140:143], v[68:71], v[0:3]\n" \
  "v_add_u32 v204, %[d3], v204\n" \
  "v_add_u32 v205, %[d3], v205\n" \
  "v_mfma_f32_16x16x32_bf16 v[4:7], v[144:147], v[68:71], v[4:7]\n" \
  "ds_read_b64_tr_b16 v[140:141], v204 offset:0\n" \
  "ds_read_b64_tr_b16 v[142:143], v205 offset:0\n" \
  "v_mfma_f32_16x16x32_bf16 v[8:11], v[148:151], v[68:71], v[8:11]\n" \
  "ds_read_b64_tr_b16 v[144:145], v204 offset:32\n" \
  "ds_read_b64_tr_b16 v[146:147], v205 offset:32\n" \
  "v_mfma_f32_16x16x32_bf16 v[12:15], v[152:155], v[68:71], v[12:15]\n" \
  "ds_read_b64_tr_b16 v[148:149], v204 offset:64\n" \
  "ds_read_b64_tr_b16 v[150:151], v205 offset:64\n" \
  "v_mfma_f32_16x16x32_bf16 v[16:19], v[156:159], v[68:71], v[16:19]\n" \
  "ds_read_b64_tr_b16 v[152:153], v204 offset:96\n" \
  "ds_read_b64_tr_b16 v[154:155], v205 offset:96\n" \
  "s_nop 0\n" \
  "ds_read_b64_tr_b16 v[156:157], v204 offset:128\n" \
  "ds_read_b64_tr_b16 v[158:159], v205 offset:128\n" \
  "s_waitcnt vmcnt(6) lgkmcnt(10)\n" \
  "v_mfma_f32_16x16x32_bf16 v[0:3], v[160:163], v[72:75], v[0:3]\n" \
  "v_add_u32 v206, %[d3], v206\n" \
  "v_add_u32 v207, %[d3], v207\n" \
  "v_mfma_f32_16x16x32_bf16 v[4:7], v[164:167], v[72:75], v[4:7]\n" \
  "ds_read_b64_tr_b16 v[160:161], v206 offset:0\n" \
  "ds_read_b64_tr_b16 v[162:163], v207 offset:0\n" \
  "v_mfma_f32_16x16x32_bf16 v[8:11], v[168:171], v[72:75], v[8:11]\n" \
  "ds_read_b64_tr_b16 v[164:165], v206 offset:32\n" \
  "ds_read_b64_tr_b16 v[166:167], v207 offset:32\n" \
  "v_mfma_f32_16x16x32_bf16 v[12:15], v[172:175], v[72:75], v[12:15]\n" \
  "ds_read_b64_tr_b16 v[168:169], v206 offset:64\n" \
  "ds_read_b64_tr_b16 v[170:171], v207 offset:64\n" \
  "v_mfma_f32_16x16x32_bf16 v[16:19], v[176:179], v[72:75], v[16:19]\n" \
  "ds_read_b64_tr_b16 v[172:173], v206 offset:96\n" \
  "ds_read_b64_tr_b16 v[174:175], v207 offset:96\n" \
  "s_nop 0\n" \
  "ds_read_b64_tr_b16 v[176:177], v206 offset:128\n" \
  "ds_read_b64_tr_b16 v[178:179], v207 offset:128\n" \
  "s_waitcnt vmcnt(5) lgkmcnt(10)\n" \
  "v_mfma_f32_16x16x32_bf16 v[0:3], v[180:183], v[76:79], v[0:3]\n" \
  "v_add_u32 v208, %[d3], v208\n" \
  "v_add_u32 v209, %[d3], v209\n" \
  "v_mfma_f32_16x16x32_bf16 v[4:7], v[184:187], v[76:79], v[4:7]\n" \
  "ds_read_b64_tr_b16 v[180:181], v208 offset:0\n" \
  "ds_read_b64_tr_b16 v[182:183], v209 offset:0\n" \
  "v_mfma_f32_16x16x32_bf16 v[8:11], v[188:191], v[76:79], v[8:11]\n" \
  "ds_read_b64_tr_b16 v[184:185], v208 offset:32\n" \
  "ds_read_b64_tr_b16 v[186:187], v209 offset:32\n" \
  "v_mfma_f32_16x16x32_bf16 v[12:15], v[192:195], v[76:79], v[12:15]\n" \
  "ds_read_b64_tr_b16 v[188:189], v208 offset:64\n" \
  "ds_read_b64_tr_b16 v[190:191], v209 offset:64\n" \
  "v_mfma_f32_16x16x32_bf16 v[16:19], v[196:199], v[76:79], v[16:19]\n" \
  "ds_read_b64_tr_b16 v[192:193], v208 offset:96\n" \
  "ds_read_b64_tr_b16 v[194:195], v209 offset:96\n" \
  "s_nop 0\n" \
  "ds_read_b64_tr_b16 v[196:197], v208 offset:128\n" \
  "ds_read_b64_tr_b16 v[198:199], v209 offset:128\n" \
  "s_waitcnt vmcnt(4) lgkmcnt(10)\n" \
  "v_mfma_f32_16x16x32_bf16 v[0:3], v[100:103], v[80:83], v[0:3]\n" \
  "v_mfma_f32_16x16x32_bf16 v[4:7], v[104:107], v[80:83], v[4:7]\n" \
  "v_mfma_f32_16x16x32_bf16 v[8:11], v[108:111], v[80:83], v[8:11]\n" \
  "v_mfma_f32_16x16x32_bf16 v[12:15], v[112:115], v[80:83], v[12:15]\n" \
  "v_mfma_f32_16x16x32_bf16 v[16:19], v[116:119], v[80:83], v[16:19]\n" \
  "s_waitcnt vmcnt(3) lgkmcnt(10)\n" \
  "v_mfma_f32_16x16x32_bf16 v[0:3], v[120:123], v[84:87], v[0:3]\n" \
  "v_mfma_f32_16x16x32_bf16 v[4:7], v[124:127], v[84:87], v[4:7]\n" \
  "v_mfma_f32_16x16x32_bf16 v[8:11], v[128:131], v[84:87], v[8:11]\n" \
  "v_mfma_f32_16x16x32_bf16 v[12:15], v[132:135], v[84:87], v[12:15]\n" \
  "v_mfma_f32_16x16x32_bf16 v[16:19], v[136:139], v[84:87], v[16:19]\n" \
  "s_waitcnt vmcnt(2) lgkmcnt(10)\n" \
  "v_mfma_f32_16x16x32_bf16 v[0:3], v[140:143], v[88:91], v[0:3]\n" \
  "v_mfma_f32_16x16x32_bf16 v[4:7], v[144:147], v[88:91], v[4:7]\n" \
  "v_mfma_f32_16x16x32_bf16 v[8:11], v[148:151], v[88:91], v[8:11]\n" \
  "v_mfma_f32_16x16x32_bf16 v[12:15], v[152:155], v[88:91], v[12:15]\n" \
  "v_mfma_f32_16x16x32_bf16 v[16:19], v[156:159], v[88:91], v[16:19]\n" \
  "s_waitcnt vmcnt(1) lgkmcnt(10)\n" \
  "v_mfma_f32_16x16x32_bf16 v[0:3], v[160:163], v[92:95], v[0:3]\n" \
  "v_mfma_f32_16x16x32_bf16 v[4:7], v[164:167], v[92:95], v[4:7]\n" \
  "v_mfma_f32_16x16x32_bf16 v[8:11], v[168:171], v[92:95], v[8:11]\n" \
  "v_mfma_f32_16x16x32_bf16 v[12:15], v[172:175], v[92:95], v[12:15]\n" \
  "v_mfma_f32_16x16x32_bf16 v[16:19], v[176:179], v[92:95], v[16:19]\n" \
  "s_waitcnt vmcnt(0) lgkmcnt(0)\n" \
  "v_mfma_f32_16x16x32_bf16 v[0:3], v[180:183], v[96:99], v[0:3]\n" \
  "v_mfma_f32_16x16x32_bf16 v[4:7], v[184:187], v[96:99], v[4:7]\n" \
  "v_mfma_f32_16x16x32_bf16 v[8:11], v[188:191], v[96:99], v[8:11]\n" \
  "v_mfma_f32_16x16x32_bf16 v[12:15], v[192:195], v[96:99], v[12:15]\n" \
  "v_mfma_f32_16x16x32_bf16 v[16:19], v[196:199], v[96:99], v[16:19]\n" \
  "s_nop 7\n"
#define TAIL_CLOB "v20", "v21", "v22", "v23", "v24", "v25", "v26", "v27", "v28", "v29", "v30", "v31", "v32", "v33", "v34", "v35", "v36", "v37", "v38", "v39", "v40", "v41", "v42", "v43", "v44", "v45", "v46", "v47", "v48", "v49", "v50", "v51", "v52", "v53", "v54", "v55", "v56", "v57", "v58", "v59", "v60", "v61", "v62", "v63", "v64", "v65", "v66", "v67", "v68", "v69", "v70", "v71", "v72", "v73", "v74", "v75", "v76", "v77", "v78", "v79", "v80", "v81", "v82", "v83", "v84", "v85", "v86", "v87", "v88", "v89", "v90", "v91", "v92", "v93", "v94", "v95", "v96", "v97", "v98", "v99", "v100", "v101", "v102", "v103", "v104", "v105", "v106", "v107", "v108", "v109", "v110", "v111", "v112", "v113", "v114", "v115", "v116", "v117", "v118", "v119", "v120", "v121", "v122", "v123", "v124", "v125", "v126", "v127", "v128", "v129", "v130", "v131", "v132", "v133", "v134", "v135", "v136", "v137", "v138", "v139", "v140", "v141", "v142", "v143", "v144", "v145", "v146", "v147", "v148", "v149", "v150", "v151", "v152", "v153", "v154", "v155", "v156", "v157", "v158", "v159", "v160", "v161", "v162", "v163", "v164", "v165", "v166", "v167", "v168", "v169", "v170", "v171", "v172", "v173", "v174", "v175", "v176", "v177", "v178", "v179", "v180", "v181", "v182", "v183", "v184", "v185", "v186", "v187", "v188", "v189", "v190", "v191", "v192", "v193", "v194", "v195", "v196", "v197", "v198", "v199"
#define OEPI_ASM \
  "v_mov_b32 v34, v8\n" \
  "v_mov_b32 v35, v8\n" \
  "v_mov_b32 v36, v24\n" \
  "v_mov_b32 v37, v24\n" \
  "s_nop 0\n" \
  "v_permlane32_swap_b32 v34, v35\n" \
  "v_permlane32_swap_b32 v36, v37\n" \
  "s_nop 0\n" \
  "v_rcp_f32 v35, v35\n" \
  "v_rcp_f32 v37, v37\n" \
  "s_nop 0\n" \
  "v_mul_f32 v34, v35, v32\n" \
  "v_mul_f32 v36, v37, v32\n" \
  "v_pk_mul_f32 v[0:1], v[0:1], v[34:35] op_sel_hi:[1,0]\n" \
  "v_pk_mul_f32 v[2:3], v[2:3], v[34:35] op_sel_hi:[1,0]\n" \
  "v_pk_mul_f32 v[4:5], v[4:5], v[34:35] op_sel_hi:[1,0]\n" \
  "v_pk_mul_f32 v[6:7], v[6:7], v[34:35] op_sel_hi:[1,0]\n" \
  "v_pk_mul_f32 v[8:9], v[8:9], v[34:35] op_sel_hi:[1,0]\n" \
  "v_pk_mul_f32 v[10:11], v[10:11], v[34:35] op_sel_hi:[1,0]\n" \
  "v_cvt_pk_bf16_f32 v40, v0, v1\n" \
  "v_cvt_pk_bf16_f32 v41, v2, v3\n" \
  "v_cvt_pk_bf16_f32 v42, v4, v5\n" \
  "v_cvt_pk_bf16_f32 v43, v6, v7\n" \
  "v_cvt_pk_bf16_f32 v48, v8, v9\n" \
  "v_cvt_pk_bf16_f32 v49, v10, v11\n" \
  "buffer_store_dwordx4 v[40:43], %[a0], %[rs], 0 offen offset:0 sc1\n" \
  "v_pk_mul_f32 v[16:17], v[16:17], v[36:37] op_sel_hi:[1,0]\n" \
  "v_pk_mul_f32 v[18:19], v[18:19], v[36:37] op_sel_hi:[1,0]\n" \
  "v_pk_mul_f32 v[20:21], v[20:21], v[36:37] op_sel_hi:[1,0]\n" \
  "v_pk_mul_f32 v[22:23], v[22:23], v[36:37] op_sel_hi:[1,0]\n" \
  "v_pk_mul_f32 v[24:25], v[24:25], v[36:37] op_sel_hi:[1,0]\n" \
  "v_pk_mul_f32 v[26:27], v[26:27], v[36:37] op_sel_hi:[1,0]\n" \
  "v_cvt_pk_bf16_f32 v44, v16, v17\n" \
  "v_cvt_pk_bf16_f32 v45, v18, v19\n" \
  "v_cvt_pk_bf16_f32 v46, v20, v21\n" \
  "v_cvt_pk_bf16_f32 v47, v22, v23\n" \
  "v_cvt_pk_bf16_f32 v50, v24, v25\n" \
  "v_cvt_pk_bf16_f32 v51, v26, v27\n" \
  "buffer_store_dwordx4 v[44:47], %[a0], %[rs], 0 offen offset:512 sc1\n" \
  "s_mov_b32 exec_hi, 0\n" \
  "buffer_store_dwordx4 v[48:51], %[y0], %[rs], 0 offen sc1\n" \
  "s_mov_b64 exec, 1\n" \
  "v_mov_b32 v34, 1\n" \
  "s_waitcnt vmcnt(0)\n" \
  "ds_add_rtn_u32 v34, %[ca], v34\n" \
  "s_mov_b64 exec, 1\n" \
  "ds_read_b64 v[36:37], %[ca] offset:36\n" \
  "s_waitcnt lgkmcnt(0)\n" \
  "v_readfirstlane_b32 %[t], v34\n" \
  "s_nop 0\n" \
  "s_cmp_lg_u32 %[t], 3\n" \
  "s_cbranch_scc1 1f\n" \
  "buffer_store_dwordx2 v[36:37], %[fa], %[rs], 0 offen sc1\n" \
  "1:\n" \
  "s_mov_b64 exec, -1\n"
#define OEPI_CLOB "v34", "v35", "v36", "v37", "v38", "v39", "v40", "v41", "v42", "v43", "v44", "v45", "v46", "v47", "v48", "v49", "v50", "v51", "v52", "v53", "v54", "v55"
typedef __attribute__((ext_vector_type(16))) float f32x16;
typedef __attribute__((ext_vector_type(4))) float f32x4;
typedef __attribute__((ext_vector_type(4))) unsigned u32x4;
typedef __attribute__((ext_vector_type(2))) unsigned u32x2;
typedef __attribute__((ext_vector_type(8))) unsigned u32x8;
typedef __attribute__((ext_vector_type(3))) unsigned u32x3;
typedef __attribute__((ext_vector_type(16))) unsigned u32x16;
typedef unsigned long long u64;
#define LAS __attribute__((address_space(3)))
#define GAS __attribute__((address_space(1)))
typedef GAS u64 gu64;
#define RLX_AGENT __ATOMIC_RELAXED, __HIP_MEMORY_SCOPE_AGENT

constexpr int STG = 49152, QLDS = 3 * STG;
constexpr int REC = 3584, KVSLOT = 8 * REC, KVA = 51200;
constexpr int RING23 = 108544, RINGSZ = 25600, WOHI = 67584;
constexpr int CTL = 159744, LDSB = 163840;
constexpr int WS_E = 0, WS_KMAX = 16384, WS_G = 20480, WS_KV = 32768, WS_FKV = 524288, WS_FH = WS_FKV + 32768, WS_FO = WS_FH + 32768;
constexpr int WS_H = 1 << 20, WS_O = 8 << 20, WS_Q = 13 << 20, WS_Q2 = 15 << 20, WS_SINK = 11 << 20, WS_USED = 16 << 20;
constexpr float QSCALE = 0.22360679774997896f * 1.4426950408889634f;
constexpr float MSAFE = 40.f;
constexpr int CODE_LINES = 357;

typedef __attribute__((ext_vector_type(2))) float f32x2;
typedef __attribute__((ext_vector_type(2))) __bf16 bf16x2;
__device__ __forceinline__ unsigned cvtpk(float lo, float hi) { return __builtin_bit_cast(unsigned, __builtin_convertvector(f32x2{lo, hi}, bf16x2)); }
#define WAITVM(n) asm volatile("s_waitcnt vmcnt(" #n ")" ::: "memory")
__device__ __forceinline__ unsigned lo32(const void* p) { return (unsigned)(u64)p; }
__device__ __forceinline__ unsigned hi32(const void* p) { return (unsigned)((u64)p >> 32); }
__device__ __forceinline__ unsigned arrive(const LAS char* p, int lane) {
    unsigned r = 0;
    if (lane == 0) r = __hip_atomic_fetch_add((LAS unsigned*)p, 1u, __ATOMIC_RELAXED, __HIP_MEMORY_SCOPE_WORKGROUP);
    return __builtin_amdgcn_readfirstlane(r);
}
constexpr unsigned worows(int s, int jh) {
    unsigned c = 0;
    for (int g = 0; g < 4; ++g) { const int P = 4 * s + g, pr = P / 5, i = P % 5;
        c |= (unsigned)(i < 4 ? (2 * pr + (i >> 1)) * 20 + 8 * jh + 4 * (i & 1) : (2 * pr + jh) * 20 + 16) << (8 * g); }
    return c;
}

__global__ __launch_bounds__(512, 2) void fused_kernel(const char* __restrict__ x, const char* __restrict__ wq, const char* __restrict__ wk, const char* __restrict__ wv,
                                                       const char* __restrict__ wo, const char* __restrict__ w1, const char* __restrict__ w3, const char* __restrict__ w2,
                                                       const char* __restrict__ gw, char* __restrict__ out, char* ws) {
    __shared__ __attribute__((aligned(16))) char lds[LDSB];
    const LAS char* L = (const LAS char*)lds;
    asm volatile("" ::"v"(L) : "memory");
    u64 pc0; asm volatile("s_getpc_b64 %0" : "=s"(pc0));
    const int lane = threadIdx.x & 63, wid = __builtin_amdgcn_readfirstlane(threadIdx.x >> 6), w = wid & 3;
    const int b = blockIdx.x, xcd = b & 7, bi = b >> 3;
    const int tg = (xcd & 1) * 8 + (bi & 7), cg = (xcd >> 1) * 4 + (bi >> 3);
    const int quad = xcd >> 1; const bool proj = (bi >> 3) == 3;
    const int t64 = (xcd & 3) * 8 + (bi & 7), ng = (xcd >> 2) * 4 + (bi >> 3);
    const int g = lane >> 4, l15 = lane & 15, q4 = (lane & 15) >> 2, p4 = lane & 3, l31 = lane & 31, hh = lane >> 5;
    const auto rs = __builtin_amdgcn_make_buffer_rsrc(ws, 0, WS_USED, 0x00020000);

    if (wid < 4) {
        const int tp = w & 1, half = w >> 1;
        const char* pt = (const char*)pc0 + min(bi * 12 + w * 3 + min(lane, 2), CODE_LINES - 1) * 128;
        if (proj && lane >= 3 && lane < 30) {
            const int u = ((bi & 7) * 8 + xcd) * 4 + w;
            pt = lane < 28 ? wq + (u * 25 + lane - 3) * 128 : (lane == 28 ? wk : wv) + min(u, 199) * 128;
        }
        if (!proj) {
            f32x16 a0 = {}, a1 = {}, a2 = {}, a3 = {}, a4 = {}, a5 = {}, a6 = {}, a7 = {};
            const unsigned f = q4 + 4 * (g & 1), w0 = (8 * g + q4) * 256 + p4 * 8, xa = (tp * 64 + l15) * 128 + ((g ^ ((l15 >> 1) & 7)) << 4);
            u32x8 ad = {w0 + (((4 * half) ^ f) << 5), w0 + (((4 * half + 1) ^ f) << 5), w0 + (((4 * half + 2) ^ f) << 5), w0 + (((4 * half + 3) ^ f) << 5), xa, xa ^ 64, 0, 0};
            unsigned sl, d, cnt;
            const int fc = (bi >> 3) * 64 + (bi & 7) * 8 + xcd, id = min(fc * 27 + min(lane, 26), 5119);
            const char* pp = id < 1536 ? w1 + id * 128 : id < 3072 ? w3 + (id - 1536) * 128 : x + (id - 3072) * 1280;
            const unsigned ps = id < 3072 ? 196608 : 128;
            pp += (u64)ps * min(w + 4, 9);
            u32x3 pf3 = {lo32(pp), hi32(pp), ps * 4};
#define ACC8 "+{v[0:15]}"(a0), "+{v[16:31]}"(a1), "+{v[32:47]}"(a2), "+{v[48:63]}"(a3), "+{v[64:79]}"(a4), "+{v[80:95]}"(a5), "+{v[96:111]}"(a6), "+{v[112:127]}"(a7)
            asm volatile(GEMM1_ASM : ACC8, "+{v[224:231]}"(ad), "+{v[232:234]}"(pf3), [sl] "=&s"(sl), [d] "=&s"(d), [cnt] "=&s"(cnt)
                                                  : [p0] "s"((512 >> w) | (w + 4 + 4 <= 9 ? 32 >> w : 0)), "{v[238:239]}"(pt) : "memory", "scc", "vcc", GEMM1_CLOB);
            asm volatile(HEPI_ASM : ACC8 : [vo] "v"(w * 8192 + lane * 16), [c] "s"(0xbfb8aa3bbfb8aa3bull), [one] "s"(0x3f8000003f800000ull) : "memory", HEPI_CLOB);
        } else {
            const unsigned xa = (tp * 64 + l31) * 128 + ((hh ^ ((l31 >> 1) & 7)) << 4);
            const unsigned qo = WS_Q + ((tg * 32 + quad * 8) * 4 + tp * 2) * 1024, rec = WS_KV + (tg * 4 + tp * 2) * REC;
            u32x8 ad = {xa, xa ^ 32, xa ^ 64, xa ^ 96, (unsigned)((8 * hh + q4) * 320 + (g & 1) * 32 + p4 * 8 + half * 192), (unsigned)(quad < 2 ? (8 * hh + q4) * 40 + ((g & 1) ? 32 : p4 * 8) : hh * 16),
                        qo + lane * 16, qo + (lane ^ 32) * 16};
            const u32x4 ao = {(unsigned)(WS_Q2 + ((tg * 4 + quad) * 8 + tp * 2) * 1024 + lane * 16),
                              quad == 0 ? rec + lane * 16 : quad == 1 ? rec + 2 * REC + 1536 + lane * 16 : quad == 2 ? (unsigned)(WS_G + (tg * 128 + tp * 64 + lane) * 4) : (unsigned)(WS_SINK + b * 256 + lane * 4),
                              rec + 1024 + lane * 8, quad == 0 ? (unsigned)(WS_KMAX + (tg * 2 + tp) * 4) : l31 == 20 ? 0x3f803f80u : 0u};
            unsigned sl, d, cnt;
            asm volatile(ZERO96 GEMMD_ASM : "+{v[224:231]}"(ad), [sl] "=&s"(sl), [d] "=&s"(d), [cnt] "=&s"(cnt)
                         : [role] "s"(half ? min(quad, 2) : 3), "{v[238:239]}"(pt), "{v[240:243]}"(ao), [rs] "s"(rs), [qs] "s"(QSCALE) : "memory", "scc", "vcc", "s84", "s85", GEMMD_CLOB);
            const u64 tg0 = *(const LAS u64*)(L + CTL + 72);
            if (half) {
                WAITVM(8);
                if (quad < 3 && arrive(L + CTL + 32, lane) == 1 && lane == 0)
                    __hip_atomic_store((gu64*)(ws + WS_FKV) + (tg * 3 + quad) * 16 + lane, tg0, RLX_AGENT);
            }
            WAITVM(0);
            if (arrive(L + CTL + 44, lane) == 3 && lane == 0) __hip_atomic_store((gu64*)(ws + WS_FKV) + (48 + tg * 4 + quad) * 16 + lane, tg0, RLX_AGENT);
        }
        f32x16 O0, O1; float gate;
        const unsigned ob = WS_O + (tg * 8 + w * 2 + (l31 >> 4)) * 20480 + l15 * 16;
        unsigned oa = ob + (cg * 5 + hh) * 256, oy = ob + (cg * 5 + 4) * 256, fa = WS_FO + (tg * 16 + cg) * 128 + l15 * 8;
        asm volatile("" : "+v"(oa), "+v"(oy), "+v"(fa));
        {
            unsigned a16 = KVA + lane * 16, a8 = KVA + 1024 + lane * 8, d, cnt, sp;
            const u32x4 qa = {(unsigned)(CTL + 80 + w * 4), (unsigned)(QLDS + w * 3072 + lane * 16), (unsigned)(QLDS + w * 3072 + 2048 + l31 * 16 + (cg & 1) * 8), (unsigned)(CTL + 640 + w * 128 + l31 * 4)};
            const u32x2 ka = {(unsigned)(WS_KV + lane * 16), (unsigned)(WS_KV + 1024 + lane * 8)};
            __builtin_amdgcn_s_setprio(1);
            asm volatile(ATTN_ASM
                         : "=&{v[0:15]}"(O0), "=&{v[16:31]}"(O1), "+{v44}"(a16), "+{v45}"(a8), "=&{v32}"(gate), [d] "=&s"(d), [cnt] "=&s"(cnt), [sp] "=&s"(sp)
                         : "{v[176:179]}"(qa), "{v[180:181]}"(ka), [rs] "s"(rs), [ms] "s"(-MSAFE), [ga] "v"(CTL + 128 + (w * 32 + l31) * 4), [mw] "v"(CTL + 112 + w * 4)
                         : "memory", "scc", "vcc", "s86", "s87", "s88", "s89", ATTN_CLOB);
        }
        unsigned tmp;
        asm volatile(OEPI_ASM : "+{v[0:15]}"(O0), "+{v[16:31]}"(O1), [t] "=&s"(tmp) : "{v32}"(gate), [a0] "v"(oa), [y0] "v"(oy), [rs] "s"(rs), [ca] "v"(CTL + 36), [fa] "v"(fa)
                     : "memory", "scc", OEPI_CLOB);
    } else {
        u64 tagv;
        __builtin_amdgcn_s_setprio(0);
        unsigned rot = (proj ? 9 : 0);
        u32x8 sa, sb; u32x16 va = {}; const u32x4 sc = {(unsigned)w * 4096, (unsigned)w * 5120, (unsigned)w * 1024, (unsigned)w};
        const char* xb = x + (tg * 128 + w * 32) * 1280 + rot * 128;
        if (!proj) {
            const int co = quad * 768 + (bi >> 3) * 256 + w * 49152;
            const char *p1 = w1 + co, *p2 = w3 + co;
            sa = u32x8{lo32(xb), hi32(xb), lo32(p1), hi32(p1), lo32(p2), hi32(p2), 0, 0};
            asm volatile(XS1_ASM : "+{s[40:47]}"(sa), "=&{v[120:121]}"(tagv), "+{s76}"(rot) : "{v116}"(lane * 16), "{v119}"(CTL), "{s[56:59]}"(sc), "{s[60:61]}"(ws + WS_E)
                         : "memory", "scc", "vcc", "s62", "s63", "s77", "s78", "s79", XS1_CLOB);
        } else {
            const unsigned st = quad < 2 ? 2560 : 128;
            const char *p1 = wq + quad * 320 + rot * 81920, *p2 = (quad == 0 ? wk : quad == 1 ? wv : gw) + rot * st;
            sa = u32x8{lo32(xb), hi32(xb), lo32(p1), hi32(p1), lo32(p2), hi32(p2), st, 0};
            asm volatile(XS1D_ASM : "+{s[40:47]}"(sa), "=&{v[120:121]}"(tagv), "+{s76}"(rot)
                         : "{v116}"(lane * 16), "{v119}"(CTL), "{v109}"(quad < 2 ? min(w * 64 + lane, 159) * 16 : (lane & 7) * 16), "{s[56:59]}"(sc), "{s[60:61]}"(ws + WS_E)
                         : "memory", "scc", "vcc", "s62", "s63", "s77", "s78", "s79", XS1_CLOB);
        }
        __builtin_amdgcn_sched_barrier(0);
        const u32x4 vb = {(unsigned)lane * 16, (unsigned)(lane < 48 ? lane : 48 + tg * 4 + quad) * 128, 0, (unsigned)CTL};
        const u64 tag = (((u64)(unsigned)__builtin_amdgcn_readfirstlane((unsigned)(tagv >> 32)) << 32) | (unsigned)__builtin_amdgcn_readfirstlane((unsigned)tagv)) + 1;
        const unsigned vh = lane * 16;
        const int ngr = ng;
        auto wofs = [](int p) { return (unsigned)((p & 1) * RINGSZ + ((p >> 1) & 1) * WOHI); };
        unsigned ar[10];
#pragma unroll
        for (int i = 0; i < 10; ++i) { ar[i] = (((worows(i >> 1, i & 1) >> (8 * g)) & 255) + q4) * 160 + p4 * 8 + wofs(ngr); asm volatile("" : "+v"(ar[i])); }
#pragma unroll
        for (int i = 0; i < 5; ++i) { const int P = i * 64 + lane, row = P / 10; va[i] = row * 1280 + (P - row * 10) * 16; }
        const unsigned ra = (4 * g + q4) * 160 + p4 * 8;
        va[5] = w * 7168 + lane * 16; va[6] = w * 2048 + lane * 16; va[7] = l15 * 128; va[8] = ra; va[9] = ra + RING23; va[10] = CTL + w * 4; va[11] = w * 8192 + lane * 16;
        asm volatile("" : "+{v[100:115]}"(va));
        u32x4 sd;
        { const char *p2 = ws + WS_FKV, *p4_ = ws + WS_G + (t64 * 64 + w * 16) * 4, *p5 = ws + WS_KV + 4096;
          const char *p1 = ws + WS_Q + ((tg * 32 + cg * 2) * 4 + w) * 1024, *p3 = ws + WS_Q2 + (((tg * 4 + quad) * 2 + ((cg >> 1) & 1)) * 4 + w) * 1024;
          sb = u32x8{(unsigned)tag, (unsigned)(tag >> 32), lo32(p1), hi32(p1), lo32(p2), hi32(p2), lo32(p3), hi32(p3)}; sd = u32x4{lo32(p4_), hi32(p4_), lo32(p5), hi32(p5)}; }
        asm volatile(XS2_ASM : "+{s[64:67]}"(sd), "+{s[48:55]}"(sb) : "{v[116:119]}"(vb), "{s[56:59]}"(sc), "{s[68:69]}"(ws + WS_KMAX) : "memory", "scc", "vcc", "s61", "s62", "s63", "s77", "s78", XS2_CLOB);
        f32x16 f0; f32x4 f1;
        {
            const char* hd = ws + WS_H + ((tg * 2 + (w & 1)) * 48 + quad * 12 + (bi >> 3) * 4 + (w >> 1) * 2) * 4096;
            const u32x4 sf = {lo32(hd), hi32(hd), lo32(hd + 4096), hi32(hd + 4096)};
            const char *p0 = w2 + ng * 160 + w * 40960, *p1 = wo + ng * 160 + w * 40960, *p2 = ws + WS_H + t64 * (48 * 4096) + w * 1024, *p3 = ws + WS_FH + (t64 >> 1) * 2048, *p5 = ws + WS_FH + (tg * 16 + cg) * 128;
            auto sl = [&](int i) { return (unsigned)(i < 10 ? i * 5120 : RING23 + (i - 10) * 5120); };
            sa = u32x8{lo32(p0), hi32(p0), lo32(p1), hi32(p1), lo32(p2), hi32(p2), lo32(p3), hi32(p3)};
            sb = u32x8{(unsigned)tag, (unsigned)(tag >> 32), lo32(p5), hi32(p5), sl(w), sl(4 + w), sl(8 + w), sl(12 + w)};
            u32x4 se = {sl(16 + w), (unsigned)(KVA + KVSLOT + w * 7168 + 4096), (unsigned)(KVA + w * 2048 + 4096), (unsigned)w};
            const u32x4 sg = {(unsigned)(w < 2 ? (8 + w) * 5120 : WOHI + (w - 2) * 5120), (unsigned)(WOHI + (2 + w) * 5120), (unsigned)(WOHI + (6 + w) * 5120), 0};
            __builtin_amdgcn_s_setprio(2);
            asm volatile(XPH2_ASM : "=&{v[0:15]}"(f0), "=&{v[16:19]}"(f1), "+{s[40:47]}"(sa), "+{s[56:59]}"(se), "+{s[64:67]}"(sd)
                         : "{s[48:55]}"(sb), "{v[100:115]}"(va), "{v[116:119]}"(vb), "{s[68:71]}"(sf), "{s60}"(bi >> 3), "{s72}"(b * 4 + (w ^ 1)), "{s[74:75]}"(ws + WS_FH), "{s[76:77]}"(ws + WS_E), "{s[78:79]}"(ws + WS_G + tg * 512), "{s[80:83]}"(sg) : "memory", "scc", "vcc", "s61", "s62", "s63", XPH2_CLOB);
        }
#define AR_IO(c) c "{v200}"(ar[0]), c "{v201}"(ar[1]), c "{v202}"(ar[2]), c "{v203}"(ar[3]), c "{v204}"(ar[4]), c "{v205}"(ar[5]), c "{v206}"(ar[6]), c "{v207}"(ar[7]), c "{v208}"(ar[8]), c "{v209}"(ar[9])
        const char* ob = ws + WS_O + (t64 * 4 + w) * 20480 + 1024;
        asm volatile(TAIL_ASM : "+{v[0:15]}"(f0), "+{v[16:19]}"(f1), AR_IO("+")
                     : [oo] "v"(vh), [pa] "v"(l15 * 128), [pb] "s"(ws + WS_FO + (t64 >> 1) * 2048), [tag] "s"(tag), [wv] "s"(w), [ob0] "s"(ob + (ngr & 3) * 5120), [ob1] "s"(ob + ((ngr + 1) & 3) * 5120), [ob2] "s"(ob + ((ngr + 2) & 3) * 5120), [ob3] "s"(ob + ((ngr + 3) & 3) * 5120),
                       [d1] "s"(wofs(ngr + 1) - wofs(ngr)), [d2] "s"(wofs(ngr + 2) - wofs(ngr + 1)), [d3] "s"(wofs(ngr + 3) - wofs(ngr + 2))
                     : "memory", "scc", "vcc", "s83", TAIL_CLOB);
        const auto ro = __builtin_amdgcn_make_buffer_rsrc(out, 0, 2048 * 1280, 0x00020000);
        const unsigned op = (t64 * 64 + w * 16 + l15) * 1280 + ng * 160 + g * 8;
#pragma unroll
        for (int nt = 0; nt < 4; ++nt) __builtin_amdgcn_raw_buffer_store_b64(u32x2{cvtpk(f0[nt * 4], f0[nt * 4 + 1]), cvtpk(f0[nt * 4 + 2], f0[nt * 4 + 3])}, ro, op + nt * 32, 0, 0);
        __builtin_amdgcn_raw_buffer_store_b64(u32x2{cvtpk(f1[0], f1[1]), cvtpk(f1[2], f1[3])}, ro, op + 128, 0, 0);
    }
}

extern "C" void kernel_launch(void* const* d_in, const int* in_sizes, int n_in, void* d_out, int out_size, void* d_ws, size_t ws_size, hipStream_t stream) {
    fused_kernel<<<256, 512, 0, stream>>>((const char*)d_in[0], (const char*)d_in[1], (const char*)d_in[2], (const char*)d_in[3], (const char*)d_in[4],
                                          (const char*)d_in[6], (const char*)d_in[7], (const char*)d_in[8], (const char*)d_in[9], (char*)d_out, (char*)d_ws);
}
```
